```python
import torch

def _softmax(x, axis=-1):
    return torch.softmax(x, dim=axis)
def _gelu(x):
    return torch.nn.functional.gelu(x, approximate="tanh")
def _silu(x):
    return torch.nn.functional.silu(x)
def _sigmoid(x):
    return torch.sigmoid(x)
def _layernorm(x, axis=-1, eps=1e-5):
    m = x.mean(dim=axis, keepdim=True)
    v = x.var(dim=axis, keepdim=True, unbiased=False)
    return (x - m) / torch.sqrt(v + eps)
def _scatter_add(tgt, idx, src, axis=0):
    return tgt.clone().index_add_(axis, idx, src)
def _segment_sum(src, seg, n):
    out = torch.zeros((n,) + src.shape[1:], dtype=src.dtype)
    return out.index_add_(0, seg, src)
def _pad(x, pw):
    flat = tuple(v for pair in reversed(pw) for v in pair)
    return torch.nn.functional.pad(x, flat)

kernel_name = 'recurrent_scan__gqa_block_cached__dense_activation_relu__prompt'

def setup_inputs():

    _w = (lambda a: (lambda t: t.to(torch.bfloat16) if t.is_floating_point() else t)(torch.as_tensor(a)))
    def _A_setup():
        B, Sq, D = 1, 2048, 512
        Hq, Hkv, Sk, Dh = 32, 8, 2048, 128
        assert Hq % Hkv == 0, (Hq, Hkv)
        return {
            "x": _w((lambda *s: torch.randn(*s, dtype=torch.bfloat16) * 0.05)(B, Sq, D)),
            "k_cache": _w((lambda *s: torch.randn(*s, dtype=torch.bfloat16) * 1.6)(B, Hkv, Sk, Dh)),
            "v_cache": _w((lambda *s: torch.randn(*s, dtype=torch.bfloat16) * 0.2)(B, Hkv, Sk, Dh)),
            "wq": _w((lambda *s: torch.randn(*s, dtype=torch.bfloat16) * 1.6)(D, Hq * Dh) * (D ** -0.5)),
            "wo": _w((lambda *s: torch.randn(*s, dtype=torch.bfloat16) * 0.2)(Hq * Dh, D) * ((Hq * Dh) ** -0.5)),
        }
    _A_in = _A_setup()

    def _B_setup():
        B, S, D, Dff = 1, 2048, 512, 1792
        return {
            "x": _w((lambda *s: torch.randn(*s, dtype=torch.bfloat16) * 0.05)(B, S, D)),
            "w1": _w((lambda *s: torch.randn(*s, dtype=torch.bfloat16) * 0.1)(D, Dff)),
            "w2": _w((lambda *s: torch.randn(*s, dtype=torch.bfloat16) * 0.1)(Dff, D)),
        }
    _B_in = _B_setup()

    return {
        "A_x": _A_in["x"],
        "A_k_cache": _A_in["k_cache"],
        "A_v_cache": _A_in["v_cache"],
        "A_wq": _A_in["wq"],
        "A_wo": _A_in["wo"],
        "B_x": _B_in["x"],
        "B_w1": _B_in["w1"],
        "B_w2": _B_in["w2"],
    }

def reference(A_x, A_k_cache, A_v_cache, A_wq, A_wo, B_x, B_w1, B_w2):
    def _A_ref(x, k_cache, v_cache, wq, wo):
        B, Sq, D = x.shape
        Hq, Dh = 32, 128
        Hkv = k_cache.shape[1]
        rep = Hq // Hkv
        q = (lambda x, *d: x.permute(*d))((x @ wq).reshape(B, Sq, Hq, Dh), 0, 2, 1, 3)
        k = (lambda x, n, axis: torch.repeat_interleave(x, n, dim=axis))(k_cache, rep, axis=1)
        v = (lambda x, n, axis: torch.repeat_interleave(x, n, dim=axis))(v_cache, rep, axis=1)
        scores = torch.einsum("bhqd,bhkd->bhqk", q, k) * (Dh ** -0.5)
        p = _softmax(scores, axis=-1)
        o = torch.einsum("bhqk,bhkd->bhqd", p, v)
        return (lambda x, *d: x.permute(*d))(o, 0, 2, 1, 3).reshape(B, Sq, Hq * Dh) @ wo

    def _B_ref(x, w1, w2):
        h = torch.einsum("bsd,df->bsf", x, w1)
        zero = torch.zeros_like(h)
        h = torch.maximum(h, zero)
        return torch.einsum("bsf,fd->bsd", h, w2)

    x = A_x
    xn = _layernorm(x, axis=-1)
    s = _A_ref(xn, A_k_cache, A_v_cache, A_wq, A_wo)
    g = _silu(_B_ref(xn, B_w1, B_w2))
    return x + s * g
```

```cpp
#include <hip/hip_runtime.h>
#include <hip/hip_bf16.h>
typedef unsigned short u16;
__device__ __forceinline__ float b2f(u16 v){ return __uint_as_float(((unsigned)v)<<16); }
__device__ __forceinline__ u16 f2b(float f){ __hip_bfloat16 b = __float2bfloat16(f); return *(u16*)&b; }

__global__ void ln_k(const u16* x, u16* xn){
  int row = blockIdx.x; int t = threadIdx.x;
  float v[8]; float s=0;
  for(int i=0;i<8;i++){ v[i]=b2f(x[row*512+t*8+i]); s+=v[i]; }
  for(int o=32;o;o>>=1) s+=__shfl_xor(s,o);
  float m=s/512.f; float q=0;
  for(int i=0;i<8;i++){ v[i]-=m; q+=v[i]*v[i]; }
  for(int o=32;o;o>>=1) q+=__shfl_xor(q,o);
  float r=1.f/sqrtf(q/512.f+1e-5f);
  for(int i=0;i<8;i++) xn[row*512+t*8+i]=f2b(v[i]*r);
}
template<int MODE> __global__ void gemm_k(const u16* A, const u16* B, u16* C, float* Cf, int M,int N,int K){
  __shared__ float As[16][65], Bs[16][65];
  int tx=threadIdx.x&15, ty=threadIdx.x>>4;
  int m0=blockIdx.y*64, n0=blockIdx.x*64;
  float acc[4][4]={};
  for(int k0=0;k0<K;k0+=16){
    for(int i=threadIdx.x;i<1024;i+=256){ int r=i>>4,c=i&15; As[c][r]=b2f(A[(size_t)(m0+r)*K+k0+c]); }
    for(int i=threadIdx.x;i<1024;i+=256){ int r=i>>6,c=i&63; Bs[r][c]=b2f(B[(size_t)(k0+r)*N+n0+c]); }
    __syncthreads();
    for(int k=0;k<16;k++){
      float a[4],b[4];
      for(int i=0;i<4;i++){a[i]=As[k][ty*4+i]; b[i]=Bs[k][tx*4+i];}
      for(int i=0;i<4;i++)for(int j=0;j<4;j++)acc[i][j]+=a[i]*b[j];
    }
    __syncthreads();
  }
  for(int i=0;i<4;i++)for(int j=0;j<4;j++){
    float v=acc[i][j]; if(MODE==1) v=fmaxf(v,0.f);
    size_t o=(size_t)(m0+ty*4+i)*N+n0+tx*4+j;
    if(MODE==2) Cf[o]=v; else C[o]=f2b(v);
  }
}
__global__ void attn_k(const u16* Q, const u16* Kc, const u16* Vc, u16* O){
  __shared__ float qs[16][128]; __shared__ float ps[16][2048]; __shared__ float red[16];
  int h=blockIdx.y, q0=blockIdx.x*16, kv=h>>2; int t=threadIdx.x;
  for(int i=t;i<2048;i+=256){int r=i>>7,c=i&127; qs[r][c]=b2f(Q[(size_t)(q0+r)*4096+h*128+c]);}
  __syncthreads();
  for(int k=t;k<2048;k+=256){
    float a[16]={};
    const u16* kp=Kc+((size_t)kv*2048+k)*128;
    for(int d=0;d<128;d++){ float kvv=b2f(kp[d]); for(int r=0;r<16;r++) a[r]+=qs[r][d]*kvv; }
    for(int r=0;r<16;r++) ps[r][k]=a[r]*0.08838834764831845f;
  }
  __syncthreads();
  { int r=t>>4, l=t&15; float m=-1e30f; for(int k=l;k<2048;k+=16) m=fmaxf(m,ps[r][k]);
    for(int o=8;o;o>>=1) m=fmaxf(m,__shfl_xor(m,o));
    float s=0; for(int k=l;k<2048;k+=16){ float p=__expf(ps[r][k]-m); ps[r][k]=p; s+=p; }
    for(int o=8;o;o>>=1) s+=__shfl_xor(s,o);
    if(l==0) red[r]=1.f/s; }
  __syncthreads();
  { int d=t&127, rh=t>>7; float a[8]={};
    for(int k=0;k<2048;k++){ float v=b2f(Vc[((size_t)kv*2048+k)*128+d]); for(int r=0;r<8;r++) a[r]+=ps[rh*8+r][k]*v; }
    for(int r=0;r<8;r++) O[(size_t)(q0+rh*8+r)*4096+h*128+d]=f2b(a[r]*red[rh*8+r]); }
}
__global__ void fin_k(const u16* x, const float* s, const float* f, u16* out){
  int i=blockIdx.x*256+threadIdx.x; float g=f[i]; g=g/(1.f+__expf(-g)); out[i]=f2b(b2f(x[i])+s[i]*g);
}
extern "C" void kernel_launch(void* const* d_in, const int* in_sizes, int n_in, void* d_out, int out_size, void* d_ws, size_t ws_size, hipStream_t stream){
  const u16* x=(const u16*)d_in[0]; const u16* kc=(const u16*)d_in[1]; const u16* vc=(const u16*)d_in[2];
  const u16* wq=(const u16*)d_in[3]; const u16* wo=(const u16*)d_in[4]; const u16* w1=(const u16*)d_in[6]; const u16* w2=(const u16*)d_in[7];
  char* ws=(char*)d_ws;
  u16* xn=(u16*)ws; u16* q=(u16*)(ws+(4<<20)); u16* o=(u16*)(ws+(24<<20)); u16* h=(u16*)(ws+(44<<20));
  float* s=(float*)(ws+(56<<20)); float* f=(float*)(ws+(64<<20));
  ln_k<<<2048,64,0,stream>>>(x,xn);
  gemm_k<0><<<dim3(64,32),256,0,stream>>>(xn,wq,q,nullptr,2048,4096,512);
  gemm_k<1><<<dim3(28,32),256,0,stream>>>(xn,w1,h,nullptr,2048,1792,512);
  gemm_k<2><<<dim3(8,32),256,0,stream>>>(h,w2,nullptr,f,2048,512,1792);
  attn_k<<<dim3(128,32),256,0,stream>>>(q,kc,vc,o);
  gemm_k<2><<<dim3(8,32),256,0,stream>>>(o,wo,nullptr,s,2048,512,4096);
  fin_k<<<4096,256,0,stream>>>(x,s,f,(u16*)d_out);
}
```

```cpp
#include <hip/hip_runtime.h>
#include <hip/hip_bf16.h>
struct Params { const void *x,*k,*v,*wq,*wo,*w1,*w2; void *out,*ws; };
__global__ __launch_bounds__(256, 1) void fused_main(Params p) {
  __shared__ __attribute__((aligned(16))) char lds[163840];
  const unsigned wv = __builtin_amdgcn_readfirstlane(threadIdx.x >> 6);
  asm volatile(
  "s_load_dwordx16 s[76:91], %0, 0\n"
  "s_load_dwordx2 s[38:39], %0, 64\n"
  "v_mbcnt_lo_u32_b32 v216, -1, 0\n"
  "v_mbcnt_hi_u32_b32 v216, -1, v216\n"
  "s_getpc_b64 s[96:97]\n"
  "L_pc:\n"
  "s_lshr_b32 s57, %1, 3\n"
  "s_lshl_b32 s57, s57, 2\n"
  "s_add_u32 s57, s57, %2\n"
  "s_lshl_b32 s57, s57, 7\n"
  "v_lshl_add_u32 v227, v216, 14, s57\n"
  "v_min_u32 v227, L_end-L_pc, v227\n"
  "s_mov_b64 exec, 0xf\n"
  "global_load_dword a224, v227, s[96:97]\n"
  "s_mov_b64 exec, -1\n"
  "s_and_b32 s48, %1, 7\n"
  "s_lshr_b32 s49, %1, 3\n"
  "s_mov_b32 s67, s49\n"
  "s_mov_b32 s50, %2\n"
  "v_and_b32 v217, 15, v216\n"
  "v_lshrrev_b32 v218, 4, v216\n"
  "v_lshlrev_b32 v192, 4, v216\n"
  "v_lshrrev_b32 v224, 3, v216\n"
  "v_and_b32 v219, 7, v216\n"
  "v_and_b32 v220, 6, v224\n"
  "v_xor_b32 v225, v219, v220\n"
  "v_lshlrev_b32 v219, 10, v224\n"
  "v_lshl_add_u32 v222, v225, 4, v219\n"
  "v_add_u32 v223, 0x2000, v222\n"
  "s_lshl_b32 s60, s50, 10\n"
  "s_mov_b32 s94, 0x4000\n"
  "s_mov_b32 s95, 0x20000\n"
  "s_lshl_b32 s57, s67, 1\n"
  "v_lshrrev_b32 v219, 1, v216\n"
  "v_add_u32 v219, s57, v219\n"
  "v_and_b32 v220, 1, v216\n"
  "v_lshrrev_b32 v221, 2, v216\n"
  "v_mul_u32_u24 v221, 62, v221\n"
  "v_add_u32 v219, v219, v221\n"
  "v_lshlrev_b32 v221, 13, v219\n"
  "v_lshl_add_u32 v204, v220, 7, v221\n"
  "v_mul_u32_u24 v205, 3584, v219\n"
  "s_lshl_b32 s53, s48, 10\n"
  "s_lshl_b32 s59, s50, 8\n"
  "s_add_u32 s53, s53, s59\n"
  "s_and_b32 s56, s48, 1\n"
  "s_lshl_b32 s54, s56, 2\n"
  "s_add_u32 s8, s54, s50\n"
  "s_lshl_b32 s54, s8, 9\n"
  "s_and_b32 s58, s48, 6\n"
  "s_lshl_b32 s58, s58, 6\n"
  "s_add_u32 s54, s54, s58\n"
  "s_sub_u32 s18, 4194304, s53\n"
  "s_mov_b32 s19, 0x20000\n"
  "s_sub_u32 s22, 1835008, s54\n"
  "s_cmp_eq_u32 s8, 7\n"
  "s_cselect_b32 s22, 0, s22\n"
  "s_mov_b32 s23, 0x20000\n"
  "s_cmp_eq_u32 s56, 0\n"
  "s_cbranch_scc0 L_edl\n"
  "s_sleep 8\n"
  "s_cmp_lt_u32 s48, 4\n"
  "s_cbranch_scc0 L_edl\n"
  "s_sleep 22\n"
  "L_edl:\n"
  "s_lshl_b32 s57, s49, 16\n"
  "s_lshl_b32 s58, s50, 14\n"
  "s_add_u32 s57, s57, s58\n"
  "s_waitcnt lgkmcnt(0)\n"
  "s_add_u32 s92, s76, s57\n"
  "s_addc_u32 s93, s77, 0\n"
  "s_and_b32 s93, s93, 0xffff\n"
  "s_add_u32 m0, s60, 0\n"
  "s_nop 0\n"
  "buffer_load_dwordx4 v222, s[92:95], 0 offen offset:0 lds\n"
  "s_add_u32 s16, s82, s53\n"
  "s_addc_u32 s17, s83, 0\n"
  "s_and_b32 s17, s17, 0xffff\n"
  "s_add_u32 s20, s86, s54\n"
  "s_addc_u32 s21, s87, 0\n"
  "s_and_b32 s21, s21, 0xffff\n"
  "v_and_b32 v219, 4, v224\n"
  "v_add_u32 v221, v224, v219\n"
  "v_lshlrev_b32 v219, 13, v221\n"
  "v_lshl_add_u32 v240, v225, 4, v219\n"
  "v_add_u32 v241, 130048, v240\n"
  "v_add_u32 v242, 30720, v240\n"
  "v_add_u32 v243, 160768, v240\n"
  "v_mul_u32_u24 v220, 3584, v221\n"
  "v_lshl_add_u32 v244, v225, 4, v220\n"
  "v_bfrev_b32 v226, 2\n"
  "v_add_u32 v245, 56320, v244\n"
  "v_add_u32 v246, 12288, v244\n"
  "v_add_u32 v247, 68608, v244\n"
  "s_mul_i32 s51, s50, 24576\n"
  "s_add_u32 s51, s51, 65536\n"
  "s_mov_b32 s62, 0\n"
  "s_and_b32 s62, s62, 1\n"
  "s_lshl_b32 s62, s62, 7\n"
  "v_lshl_add_u32 v248, s50, 13, v192\n"
  "v_xor_b32 v215, 0x800, v192\n"
  "s_add_u32 m0, s60, 4096\n"
  "s_nop 0\n"
  "buffer_load_dwordx4 v223, s[92:95], 0 offen offset:0 lds\n"
  "s_mov_b64 exec, 0xf\n"
  "buffer_load_dword a224, v205, s[20:23], 0 offen\n"
  "buffer_load_dword a224, v204, s[16:19], 0 offen\n"
  "s_mov_b64 exec, -1\n"
  "v_add_u32 v208, s60, v192\n"
  "s_lshl_b32 s57, s48, 19\n"
  "s_add_u32 s24, s78, s57\n"
  "s_addc_u32 s25, s79, 0\n"
  "s_and_b32 s25, s25, 0xffff\n"
  "s_mov_b32 s26, 0x80000\n"
  "s_mov_b32 s27, 0x20000\n"
  "s_add_u32 s28, s80, s57\n"
  "s_addc_u32 s29, s81, 0\n"
  "s_and_b32 s29, s29, 0xffff\n"
  "s_mov_b32 s30, 0x80000\n"
  "s_mov_b32 s31, 0x20000\n"
  "s_add_u32 s57, s57, s59\n"
  "s_add_u32 s42, s84, s57\n"
  "s_addc_u32 s43, s85, 0\n"
  "s_and_b32 s43, s43, 0xffff\n"
  "s_lshl_b32 s58, s56, 20\n"
  "s_and_b32 s57, s48, 6\n"
  "s_lshl_b32 s57, s57, 15\n"
  "s_add_u32 s57, s57, s58\n"
  "s_add_u32 m0, s51, 0\n"
  "s_mov_b32 s53, 0\n"
  "buffer_load_dwordx4 v244, s[20:23], s53 offen offset:0 lds\n"
  "s_add_u32 s57, s57, s59\n"
  "s_add_u32 s44, s88, s57\n"
  "s_addc_u32 s45, s89, 0\n"
  "s_and_b32 s45, s45, 0xffff\n"
  "s_sub_u32 s46, 1835008, s57\n"
  "s_mov_b32 s47, 0x20000\n"
  "s_lshl_b32 s57, s49, 16\n"
  "s_and_b32 s58, s48, 1\n"
  "s_lshl_b32 s58, s58, 15\n"
  "s_add_u32 s57, s57, s58\n"
  "s_add_u32 s57, s57, s59\n"
  "s_lshr_b32 s58, s48, 1\n"
  "s_lshl_b32 s58, s58, 6\n"
  "s_add_u32 s57, s57, s58\n"
  "s_add_u32 m0, s60, 8064\n"
  "s_nop 0\n"
  "buffer_load_dwordx4 v222, s[92:95], 0 offen offset:128 lds\n"
  "s_add_u32 s36, s76, s57\n"
  "s_addc_u32 s37, s77, 0\n"
  "s_add_u32 s40, s90, s57\n"
  "s_addc_u32 s41, s91, 0\n"
  "s_lshl_b32 s57, s49, 19\n"
  "s_lshl_b32 s58, s50, 13\n"
  "s_add_u32 s57, s57, s58\n"
  "s_add_u32 s32, s38, s57\n"
  "s_addc_u32 s33, s39, 0\n"
  "s_and_b32 s33, s33, 0xffff\n"
  "s_mov_b32 s34, 0x7a000\n"
  "s_mov_b32 s35, 0x20000\n"
  "s_lshl_b32 s58, s48, 16\n"
  "s_add_u32 s57, s57, s58\n"
  "s_add_u32 m0, s51, 0\n"
  "s_nop 0\n"
  "buffer_load_dwordx4 v245, s[20:23], s53 offen offset:1024 lds\n"
  "s_add_u32 s72, s38, s57\n"
  "s_addc_u32 s73, s39, 0\n"
  "s_and_b32 s73, s73, 0xffff\n"
  "s_mov_b32 s74, 0xa000\n"
  "s_mov_b32 s75, 0x20000\n"
  "s_xor_b32 s12, s48, 7\n"
  "s_and_b32 s13, s12, 4\n"
  "s_lshl_b32 s13, s13, 5\n"
  "s_xor_b32 s55, s13, 128\n"
  "s_and_b32 s57, s12, 2\n"
  "s_lshl_b32 s57, s57, 5\n"
  "v_xor_b32 v198, s57, v222\n"
  "v_add_u32 v199, 7168, v198\n"
  "v_add_u32 v200, 14336, v198\n"
  "s_add_u32 m0, s60, 12160\n"
  "s_nop 0\n"
  "buffer_load_dwordx4 v223, s[92:95], 0 offen offset:128 lds\n"
  "v_add_u32 v201, 21504, v198\n"
  "s_and_b32 s57, s12, 1\n"
  "s_lshl_b32 s57, s57, 11\n"
  "v_add_u32 v202, s57, v192\n"
  "v_xor_b32 v203, 0x800, v202\n"
  "s_lshl_b32 s12, s12, 16\n"
  "s_lshl_b32 s57, s49, 2\n"
  "s_add_u32 s57, s57, s50\n"
  "s_lshl_b32 s57, s57, 11\n"
  "s_add_u32 s57, s57, 0x2000000\n"
  "s_add_u32 s68, s38, s57\n"
  "s_addc_u32 s69, s39, 0\n"
  "s_and_b32 s69, s69, 0xffff\n"
  "s_mov_b32 s70, 0x800\n"
  "s_mov_b32 s71, 0x20000\n"
  "s_add_u32 m0, s51, 0\n"
  "s_nop 0\n"
  "buffer_load_dwordx4 v246, s[20:23], s53 offen offset:2048 lds\n"
  "s_lshl_b32 s57, s48, 7\n"
  "s_add_u32 s57, s57, 96\n"
  "v_lshlrev_b32 v219, 2, v216\n"
  "v_add_u32 v220, s57, v219\n"
  "v_cmp_gt_u32 vcc, 8, v216\n"
  "v_xor_b32 v221, s48, v216\n"
  "v_lshlrev_b32 v221, 2, v221\n"
  "v_lshl_add_u32 v221, v216, 7, v221\n"
  "v_add_u32 v221, 0x80, v221\n"
  "v_cndmask_b32 v238, v220, v219, vcc\n"
  "v_cndmask_b32 v239, v226, v221, vcc\n"
  "v_cmp_gt_u32 vcc, 24, v216\n"
  "s_mov_b32 s64, 0xffff0000\n"
  "s_mov_b32 s9, 0x4000\n"
  "s_add_u32 m0, s60, 16128\n"
  "s_nop 0\n"
  "buffer_load_dwordx4 v222, s[92:95], 0 offen offset:256 lds\n"
  "v_cndmask_b32 v238, v226, v238, vcc\n"
  "s_mov_b32 s66, 0\n"
  "s_lshl_b32 s63, s50, 12\n"
  "s_and_b32 s61, s49, 7\n"
  "s_mul_i32 s61, s61, 65536\n"
  "s_mov_b32 s52, s63\n"
  "s_getpc_b64 s[10:11]\n"
  "L_pc2:\n"
  "s_add_u32 s10, s10, L_h0-L_pc2\n"
  "s_addc_u32 s11, s11, 0\n"
  "s_lshl_b32 s57, s49, 15\n"
  "s_and_b32 s57, s57, 0xc0000\n"
  "s_and_b32 s58, s49, 7\n"
  "s_lshl_b32 s58, s58, 13\n"
  "s_add_u32 m0, s51, 0\n"
  "s_nop 0\n"
  "buffer_load_dwordx4 v247, s[20:23], s53 offen offset:3072 lds\n"
  "s_add_u32 s57, s57, s58\n"
  "v_lshrrev_b32 v219, 1, v216\n"
  "v_lshl_add_u32 v219, v219, 10, s57\n"
  "v_and_b32 v220, 1, v216\n"
  "v_lshl_add_u32 v255, v220, 7, v219\n"
  "s_lshr_b32 s58, s67, 3\n"
  "s_lshl_b32 s58, s58, 2\n"
  "s_add_u32 s58, s58, s50\n"
  "s_lshl_b32 s58, s58, 10\n"
  "v_bfe_u32 v219, v216, 0, 3\n"
  "v_lshl_add_u32 v236, v219, 7, s58\n"
  "v_bfe_u32 v219, v216, 3, 1\n"
  "v_lshl_add_u32 v236, v219, 14, v236\n"
  "v_lshlrev_b32 v237, 15, v217\n"
  "s_add_u32 m0, s60, 20224\n"
  "s_nop 0\n"
  "buffer_load_dwordx4 v223, s[92:95], 0 offen offset:256 lds\n"
  "v_lshrrev_b32 v219, 2, v216\n"
  "v_and_b32 v220, 3, v216\n"
  "v_lshlrev_b32 v221, 7, v219\n"
  "v_lshl_add_u32 v221, v220, 3, v221\n"
  "v_add_u32 v221, s51, v221\n"
  "v_bfe_u32 v226, v219, 1, 2\n"
  "v_xor_b32 v194, 0, v226\n"
  "v_lshl_add_u32 v194, v194, 5, v221\n"
  "v_xor_b32 v195, 1, v226\n"
  "v_lshl_add_u32 v195, v195, 5, v221\n"
  "v_xor_b32 v196, 2, v226\n"
  "v_lshl_add_u32 v196, v196, 5, v221\n"
  "v_xor_b32 v197, 3, v226\n"
  "v_lshl_add_u32 v197, v197, 5, v221\n"
  "s_add_u32 m0, s51, 4096\n"
  "s_mov_b32 s54, 114688\n"
  "buffer_load_dwordx4 v244, s[20:23], s54 offen offset:0 lds\n"
  "v_and_b32 v219, 6, v217\n"
  "v_xor_b32 v219, v219, v218\n"
  "v_and_b32 v220, 7, v217\n"
  "v_lshlrev_b32 v220, 7, v220\n"
  "v_lshl_add_u32 v219, v219, 4, v220\n"
  "v_lshrrev_b32 v220, 3, v217\n"
  "v_lshl_add_u32 v219, v220, 12, v219\n"
  "v_add_u32 v209, s60, v219\n"
  "v_xor_b32 v210, 64, v209\n"
  "v_lshlrev_b32 v219, 2, v218\n"
  "v_sub_u32 v219, v217, v219\n"
  "v_cmp_eq_u32 s[84:85], 0, v219\n"
  "v_cmp_eq_u32 s[86:87], 1, v219\n"
  "v_cmp_eq_u32 s[88:89], 2, v219\n"
  "s_add_u32 m0, s60, 24192\n"
  "s_nop 0\n"
  "buffer_load_dwordx4 v222, s[92:95], 0 offen offset:384 lds\n"
  "v_cmp_eq_u32 s[90:91], 3, v219\n"
  "v_mov_b32 v219, 0x3f803f80\n"
  "v_accvgpr_write_b32 a52, v219\n"
  "v_accvgpr_write_b32 a53, v219\n"
  "v_accvgpr_write_b32 a54, v219\n"
  "v_accvgpr_write_b32 a55, v219\n"
  "s_waitcnt vmcnt(12)\n"
  "ds_read_b128 v[128:131], v209 offset:0\n"
  "ds_read_b128 v[132:135], v210 offset:0\n"
  "s_add_u32 m0, s51, 4096\n"
  "s_nop 0\n"
  "buffer_load_dwordx4 v245, s[20:23], s54 offen offset:1024 lds\n"
  "s_add_u32 m0, s60, 28288\n"
  "s_nop 0\n"
  "buffer_load_dwordx4 v223, s[92:95], 0 offen offset:384 lds\n"
  "s_mov_b64 exec, 0\n"
  "s_cmp_eq_u32 s50, 0\n"
  "s_cbranch_scc1 L_dry0_0\n"
  "s_cmp_eq_u32 s50, 1\n"
  "s_cbranch_scc1 L_dry0_1\n"
  "s_cmp_eq_u32 s50, 2\n"
  "s_cbranch_scc1 L_dry0_2\n"
  "s_branch L_dry0_3\n"
  "L_dryr0:\n"
  "s_mov_b64 exec, -1\n"
  "s_add_u32 m0, s51, 4096\n"
  "s_nop 0\n"
  "buffer_load_dwordx4 v246, s[20:23], s54 offen offset:2048 lds\n"
  "s_mov_b64 exec, 0\n"
  "s_cmp_eq_u32 s50, 0\n"
  "s_cbranch_scc1 L_dry1_0\n"
  "s_cmp_eq_u32 s50, 1\n"
  "s_cbranch_scc1 L_dry1_1\n"
  "s_cmp_eq_u32 s50, 2\n"
  "s_cbranch_scc1 L_dry1_2\n"
  "s_branch L_dry1_3\n"
  "L_dryr1:\n"
  "s_mov_b64 exec, -1\n"
  "s_waitcnt vmcnt(9)\n"
  "ds_read_b128 v[136:139], v209 offset:8192\n"
  "ds_read_b128 v[140:143], v210 offset:8192\n"
  "s_waitcnt lgkmcnt(3)\n"
  "v_mfma_f32_16x16x32_bf16 v[228:231], a[52:55], v[128:131], 0\n"
  "v_lshlrev_b32 v0, 16, v128\n"
  "v_and_b32 v1, s64, v128\n"
  "v_mfma_f32_16x16x32_bf16 v[232:235], v[128:131], v[128:131], 0\n"
  "v_lshlrev_b32 v2, 16, v129\n"
  "v_and_b32 v3, s64, v129\n"
  "v_lshlrev_b32 v4, 16, v130\n"
  "v_and_b32 v5, s64, v130\n"
  "v_lshlrev_b32 v6, 16, v131\n"
  "v_and_b32 v7, s64, v131\n"
  "s_waitcnt lgkmcnt(2)\n"
  "v_mfma_f32_16x16x32_bf16 v[228:231], a[52:55], v[132:135], v[228:231]\n"
  "v_lshlrev_b32 v8, 16, v132\n"
  "v_and_b32 v9, s64, v132\n"
  "v_mfma_f32_16x16x32_bf16 v[232:235], v[132:135], v[132:135], v[232:235]\n"
  "v_lshlrev_b32 v10, 16, v133\n"
  "v_and_b32 v11, s64, v133\n"
  "v_lshlrev_b32 v12, 16, v134\n"
  "v_and_b32 v13, s64, v134\n"
  "v_lshlrev_b32 v14, 16, v135\n"
  "v_and_b32 v15, s64, v135\n"
  "s_add_u32 m0, s60, 32256\n"
  "s_nop 0\n"
  "buffer_load_dwordx4 v222, s[92:95], 0 offen offset:512 lds\n"
  "s_mov_b64 exec, 0\n"
  "s_cmp_eq_u32 s50, 0\n"
  "s_cbranch_scc1 L_dry2_0\n"
  "s_cmp_eq_u32 s50, 1\n"
  "s_cbranch_scc1 L_dry2_1\n"
  "s_cmp_eq_u32 s50, 2\n"
  "s_cbranch_scc1 L_dry2_2\n"
  "s_branch L_dry2_3\n"
  "L_dryr2:\n"
  "s_mov_b64 exec, -1\n"
  "s_add_u32 m0, s51, 4096\n"
  "s_nop 0\n"
  "buffer_load_dwordx4 v247, s[20:23], s54 offen offset:3072 lds\n"
  "s_mov_b64 exec, 0\n"
  "s_cmp_eq_u32 s50, 0\n"
  "s_cbranch_scc1 L_dry3_0\n"
  "s_cmp_eq_u32 s50, 1\n"
  "s_cbranch_scc1 L_dry3_1\n"
  "s_cmp_eq_u32 s50, 2\n"
  "s_cbranch_scc1 L_dry3_2\n"
  "s_branch L_dry3_3\n"
  "L_dryr3:\n"
  "s_mov_b64 exec, -1\n"
  "s_add_u32 m0, s60, 36352\n"
  "s_nop 0\n"
  "buffer_load_dwordx4 v223, s[92:95], 0 offen offset:512 lds\n"
  "s_add_u32 m0, s51, 8192\n"
  "s_xor_b32 s53, s62, 0\n"
  "buffer_load_dwordx4 v240, s[16:19], s53 offen offset:0 lds\n"
  "s_waitcnt vmcnt(9)\n"
  "ds_read_b128 v[144:147], v209 offset:16384\n"
  "ds_read_b128 v[148:151], v210 offset:16384\n"
  "s_waitcnt lgkmcnt(3)\n"
  "v_mfma_f32_16x16x32_bf16 v[228:231], a[52:55], v[136:139], v[228:231]\n"
  "v_lshlrev_b32 v16, 16, v136\n"
  "v_and_b32 v17, s64, v136\n"
  "v_mfma_f32_16x16x32_bf16 v[232:235], v[136:139], v[136:139], v[232:235]\n"
  "v_lshlrev_b32 v18, 16, v137\n"
  "v_and_b32 v19, s64, v137\n"
  "v_lshlrev_b32 v20, 16, v138\n"
  "v_and_b32 v21, s64, v138\n"
  "v_lshlrev_b32 v22, 16, v139\n"
  "v_and_b32 v23, s64, v139\n"
  "s_waitcnt lgkmcnt(2)\n"
  "v_mfma_f32_16x16x32_bf16 v[228:231], a[52:55], v[140:143], v[228:231]\n"
  "v_lshlrev_b32 v24, 16, v140\n"
  "v_and_b32 v25, s64, v140\n"
  "v_mfma_f32_16x16x32_bf16 v[232:235], v[140:143], v[140:143], v[232:235]\n"
  "v_lshlrev_b32 v26, 16, v141\n"
  "v_and_b32 v27, s64, v141\n"
  "v_lshlrev_b32 v28, 16, v142\n"
  "v_and_b32 v29, s64, v142\n"
  "v_lshlrev_b32 v30, 16, v143\n"
  "v_and_b32 v31, s64, v143\n"
  "s_add_u32 m0, s60, 40320\n"
  "s_nop 0\n"
  "buffer_load_dwordx4 v222, s[92:95], 0 offen offset:640 lds\n"
  "s_add_u32 m0, s51, 8192\n"
  "s_nop 0\n"
  "buffer_load_dwordx4 v241, s[16:19], s53 offen offset:1024 lds\n"
  "s_add_u32 m0, s60, 44416\n"
  "s_nop 0\n"
  "buffer_load_dwordx4 v223, s[92:95], 0 offen offset:640 lds\n"
  "s_add_u32 m0, s51, 8192\n"
  "s_nop 0\n"
  "buffer_load_dwordx4 v242, s[16:19], s53 offen offset:2048 lds\n"
  "s_waitcnt vmcnt(9)\n"
  "ds_read_b128 v[152:155], v209 offset:24576\n"
  "ds_read_b128 v[156:159], v210 offset:24576\n"
  "s_waitcnt lgkmcnt(3)\n"
  "v_mfma_f32_16x16x32_bf16 v[228:231], a[52:55], v[144:147], v[228:231]\n"
  "v_lshlrev_b32 v32, 16, v144\n"
  "v_and_b32 v33, s64, v144\n"
  "v_mfma_f32_16x16x32_bf16 v[232:235], v[144:147], v[144:147], v[232:235]\n"
  "v_lshlrev_b32 v34, 16, v145\n"
  "v_and_b32 v35, s64, v145\n"
  "v_lshlrev_b32 v36, 16, v146\n"
  "v_and_b32 v37, s64, v146\n"
  "v_lshlrev_b32 v38, 16, v147\n"
  "v_and_b32 v39, s64, v147\n"
  "s_waitcnt lgkmcnt(2)\n"
  "v_mfma_f32_16x16x32_bf16 v[228:231], a[52:55], v[148:151], v[228:231]\n"
  "v_lshlrev_b32 v40, 16, v148\n"
  "v_and_b32 v41, s64, v148\n"
  "v_mfma_f32_16x16x32_bf16 v[232:235], v[148:151], v[148:151], v[232:235]\n"
  "v_lshlrev_b32 v42, 16, v149\n"
  "v_and_b32 v43, s64, v149\n"
  "v_lshlrev_b32 v44, 16, v150\n"
  "v_and_b32 v45, s64, v150\n"
  "v_lshlrev_b32 v46, 16, v151\n"
  "v_and_b32 v47, s64, v151\n"
  "s_add_u32 m0, s60, 48384\n"
  "s_nop 0\n"
  "buffer_load_dwordx4 v222, s[92:95], 0 offen offset:768 lds\n"
  "s_add_u32 m0, s51, 8192\n"
  "s_nop 0\n"
  "buffer_load_dwordx4 v243, s[16:19], s53 offen offset:3072 lds\n"
  "s_add_u32 m0, s60, 52480\n"
  "s_nop 0\n"
  "buffer_load_dwordx4 v223, s[92:95], 0 offen offset:768 lds\n"
  "s_add_u32 m0, s51, 12288\n"
  "s_xor_b32 s54, s62, 262144\n"
  "buffer_load_dwordx4 v240, s[16:19], s54 offen offset:0 lds\n"
  "s_waitcnt vmcnt(9)\n"
  "ds_read_b128 v[160:163], v209 offset:32768\n"
  "ds_read_b128 v[164:167], v210 offset:32768\n"
  "s_waitcnt lgkmcnt(3)\n"
  "v_mfma_f32_16x16x32_bf16 v[228:231], a[52:55], v[152:155], v[228:231]\n"
  "v_lshlrev_b32 v48, 16, v152\n"
  "v_and_b32 v49, s64, v152\n"
  "v_mfma_f32_16x16x32_bf16 v[232:235], v[152:155], v[152:155], v[232:235]\n"
  "v_lshlrev_b32 v50, 16, v153\n"
  "v_and_b32 v51, s64, v153\n"
  "v_lshlrev_b32 v52, 16, v154\n"
  "v_and_b32 v53, s64, v154\n"
  "v_lshlrev_b32 v54, 16, v155\n"
  "v_and_b32 v55, s64, v155\n"
  "s_waitcnt lgkmcnt(2)\n"
  "v_mfma_f32_16x16x32_bf16 v[228:231], a[52:55], v[156:159], v[228:231]\n"
  "v_lshlrev_b32 v56, 16, v156\n"
  "v_and_b32 v57, s64, v156\n"
  "v_mfma_f32_16x16x32_bf16 v[232:235], v[156:159], v[156:159], v[232:235]\n"
  "v_lshlrev_b32 v58, 16, v157\n"
  "v_and_b32 v59, s64, v157\n"
  "v_lshlrev_b32 v60, 16, v158\n"
  "v_and_b32 v61, s64, v158\n"
  "v_lshlrev_b32 v62, 16, v159\n"
  "v_and_b32 v63, s64, v159\n"
  "ds_read_b64_tr_b16 a[64:65], v194 offset:0\n"
  "ds_read_b64_tr_b16 a[66:67], v194 offset:2048\n"
  "ds_read_b64_tr_b16 a[72:73], v195 offset:0\n"
  "ds_read_b64_tr_b16 a[74:75], v195 offset:2048\n"
  "ds_read_b64_tr_b16 a[80:81], v196 offset:0\n"
  "ds_read_b64_tr_b16 a[82:83], v196 offset:2048\n"
  "ds_read_b64_tr_b16 a[88:89], v197 offset:0\n"
  "ds_read_b64_tr_b16 a[90:91], v197 offset:2048\n"
  "ds_read_b64_tr_b16 a[68:69], v194 offset:4096\n"
  "ds_read_b64_tr_b16 a[70:71], v194 offset:6144\n"
  "ds_read_b64_tr_b16 a[76:77], v195 offset:4096\n"
  "ds_read_b64_tr_b16 a[78:79], v195 offset:6144\n"
  "ds_read_b64_tr_b16 a[84:85], v196 offset:4096\n"
  "ds_read_b64_tr_b16 a[86:87], v196 offset:6144\n"
  "ds_read_b64_tr_b16 a[92:93], v197 offset:4096\n"
  "ds_read_b64_tr_b16 a[94:95], v197 offset:6144\n"
  "s_add_u32 m0, s60, 56448\n"
  "s_nop 0\n"
  "buffer_load_dwordx4 v222, s[92:95], 0 offen offset:896 lds\n"
  "s_add_u32 m0, s51, 12288\n"
  "s_nop 0\n"
  "buffer_load_dwordx4 v241, s[16:19], s54 offen offset:1024 lds\n"
  "s_add_u32 m0, s60, 60544\n"
  "s_nop 0\n"
  "buffer_load_dwordx4 v223, s[92:95], 0 offen offset:896 lds\n"
  "s_add_u32 m0, s51, 12288\n"
  "s_nop 0\n"
  "buffer_load_dwordx4 v242, s[16:19], s54 offen offset:2048 lds\n"
  "s_waitcnt vmcnt(9)\n"
  "ds_read_b128 v[168:171], v209 offset:40960\n"
  "ds_read_b128 v[172:175], v210 offset:40960\n"
  "s_waitcnt lgkmcnt(15)\n"
  "v_mfma_f32_16x16x32_bf16 v[228:231], a[52:55], v[160:163], v[228:231]\n"
  "v_lshlrev_b32 v64, 16, v160\n"
  "v_and_b32 v65, s64, v160\n"
  "v_mfma_f32_16x16x32_bf16 v[232:235], v[160:163], v[160:163], v[232:235]\n"
  "v_lshlrev_b32 v66, 16, v161\n"
  "v_and_b32 v67, s64, v161\n"
  "v_lshlrev_b32 v68, 16, v162\n"
  "v_and_b32 v69, s64, v162\n"
  "v_lshlrev_b32 v70, 16, v163\n"
  "v_and_b32 v71, s64, v163\n"
  "v_mfma_f32_16x16x32_bf16 v[228:231], a[52:55], v[164:167], v[228:231]\n"
  "v_lshlrev_b32 v72, 16, v164\n"
  "v_and_b32 v73, s64, v164\n"
  "v_mfma_f32_16x16x32_bf16 v[232:235], v[164:167], v[164:167], v[232:235]\n"
  "v_lshlrev_b32 v74, 16, v165\n"
  "v_and_b32 v75, s64, v165\n"
  "v_lshlrev_b32 v76, 16, v166\n"
  "v_and_b32 v77, s64, v166\n"
  "v_lshlrev_b32 v78, 16, v167\n"
  "v_and_b32 v79, s64, v167\n"
  "buffer_load_dwordx4 v243, s[16:19], s54 offen offset:3072 lds\n"
  "s_add_u32 m0, s51, 16384\n"
  "s_xor_b32 s53, s62, 128\n"
  "buffer_load_dwordx4 v240, s[16:19], s53 offen offset:0 lds\n"
  "buffer_load_dwordx4 v241, s[16:19], s53 offen offset:1024 lds\n"
  "buffer_load_dwordx4 v242, s[16:19], s53 offen offset:2048 lds\n"
  "s_waitcnt vmcnt(9)\n"
  "ds_read_b128 v[176:179], v209 offset:49152\n"
  "ds_read_b128 v[180:183], v210 offset:49152\n"
  "s_waitcnt lgkmcnt(3)\n"
  "v_mfma_f32_16x16x32_bf16 v[228:231], a[52:55], v[168:171], v[228:231]\n"
  "v_lshlrev_b32 v80, 16, v168\n"
  "v_and_b32 v81, s64, v168\n"
  "v_mfma_f32_16x16x32_bf16 v[232:235], v[168:171], v[168:171], v[232:235]\n"
  "v_lshlrev_b32 v82, 16, v169\n"
  "v_and_b32 v83, s64, v169\n"
  "v_lshlrev_b32 v84, 16, v170\n"
  "v_and_b32 v85, s64, v170\n"
  "v_lshlrev_b32 v86, 16, v171\n"
  "v_and_b32 v87, s64, v171\n"
  "s_waitcnt lgkmcnt(2)\n"
  "v_mfma_f32_16x16x32_bf16 v[228:231], a[52:55], v[172:175], v[228:231]\n"
  "v_lshlrev_b32 v88, 16, v172\n"
  "v_and_b32 v89, s64, v172\n"
  "v_mfma_f32_16x16x32_bf16 v[232:235], v[172:175], v[172:175], v[232:235]\n"
  "v_lshlrev_b32 v90, 16, v173\n"
  "v_and_b32 v91, s64, v173\n"
  "v_lshlrev_b32 v92, 16, v174\n"
  "v_and_b32 v93, s64, v174\n"
  "v_lshlrev_b32 v94, 16, v175\n"
  "v_and_b32 v95, s64, v175\n"
  "buffer_load_dwordx4 v243, s[16:19], s53 offen offset:3072 lds\n"
  "s_add_u32 m0, s51, 20480\n"
  "s_xor_b32 s54, s62, 262272\n"
  "buffer_load_dwordx4 v240, s[16:19], s54 offen offset:0 lds\n"
  "buffer_load_dwordx4 v241, s[16:19], s54 offen offset:1024 lds\n"
  "buffer_load_dwordx4 v242, s[16:19], s54 offen offset:2048 lds\n"
  "s_waitcnt vmcnt(9)\n"
  "ds_read_b128 v[184:187], v209 offset:57344\n"
  "ds_read_b128 v[188:191], v210 offset:57344\n"
  "s_waitcnt lgkmcnt(3)\n"
  "v_mfma_f32_16x16x32_bf16 v[228:231], a[52:55], v[176:179], v[228:231]\n"
  "v_lshlrev_b32 v96, 16, v176\n"
  "v_and_b32 v97, s64, v176\n"
  "v_mfma_f32_16x16x32_bf16 v[232:235], v[176:179], v[176:179], v[232:235]\n"
  "v_lshlrev_b32 v98, 16, v177\n"
  "v_and_b32 v99, s64, v177\n"
  "v_lshlrev_b32 v100, 16, v178\n"
  "v_and_b32 v101, s64, v178\n"
  "v_lshlrev_b32 v102, 16, v179\n"
  "v_and_b32 v103, s64, v179\n"
  "s_waitcnt lgkmcnt(2)\n"
  "v_mfma_f32_16x16x32_bf16 v[228:231], a[52:55], v[180:183], v[228:231]\n"
  "v_lshlrev_b32 v104, 16, v180\n"
  "v_and_b32 v105, s64, v180\n"
  "v_mfma_f32_16x16x32_bf16 v[232:235], v[180:183], v[180:183], v[232:235]\n"
  "v_lshlrev_b32 v106, 16, v181\n"
  "v_and_b32 v107, s64, v181\n"
  "v_lshlrev_b32 v108, 16, v182\n"
  "v_and_b32 v109, s64, v182\n"
  "v_lshlrev_b32 v110, 16, v183\n"
  "v_and_b32 v111, s64, v183\n"
  "buffer_load_dwordx4 v243, s[16:19], s54 offen offset:3072 lds\n"
  "s_bfm_b64 exec, 32, 0\n"
  "buffer_load_dword a224, v205, s[20:23], 0 offen\n"
  "buffer_load_dword a224, v204, s[16:19], 0 offen\n"
  "s_mov_b64 exec, -1\n"
  "buffer_load_dword v193, v238, s[68:71], 0 offen sc1\n"
  "s_waitcnt lgkmcnt(1)\n"
  "v_mfma_f32_16x16x32_bf16 v[228:231], a[52:55], v[184:187], v[228:231]\n"
  "v_lshlrev_b32 v112, 16, v184\n"
  "v_and_b32 v113, s64, v184\n"
  "v_mfma_f32_16x16x32_bf16 v[232:235], v[184:187], v[184:187], v[232:235]\n"
  "v_lshlrev_b32 v114, 16, v185\n"
  "v_and_b32 v115, s64, v185\n"
  "v_lshlrev_b32 v116, 16, v186\n"
  "v_and_b32 v117, s64, v186\n"
  "v_lshlrev_b32 v118, 16, v187\n"
  "v_and_b32 v119, s64, v187\n"
  "s_waitcnt lgkmcnt(0)\n"
  "v_mfma_f32_16x16x32_bf16 v[228:231], a[52:55], v[188:191], v[228:231]\n"
  "v_lshlrev_b32 v120, 16, v188\n"
  "v_and_b32 v121, s64, v188\n"
  "v_mfma_f32_16x16x32_bf16 v[232:235], v[188:191], v[188:191], v[232:235]\n"
  "v_lshlrev_b32 v122, 16, v189\n"
  "v_and_b32 v123, s64, v189\n"
  "v_lshlrev_b32 v124, 16, v190\n"
  "v_and_b32 v125, s64, v190\n"
  "v_lshlrev_b32 v126, 16, v191\n"
  "v_and_b32 v127, s64, v191\n"
  "L_warm0:\n"
  "v_mul_f32 v207, 0x3b000000, v228\n"
  "s_nop 0\n"
  "v_cndmask_b32 v206, 0, v232, s[84:85]\n"
  "v_cndmask_b32 v206, v206, v233, s[86:87]\n"
  "v_cndmask_b32 v206, v206, v234, s[88:89]\n"
  "v_cndmask_b32 v206, v206, v235, s[90:91]\n"
  "v_mov_b32 v219, v206\n"
  "s_nop 1\n"
  "v_permlane16_swap_b32 v206, v219\n"
  "v_add_f32 v206, v206, v219\n"
  "v_mov_b32 v219, v206\n"
  "s_nop 1\n"
  "v_permlane32_swap_b32 v206, v219\n"
  "v_add_f32 v206, v206, v219\n"
  "v_mul_f32 v206, 0x3b000000, v206\n"
  "v_fma_f32 v206, -v207, v207, v206\n"
  "v_max_f32 v206, 0, v206\n"
  "v_cvt_pk_bf16_f32 v206, v206, v206\n"
  "v_and_b32 v206, s64, v206\n"
  "v_add_f32 v206, 0x3727c5ac, v206\n"
  "v_cvt_pk_bf16_f32 v206, v206, v206\n"
  "v_and_b32 v206, s64, v206\n"
  "v_sqrt_f32 v206, v206\n"
  "v_cvt_pk_bf16_f32 v207, v207, v207\n"
  "v_and_b32 v207, s64, v207\n"
  "v_cvt_pk_bf16_f32 v206, v206, v206\n"
  "v_and_b32 v206, s64, v206\n"
  "v_rcp_f32 v206, v206\n"
  "s_nop 0\n"
  "v_mul_f32 v206, 0x3e0293ee, v206\n"
  "v_mul_f32 v207, v207, v206\n"
  "v_pk_fma_f32 v[0:1], v[0:1], v[206:207], v[206:207] op_sel:[0,0,1] op_sel_hi:[1,0,1] neg_lo:[0,0,1] neg_hi:[0,0,1]\n"
  "v_pk_fma_f32 v[2:3], v[2:3], v[206:207], v[206:207] op_sel:[0,0,1] op_sel_hi:[1,0,1] neg_lo:[0,0,1] neg_hi:[0,0,1]\n"
  "v_pk_fma_f32 v[4:5], v[4:5], v[206:207], v[206:207] op_sel:[0,0,1] op_sel_hi:[1,0,1] neg_lo:[0,0,1] neg_hi:[0,0,1]\n"
  "v_pk_fma_f32 v[6:7], v[6:7], v[206:207], v[206:207] op_sel:[0,0,1] op_sel_hi:[1,0,1] neg_lo:[0,0,1] neg_hi:[0,0,1]\n"
  "v_cvt_pk_bf16_f32 v128, v0, v1\n"
  "v_cvt_pk_bf16_f32 v129, v2, v3\n"
  "v_cvt_pk_bf16_f32 v130, v4, v5\n"
  "v_cvt_pk_bf16_f32 v131, v6, v7\n"
  "ds_write_b128 v208, v[128:131] offset:0\n"
  "v_pk_fma_f32 v[8:9], v[8:9], v[206:207], v[206:207] op_sel:[0,0,1] op_sel_hi:[1,0,1] neg_lo:[0,0,1] neg_hi:[0,0,1]\n"
  "v_pk_fma_f32 v[10:11], v[10:11], v[206:207], v[206:207] op_sel:[0,0,1] op_sel_hi:[1,0,1] neg_lo:[0,0,1] neg_hi:[0,0,1]\n"
  "v_pk_fma_f32 v[12:13], v[12:13], v[206:207], v[206:207] op_sel:[0,0,1] op_sel_hi:[1,0,1] neg_lo:[0,0,1] neg_hi:[0,0,1]\n"
  "v_pk_fma_f32 v[14:15], v[14:15], v[206:207], v[206:207] op_sel:[0,0,1] op_sel_hi:[1,0,1] neg_lo:[0,0,1] neg_hi:[0,0,1]\n"
  "v_cvt_pk_bf16_f32 v132, v8, v9\n"
  "v_cvt_pk_bf16_f32 v133, v10, v11\n"
  "v_cvt_pk_bf16_f32 v134, v12, v13\n"
  "v_cvt_pk_bf16_f32 v135, v14, v15\n"
  "ds_write_b128 v208, v[132:135] offset:4096\n"
  "v_pk_fma_f32 v[16:17], v[16:17], v[206:207], v[206:207] op_sel:[0,0,1] op_sel_hi:[1,0,1] neg_lo:[0,0,1] neg_hi:[0,0,1]\n"
  "v_pk_fma_f32 v[18:19], v[18:19], v[206:207], v[206:207] op_sel:[0,0,1] op_sel_hi:[1,0,1] neg_lo:[0,0,1] neg_hi:[0,0,1]\n"
  "v_pk_fma_f32 v[20:21], v[20:21], v[206:207], v[206:207] op_sel:[0,0,1] op_sel_hi:[1,0,1] neg_lo:[0,0,1] neg_hi:[0,0,1]\n"
  "v_pk_fma_f32 v[22:23], v[22:23], v[206:207], v[206:207] op_sel:[0,0,1] op_sel_hi:[1,0,1] neg_lo:[0,0,1] neg_hi:[0,0,1]\n"
  "v_cvt_pk_bf16_f32 v136, v16, v17\n"
  "v_cvt_pk_bf16_f32 v137, v18, v19\n"
  "v_cvt_pk_bf16_f32 v138, v20, v21\n"
  "v_cvt_pk_bf16_f32 v139, v22, v23\n"
  "ds_write_b128 v208, v[136:139] offset:8192\n"
  "v_pk_fma_f32 v[24:25], v[24:25], v[206:207], v[206:207] op_sel:[0,0,1] op_sel_hi:[1,0,1] neg_lo:[0,0,1] neg_hi:[0,0,1]\n"
  "v_pk_fma_f32 v[26:27], v[26:27], v[206:207], v[206:207] op_sel:[0,0,1] op_sel_hi:[1,0,1] neg_lo:[0,0,1] neg_hi:[0,0,1]\n"
  "v_pk_fma_f32 v[28:29], v[28:29], v[206:207], v[206:207] op_sel:[0,0,1] op_sel_hi:[1,0,1] neg_lo:[0,0,1] neg_hi:[0,0,1]\n"
  "v_pk_fma_f32 v[30:31], v[30:31], v[206:207], v[206:207] op_sel:[0,0,1] op_sel_hi:[1,0,1] neg_lo:[0,0,1] neg_hi:[0,0,1]\n"
  "v_cvt_pk_bf16_f32 v140, v24, v25\n"
  "v_cvt_pk_bf16_f32 v141, v26, v27\n"
  "v_cvt_pk_bf16_f32 v142, v28, v29\n"
  "v_cvt_pk_bf16_f32 v143, v30, v31\n"
  "ds_write_b128 v208, v[140:143] offset:12288\n"
  "v_pk_fma_f32 v[32:33], v[32:33], v[206:207], v[206:207] op_sel:[0,0,1] op_sel_hi:[1,0,1] neg_lo:[0,0,1] neg_hi:[0,0,1]\n"
  "v_pk_fma_f32 v[34:35], v[34:35], v[206:207], v[206:207] op_sel:[0,0,1] op_sel_hi:[1,0,1] neg_lo:[0,0,1] neg_hi:[0,0,1]\n"
  "v_pk_fma_f32 v[36:37], v[36:37], v[206:207], v[206:207] op_sel:[0,0,1] op_sel_hi:[1,0,1] neg_lo:[0,0,1] neg_hi:[0,0,1]\n"
  "v_pk_fma_f32 v[38:39], v[38:39], v[206:207], v[206:207] op_sel:[0,0,1] op_sel_hi:[1,0,1] neg_lo:[0,0,1] neg_hi:[0,0,1]\n"
  "v_cvt_pk_bf16_f32 v144, v32, v33\n"
  "v_cvt_pk_bf16_f32 v145, v34, v35\n"
  "v_cvt_pk_bf16_f32 v146, v36, v37\n"
  "v_cvt_pk_bf16_f32 v147, v38, v39\n"
  "ds_write_b128 v208, v[144:147] offset:16384\n"
  "v_pk_fma_f32 v[40:41], v[40:41], v[206:207], v[206:207] op_sel:[0,0,1] op_sel_hi:[1,0,1] neg_lo:[0,0,1] neg_hi:[0,0,1]\n"
  "v_pk_fma_f32 v[42:43], v[42:43], v[206:207], v[206:207] op_sel:[0,0,1] op_sel_hi:[1,0,1] neg_lo:[0,0,1] neg_hi:[0,0,1]\n"
  "v_pk_fma_f32 v[44:45], v[44:45], v[206:207], v[206:207] op_sel:[0,0,1] op_sel_hi:[1,0,1] neg_lo:[0,0,1] neg_hi:[0,0,1]\n"
  "v_pk_fma_f32 v[46:47], v[46:47], v[206:207], v[206:207] op_sel:[0,0,1] op_sel_hi:[1,0,1] neg_lo:[0,0,1] neg_hi:[0,0,1]\n"
  "v_cvt_pk_bf16_f32 v148, v40, v41\n"
  "v_cvt_pk_bf16_f32 v149, v42, v43\n"
  "v_cvt_pk_bf16_f32 v150, v44, v45\n"
  "v_cvt_pk_bf16_f32 v151, v46, v47\n"
  "ds_write_b128 v208, v[148:151] offset:20480\n"
  "v_pk_fma_f32 v[48:49], v[48:49], v[206:207], v[206:207] op_sel:[0,0,1] op_sel_hi:[1,0,1] neg_lo:[0,0,1] neg_hi:[0,0,1]\n"
  "v_pk_fma_f32 v[50:51], v[50:51], v[206:207], v[206:207] op_sel:[0,0,1] op_sel_hi:[1,0,1] neg_lo:[0,0,1] neg_hi:[0,0,1]\n"
  "v_pk_fma_f32 v[52:53], v[52:53], v[206:207], v[206:207] op_sel:[0,0,1] op_sel_hi:[1,0,1] neg_lo:[0,0,1] neg_hi:[0,0,1]\n"
  "v_pk_fma_f32 v[54:55], v[54:55], v[206:207], v[206:207] op_sel:[0,0,1] op_sel_hi:[1,0,1] neg_lo:[0,0,1] neg_hi:[0,0,1]\n"
  "v_cvt_pk_bf16_f32 v152, v48, v49\n"
  "v_cvt_pk_bf16_f32 v153, v50, v51\n"
  "v_cvt_pk_bf16_f32 v154, v52, v53\n"
  "v_cvt_pk_bf16_f32 v155, v54, v55\n"
  "ds_write_b128 v208, v[152:155] offset:24576\n"
  "v_pk_fma_f32 v[56:57], v[56:57], v[206:207], v[206:207] op_sel:[0,0,1] op_sel_hi:[1,0,1] neg_lo:[0,0,1] neg_hi:[0,0,1]\n"
  "v_pk_fma_f32 v[58:59], v[58:59], v[206:207], v[206:207] op_sel:[0,0,1] op_sel_hi:[1,0,1] neg_lo:[0,0,1] neg_hi:[0,0,1]\n"
  "v_pk_fma_f32 v[60:61], v[60:61], v[206:207], v[206:207] op_sel:[0,0,1] op_sel_hi:[1,0,1] neg_lo:[0,0,1] neg_hi:[0,0,1]\n"
  "v_pk_fma_f32 v[62:63], v[62:63], v[206:207], v[206:207] op_sel:[0,0,1] op_sel_hi:[1,0,1] neg_lo:[0,0,1] neg_hi:[0,0,1]\n"
  "v_cvt_pk_bf16_f32 v156, v56, v57\n"
  "v_cvt_pk_bf16_f32 v157, v58, v59\n"
  "v_cvt_pk_bf16_f32 v158, v60, v61\n"
  "v_cvt_pk_bf16_f32 v159, v62, v63\n"
  "ds_write_b128 v208, v[156:159] offset:28672\n"
  "L_dry0_0:\n"
  "L_pad0:\n"
  "s_cbranch_execz L_dryr0\n"
  "v_pk_fma_f32 v[64:65], v[64:65], v[206:207], v[206:207] op_sel:[0,0,1] op_sel_hi:[1,0,1] neg_lo:[0,0,1] neg_hi:[0,0,1]\n"
  "v_pk_fma_f32 v[66:67], v[66:67], v[206:207], v[206:207] op_sel:[0,0,1] op_sel_hi:[1,0,1] neg_lo:[0,0,1] neg_hi:[0,0,1]\n"
  "v_pk_fma_f32 v[68:69], v[68:69], v[206:207], v[206:207] op_sel:[0,0,1] op_sel_hi:[1,0,1] neg_lo:[0,0,1] neg_hi:[0,0,1]\n"
  "v_pk_fma_f32 v[70:71], v[70:71], v[206:207], v[206:207] op_sel:[0,0,1] op_sel_hi:[1,0,1] neg_lo:[0,0,1] neg_hi:[0,0,1]\n"
  "v_cvt_pk_bf16_f32 v160, v64, v65\n"
  "v_cvt_pk_bf16_f32 v161, v66, v67\n"
  "v_cvt_pk_bf16_f32 v162, v68, v69\n"
  "v_cvt_pk_bf16_f32 v163, v70, v71\n"
  "ds_write_b128 v208, v[160:163] offset:32768\n"
  "v_pk_fma_f32 v[72:73], v[72:73], v[206:207], v[206:207] op_sel:[0,0,1] op_sel_hi:[1,0,1] neg_lo:[0,0,1] neg_hi:[0,0,1]\n"
  "v_pk_fma_f32 v[74:75], v[74:75], v[206:207], v[206:207] op_sel:[0,0,1] op_sel_hi:[1,0,1] neg_lo:[0,0,1] neg_hi:[0,0,1]\n"
  "v_pk_fma_f32 v[76:77], v[76:77], v[206:207], v[206:207] op_sel:[0,0,1] op_sel_hi:[1,0,1] neg_lo:[0,0,1] neg_hi:[0,0,1]\n"
  "v_pk_fma_f32 v[78:79], v[78:79], v[206:207], v[206:207] op_sel:[0,0,1] op_sel_hi:[1,0,1] neg_lo:[0,0,1] neg_hi:[0,0,1]\n"
  "v_cvt_pk_bf16_f32 v164, v72, v73\n"
  "v_cvt_pk_bf16_f32 v165, v74, v75\n"
  "v_cvt_pk_bf16_f32 v166, v76, v77\n"
  "v_cvt_pk_bf16_f32 v167, v78, v79\n"
  "ds_write_b128 v208, v[164:167] offset:36864\n"
  "v_pk_fma_f32 v[80:81], v[80:81], v[206:207], v[206:207] op_sel:[0,0,1] op_sel_hi:[1,0,1] neg_lo:[0,0,1] neg_hi:[0,0,1]\n"
  "v_pk_fma_f32 v[82:83], v[82:83], v[206:207], v[206:207] op_sel:[0,0,1] op_sel_hi:[1,0,1] neg_lo:[0,0,1] neg_hi:[0,0,1]\n"
  "v_pk_fma_f32 v[84:85], v[84:85], v[206:207], v[206:207] op_sel:[0,0,1] op_sel_hi:[1,0,1] neg_lo:[0,0,1] neg_hi:[0,0,1]\n"
  "v_pk_fma_f32 v[86:87], v[86:87], v[206:207], v[206:207] op_sel:[0,0,1] op_sel_hi:[1,0,1] neg_lo:[0,0,1] neg_hi:[0,0,1]\n"
  "v_cvt_pk_bf16_f32 v168, v80, v81\n"
  "v_cvt_pk_bf16_f32 v169, v82, v83\n"
  "v_cvt_pk_bf16_f32 v170, v84, v85\n"
  "v_cvt_pk_bf16_f32 v171, v86, v87\n"
  "ds_write_b128 v208, v[168:171] offset:40960\n"
  "v_pk_fma_f32 v[88:89], v[88:89], v[206:207], v[206:207] op_sel:[0,0,1] op_sel_hi:[1,0,1] neg_lo:[0,0,1] neg_hi:[0,0,1]\n"
  "v_pk_fma_f32 v[90:91], v[90:91], v[206:207], v[206:207] op_sel:[0,0,1] op_sel_hi:[1,0,1] neg_lo:[0,0,1] neg_hi:[0,0,1]\n"
  "v_pk_fma_f32 v[92:93], v[92:93], v[206:207], v[206:207] op_sel:[0,0,1] op_sel_hi:[1,0,1] neg_lo:[0,0,1] neg_hi:[0,0,1]\n"
  "v_pk_fma_f32 v[94:95], v[94:95], v[206:207], v[206:207] op_sel:[0,0,1] op_sel_hi:[1,0,1] neg_lo:[0,0,1] neg_hi:[0,0,1]\n"
  "v_cvt_pk_bf16_f32 v172, v88, v89\n"
  "v_cvt_pk_bf16_f32 v173, v90, v91\n"
  "v_cvt_pk_bf16_f32 v174, v92, v93\n"
  "v_cvt_pk_bf16_f32 v175, v94, v95\n"
  "ds_write_b128 v208, v[172:175] offset:45056\n"
  "v_pk_fma_f32 v[96:97], v[96:97], v[206:207], v[206:207] op_sel:[0,0,1] op_sel_hi:[1,0,1] neg_lo:[0,0,1] neg_hi:[0,0,1]\n"
  "v_pk_fma_f32 v[98:99], v[98:99], v[206:207], v[206:207] op_sel:[0,0,1] op_sel_hi:[1,0,1] neg_lo:[0,0,1] neg_hi:[0,0,1]\n"
  "v_pk_fma_f32 v[100:101], v[100:101], v[206:207], v[206:207] op_sel:[0,0,1] op_sel_hi:[1,0,1] neg_lo:[0,0,1] neg_hi:[0,0,1]\n"
  "v_pk_fma_f32 v[102:103], v[102:103], v[206:207], v[206:207] op_sel:[0,0,1] op_sel_hi:[1,0,1] neg_lo:[0,0,1] neg_hi:[0,0,1]\n"
  "v_cvt_pk_bf16_f32 v176, v96, v97\n"
  "v_cvt_pk_bf16_f32 v177, v98, v99\n"
  "v_cvt_pk_bf16_f32 v178, v100, v101\n"
  "v_cvt_pk_bf16_f32 v179, v102, v103\n"
  "ds_write_b128 v208, v[176:179] offset:49152\n"
  "v_pk_fma_f32 v[104:105], v[104:105], v[206:207], v[206:207] op_sel:[0,0,1] op_sel_hi:[1,0,1] neg_lo:[0,0,1] neg_hi:[0,0,1]\n"
  "v_pk_fma_f32 v[106:107], v[106:107], v[206:207], v[206:207] op_sel:[0,0,1] op_sel_hi:[1,0,1] neg_lo:[0,0,1] neg_hi:[0,0,1]\n"
  "v_pk_fma_f32 v[108:109], v[108:109], v[206:207], v[206:207] op_sel:[0,0,1] op_sel_hi:[1,0,1] neg_lo:[0,0,1] neg_hi:[0,0,1]\n"
  "v_pk_fma_f32 v[110:111], v[110:111], v[206:207], v[206:207] op_sel:[0,0,1] op_sel_hi:[1,0,1] neg_lo:[0,0,1] neg_hi:[0,0,1]\n"
  "v_cvt_pk_bf16_f32 v180, v104, v105\n"
  "v_cvt_pk_bf16_f32 v181, v106, v107\n"
  "v_cvt_pk_bf16_f32 v182, v108, v109\n"
  "v_cvt_pk_bf16_f32 v183, v110, v111\n"
  "ds_write_b128 v208, v[180:183] offset:53248\n"
  "v_pk_fma_f32 v[112:113], v[112:113], v[206:207], v[206:207] op_sel:[0,0,1] op_sel_hi:[1,0,1] neg_lo:[0,0,1] neg_hi:[0,0,1]\n"
  "v_pk_fma_f32 v[114:115], v[114:115], v[206:207], v[206:207] op_sel:[0,0,1] op_sel_hi:[1,0,1] neg_lo:[0,0,1] neg_hi:[0,0,1]\n"
  "v_pk_fma_f32 v[116:117], v[116:117], v[206:207], v[206:207] op_sel:[0,0,1] op_sel_hi:[1,0,1] neg_lo:[0,0,1] neg_hi:[0,0,1]\n"
  "v_pk_fma_f32 v[118:119], v[118:119], v[206:207], v[206:207] op_sel:[0,0,1] op_sel_hi:[1,0,1] neg_lo:[0,0,1] neg_hi:[0,0,1]\n"
  "v_cvt_pk_bf16_f32 v184, v112, v113\n"
  "v_cvt_pk_bf16_f32 v185, v114, v115\n"
  "v_cvt_pk_bf16_f32 v186, v116, v117\n"
  "v_cvt_pk_bf16_f32 v187, v118, v119\n"
  "ds_write_b128 v208, v[184:187] offset:57344\n"
  "v_pk_fma_f32 v[120:121], v[120:121], v[206:207], v[206:207] op_sel:[0,0,1] op_sel_hi:[1,0,1] neg_lo:[0,0,1] neg_hi:[0,0,1]\n"
  "v_pk_fma_f32 v[122:123], v[122:123], v[206:207], v[206:207] op_sel:[0,0,1] op_sel_hi:[1,0,1] neg_lo:[0,0,1] neg_hi:[0,0,1]\n"
  "v_pk_fma_f32 v[124:125], v[124:125], v[206:207], v[206:207] op_sel:[0,0,1] op_sel_hi:[1,0,1] neg_lo:[0,0,1] neg_hi:[0,0,1]\n"
  "v_pk_fma_f32 v[126:127], v[126:127], v[206:207], v[206:207] op_sel:[0,0,1] op_sel_hi:[1,0,1] neg_lo:[0,0,1] neg_hi:[0,0,1]\n"
  "v_cvt_pk_bf16_f32 v188, v120, v121\n"
  "v_cvt_pk_bf16_f32 v189, v122, v123\n"
  "v_cvt_pk_bf16_f32 v190, v124, v125\n"
  "v_cvt_pk_bf16_f32 v191, v126, v127\n"
  "ds_write_b128 v208, v[188:191] offset:61440\n"
  "s_waitcnt lgkmcnt(0)\n"
  "s_barrier\n"
  "ds_read_b128 a[0:3], v192 offset:0\n"
  "ds_read_b128 a[4:7], v192 offset:4096\n"
  "ds_read_b128 a[8:11], v192 offset:1024\n"
  "ds_read_b128 a[12:15], v192 offset:5120\n"
  "s_waitcnt lgkmcnt(3)\n"
  "v_mfma_f32_16x16x32_bf16 v[128:131], a[64:67], a[0:3], 0\n"
  "s_waitcnt lgkmcnt(2)\n"
  "s_add_u32 m0, s51, 0\n"
  "ds_read_b128 a[16:19], v215 offset:0\n"
  "v_mfma_f32_16x16x32_bf16 v[128:131], a[68:71], a[4:7], v[128:131]\n"
  "s_mov_b32 s53, 229376\n"
  "ds_read_b128 a[20:23], v215 offset:4096\n"
  "v_mfma_f32_16x16x32_bf16 v[144:147], a[72:75], a[0:3], 0\n"
  "buffer_load_dwordx4 v244, s[20:23], s53 offen offset:0 lds\n"
  "v_mfma_f32_16x16x32_bf16 v[144:147], a[76:79], a[4:7], v[144:147]\n"
  "ds_read_b128 a[24:27], v215 offset:1024\n"
  "v_mfma_f32_16x16x32_bf16 v[160:163], a[80:83], a[0:3], 0\n"
  "ds_read_b128 a[28:31], v215 offset:5120\n"
  "v_mfma_f32_16x16x32_bf16 v[160:163], a[84:87], a[4:7], v[160:163]\n"
  "ds_read_b64_tr_b16 a[96:97], v194 offset:8192\n"
  "v_mfma_f32_16x16x32_bf16 v[176:179], a[88:91], a[0:3], 0\n"
  "buffer_load_dwordx4 v245, s[20:23], s53 offen offset:1024 lds\n"
  "v_mfma_f32_16x16x32_bf16 v[176:179], a[92:95], a[4:7], v[176:179]\n"
  "s_waitcnt lgkmcnt(6)\n"
  "ds_read_b64_tr_b16 a[98:99], v194 offset:10240\n"
  "v_mfma_f32_16x16x32_bf16 v[132:135], a[64:67], a[8:11], 0\n"
  "s_waitcnt lgkmcnt(6)\n"
  "ds_read_b64_tr_b16 a[104:105], v195 offset:8192\n"
  "v_mfma_f32_16x16x32_bf16 v[132:135], a[68:71], a[12:15], v[132:135]\n"
  "ds_read_b64_tr_b16 a[106:107], v195 offset:10240\n"
  "v_mfma_f32_16x16x32_bf16 v[148:151], a[72:75], a[8:11], 0\n"
  "buffer_load_dwordx4 v246, s[20:23], s53 offen offset:2048 lds\n"
  "v_mfma_f32_16x16x32_bf16 v[148:151], a[76:79], a[12:15], v[148:151]\n"
  "ds_read_b64_tr_b16 a[112:113], v196 offset:8192\n"
  "v_mfma_f32_16x16x32_bf16 v[164:167], a[80:83], a[8:11], 0\n"
  "ds_read_b64_tr_b16 a[114:115], v196 offset:10240\n"
  "v_mfma_f32_16x16x32_bf16 v[164:167], a[84:87], a[12:15], v[164:167]\n"
  "ds_read_b64_tr_b16 a[120:121], v197 offset:8192\n"
  "v_mfma_f32_16x16x32_bf16 v[180:183], a[88:91], a[8:11], 0\n"
  "buffer_load_dwordx4 v247, s[20:23], s53 offen offset:3072 lds\n"
  "v_mfma_f32_16x16x32_bf16 v[180:183], a[92:95], a[12:15], v[180:183]\n"
  "s_waitcnt lgkmcnt(6)\n"
  "ds_read_b64_tr_b16 a[122:123], v197 offset:10240\n"
  "v_mfma_f32_16x16x32_bf16 v[136:139], a[64:67], a[16:19], 0\n"
  "s_waitcnt vmcnt(15)\n"
  "s_add_u32 m0, s51, 4096\n"
  "ds_read_b64_tr_b16 a[100:101], v194 offset:12288\n"
  "v_mfma_f32_16x16x32_bf16 v[136:139], a[68:71], a[20:23], v[136:139]\n"
  "s_mov_b32 s54, 344064\n"
  "ds_read_b64_tr_b16 a[102:103], v194 offset:14336\n"
  "v_mfma_f32_16x16x32_bf16 v[152:155], a[72:75], a[16:19], 0\n"
  "buffer_load_dwordx4 v244, s[20:23], s54 offen offset:0 lds\n"
  "v_mfma_f32_16x16x32_bf16 v[152:155], a[76:79], a[20:23], v[152:155]\n"
  "ds_read_b64_tr_b16 a[108:109], v195 offset:12288\n"
  "v_mfma_f32_16x16x32_bf16 v[168:171], a[80:83], a[16:19], 0\n"
  "ds_read_b64_tr_b16 a[110:111], v195 offset:14336\n"
  "v_mfma_f32_16x16x32_bf16 v[168:171], a[84:87], a[20:23], v[168:171]\n"
  "ds_read_b64_tr_b16 a[116:117], v196 offset:12288\n"
  "v_mfma_f32_16x16x32_bf16 v[184:187], a[88:91], a[16:19], 0\n"
  "buffer_load_dwordx4 v245, s[20:23], s54 offen offset:1024 lds\n"
  "v_mfma_f32_16x16x32_bf16 v[184:187], a[92:95], a[20:23], v[184:187]\n"
  "ds_read_b64_tr_b16 a[118:119], v196 offset:14336\n"
  "v_mfma_f32_16x16x32_bf16 v[140:143], a[64:67], a[24:27], 0\n"
  "ds_read_b64_tr_b16 a[124:125], v197 offset:12288\n"
  "v_mfma_f32_16x16x32_bf16 v[140:143], a[68:71], a[28:31], v[140:143]\n"
  "ds_read_b64_tr_b16 a[126:127], v197 offset:14336\n"
  "v_mfma_f32_16x16x32_bf16 v[156:159], a[72:75], a[24:27], 0\n"
  "buffer_load_dwordx4 v246, s[20:23], s54 offen offset:2048 lds\n"
  "v_mfma_f32_16x16x32_bf16 v[156:159], a[76:79], a[28:31], v[156:159]\n"
  "v_mfma_f32_16x16x32_bf16 v[172:175], a[80:83], a[24:27], 0\n"
  "v_mfma_f32_16x16x32_bf16 v[172:175], a[84:87], a[28:31], v[172:175]\n"
  "v_mfma_f32_16x16x32_bf16 v[188:191], a[88:91], a[24:27], 0\n"
  "buffer_load_dwordx4 v247, s[20:23], s54 offen offset:3072 lds\n"
  "v_mfma_f32_16x16x32_bf16 v[188:191], a[92:95], a[28:31], v[188:191]\n"
  "s_waitcnt lgkmcnt(3)\n"
  "v_mfma_f32_16x16x32_bf16 v[0:3], a[96:99], a[0:3], 0\n"
  "s_waitcnt vmcnt(15)\n"
  "s_add_u32 m0, s51, 8192\n"
  "ds_read_b64_tr_b16 a[64:65], v194 offset:16384\n"
  "v_mfma_f32_16x16x32_bf16 v[0:3], a[100:103], a[4:7], v[0:3]\n"
  "s_xor_b32 s53, s62, 524288\n"
  "ds_read_b64_tr_b16 a[66:67], v194 offset:18432\n"
  "v_mfma_f32_16x16x32_bf16 v[16:19], a[104:107], a[0:3], 0\n"
  "buffer_load_dwordx4 v240, s[16:19], s53 offen offset:0 lds\n"
  "v_mfma_f32_16x16x32_bf16 v[16:19], a[108:111], a[4:7], v[16:19]\n"
  "ds_read_b64_tr_b16 a[72:73], v195 offset:16384\n"
  "v_mfma_f32_16x16x32_bf16 v[32:35], a[112:115], a[0:3], 0\n"
  "s_waitcnt lgkmcnt(3)\n"
  "ds_read_b64_tr_b16 a[74:75], v195 offset:18432\n"
  "v_mfma_f32_16x16x32_bf16 v[32:35], a[116:119], a[4:7], v[32:35]\n"
  "ds_read_b64_tr_b16 a[80:81], v196 offset:16384\n"
  "v_mfma_f32_16x16x32_bf16 v[48:51], a[120:123], a[0:3], 0\n"
  "buffer_load_dwordx4 v241, s[16:19], s53 offen offset:1024 lds\n"
  "v_mfma_f32_16x16x32_bf16 v[48:51], a[124:127], a[4:7], v[48:51]\n"
  "ds_read_b64_tr_b16 a[82:83], v196 offset:18432\n"
  "v_mfma_f32_16x16x32_bf16 v[4:7], a[96:99], a[8:11], 0\n"
  "ds_read_b64_tr_b16 a[88:89], v197 offset:16384\n"
  "v_mfma_f32_16x16x32_bf16 v[4:7], a[100:103], a[12:15], v[4:7]\n"
  "ds_read_b64_tr_b16 a[90:91], v197 offset:18432\n"
  "v_mfma_f32_16x16x32_bf16 v[20:23], a[104:107], a[8:11], 0\n"
  "buffer_load_dwordx4 v242, s[16:19], s53 offen offset:2048 lds\n"
  "v_mfma_f32_16x16x32_bf16 v[20:23], a[108:111], a[12:15], v[20:23]\n"
  "v_mfma_f32_16x16x32_bf16 v[36:39], a[112:115], a[8:11], 0\n"
  "v_mfma_f32_16x16x32_bf16 v[36:39], a[116:119], a[12:15], v[36:39]\n"
  "s_waitcnt vmcnt(14)\n"
  "ds_read_b64_tr_b16 a[68:69], v194 offset:20480\n"
  "v_mfma_f32_16x16x32_bf16 v[52:55], a[120:123], a[8:11], 0\n"
  "buffer_load_dwordx4 v243, s[16:19], s53 offen offset:3072 lds\n"
  "v_mfma_f32_16x16x32_bf16 v[52:55], a[124:127], a[12:15], v[52:55]\n"
  "ds_read_b64_tr_b16 a[70:71], v194 offset:22528\n"
  "v_mfma_f32_16x16x32_bf16 v[8:11], a[96:99], a[16:19], 0\n"
  "s_add_u32 m0, s51, 12288\n"
  "ds_read_b64_tr_b16 a[76:77], v195 offset:20480\n"
  "v_mfma_f32_16x16x32_bf16 v[8:11], a[100:103], a[20:23], v[8:11]\n"
  "s_xor_b32 s54, s62, 786432\n"
  "ds_read_b64_tr_b16 a[78:79], v195 offset:22528\n"
  "v_mfma_f32_16x16x32_bf16 v[24:27], a[104:107], a[16:19], 0\n"
  "buffer_load_dwordx4 v240, s[16:19], s54 offen offset:0 lds\n"
  "v_mfma_f32_16x16x32_bf16 v[24:27], a[108:111], a[20:23], v[24:27]\n"
  "ds_read_b64_tr_b16 a[84:85], v196 offset:20480\n"
  "v_mfma_f32_16x16x32_bf16 v[40:43], a[112:115], a[16:19], 0\n"
  "ds_read_b64_tr_b16 a[86:87], v196 offset:22528\n"
  "v_mfma_f32_16x16x32_bf16 v[40:43], a[116:119], a[20:23], v[40:43]\n"
  "ds_read_b64_tr_b16 a[92:93], v197 offset:20480\n"
  "v_mfma_f32_16x16x32_bf16 v[56:59], a[120:123], a[16:19], 0\n"
  "buffer_load_dwordx4 v241, s[16:19], s54 offen offset:1024 lds\n"
  "v_mfma_f32_16x16x32_bf16 v[56:59], a[124:127], a[20:23], v[56:59]\n"
  "ds_read_b64_tr_b16 a[94:95], v197 offset:22528\n"
  "v_mfma_f32_16x16x32_bf16 v[12:15], a[96:99], a[24:27], 0\n"
  "v_mfma_f32_16x16x32_bf16 v[12:15], a[100:103], a[28:31], v[12:15]\n"
  "v_mfma_f32_16x16x32_bf16 v[28:31], a[104:107], a[24:27], 0\n"
  "buffer_load_dwordx4 v242, s[16:19], s54 offen offset:2048 lds\n"
  "v_mfma_f32_16x16x32_bf16 v[28:31], a[108:111], a[28:31], v[28:31]\n"
  "v_mfma_f32_16x16x32_bf16 v[44:47], a[112:115], a[24:27], 0\n"
  "v_mfma_f32_16x16x32_bf16 v[44:47], a[116:119], a[28:31], v[44:47]\n"
  "v_mfma_f32_16x16x32_bf16 v[60:63], a[120:123], a[24:27], 0\n"
  "buffer_load_dwordx4 v243, s[16:19], s54 offen offset:3072 lds\n"
  "v_mfma_f32_16x16x32_bf16 v[60:63], a[124:127], a[28:31], v[60:63]\n"
  "s_waitcnt lgkmcnt(1)\n"
  "v_mfma_f32_16x16x32_bf16 v[64:67], a[64:67], a[0:3], 0\n"
  "s_waitcnt vmcnt(12)\n"
  "s_add_u32 m0, s51, 16384\n"
  "ds_read_b64_tr_b16 a[96:97], v194 offset:0\n"
  "v_mfma_f32_16x16x32_bf16 v[64:67], a[68:71], a[4:7], v[64:67]\n"
  "s_xor_b32 s53, s62, 524416\n"
  "ds_read_b64_tr_b16 a[98:99], v194 offset:2048\n"
  "v_mfma_f32_16x16x32_bf16 v[80:83], a[72:75], a[0:3], 0\n"
  "buffer_load_dwordx4 v240, s[16:19], s53 offen offset:0 lds\n"
  "v_mfma_f32_16x16x32_bf16 v[80:83], a[76:79], a[4:7], v[80:83]\n"
  "ds_read_b64_tr_b16 a[104:105], v195 offset:0\n"
  "v_mfma_f32_16x16x32_bf16 v[96:99], a[80:83], a[0:3], 0\n"
  "ds_read_b64_tr_b16 a[106:107], v195 offset:2048\n"
  "v_mfma_f32_16x16x32_bf16 v[96:99], a[84:87], a[4:7], v[96:99]\n"
  "ds_read_b64_tr_b16 a[112:113], v196 offset:0\n"
  "v_mfma_f32_16x16x32_bf16 v[112:115], a[88:91], a[0:3], 0\n"
  "s_waitcnt lgkmcnt(5)\n"
  "buffer_load_dwordx4 v241, s[16:19], s53 offen offset:1024 lds\n"
  "v_mfma_f32_16x16x32_bf16 v[112:115], a[92:95], a[4:7], v[112:115]\n"
  "ds_read_b64_tr_b16 a[114:115], v196 offset:2048\n"
  "v_mfma_f32_16x16x32_bf16 v[68:71], a[64:67], a[8:11], 0\n"
  "ds_read_b128 a[0:3], v192 offset:8192\n"
  "v_mfma_f32_16x16x32_bf16 v[68:71], a[68:71], a[12:15], v[68:71]\n"
  "ds_read_b128 a[4:7], v192 offset:12288\n"
  "v_mfma_f32_16x16x32_bf16 v[84:87], a[72:75], a[8:11], 0\n"
  "buffer_load_dwordx4 v242, s[16:19], s53 offen offset:2048 lds\n"
  "v_mfma_f32_16x16x32_bf16 v[84:87], a[76:79], a[12:15], v[84:87]\n"
  "ds_read_b64_tr_b16 a[120:121], v197 offset:0\n"
  "v_mfma_f32_16x16x32_bf16 v[100:103], a[80:83], a[8:11], 0\n"
  "ds_read_b64_tr_b16 a[122:123], v197 offset:2048\n"
  "v_mfma_f32_16x16x32_bf16 v[100:103], a[84:87], a[12:15], v[100:103]\n"
  "s_waitcnt vmcnt(11)\n"
  "ds_read_b64_tr_b16 a[100:101], v194 offset:4096\n"
  "v_mfma_f32_16x16x32_bf16 v[116:119], a[88:91], a[8:11], 0\n"
  "buffer_load_dwordx4 v243, s[16:19], s53 offen offset:3072 lds\n"
  "v_mfma_f32_16x16x32_bf16 v[116:119], a[92:95], a[12:15], v[116:119]\n"
  "ds_read_b64_tr_b16 a[102:103], v194 offset:6144\n"
  "v_mfma_f32_16x16x32_bf16 v[72:75], a[64:67], a[16:19], 0\n"
  "s_add_u32 m0, s51, 20480\n"
  "ds_read_b128 a[8:11], v192 offset:9216\n"
  "v_mfma_f32_16x16x32_bf16 v[72:75], a[68:71], a[20:23], v[72:75]\n"
  "s_xor_b32 s54, s62, 786560\n"
  "ds_read_b128 a[12:15], v192 offset:13312\n"
  "v_mfma_f32_16x16x32_bf16 v[88:91], a[72:75], a[16:19], 0\n"
  "buffer_load_dwordx4 v240, s[16:19], s54 offen offset:0 lds\n"
  "v_mfma_f32_16x16x32_bf16 v[88:91], a[76:79], a[20:23], v[88:91]\n"
  "ds_read_b64_tr_b16 a[108:109], v195 offset:4096\n"
  "v_mfma_f32_16x16x32_bf16 v[104:107], a[80:83], a[16:19], 0\n"
  "ds_read_b64_tr_b16 a[110:111], v195 offset:6144\n"
  "v_mfma_f32_16x16x32_bf16 v[104:107], a[84:87], a[20:23], v[104:107]\n"
  "ds_read_b64_tr_b16 a[116:117], v196 offset:4096\n"
  "v_mfma_f32_16x16x32_bf16 v[120:123], a[88:91], a[16:19], 0\n"
  "buffer_load_dwordx4 v241, s[16:19], s54 offen offset:1024 lds\n"
  "v_mfma_f32_16x16x32_bf16 v[120:123], a[92:95], a[20:23], v[120:123]\n"
  "ds_read_b64_tr_b16 a[118:119], v196 offset:6144\n"
  "v_mfma_f32_16x16x32_bf16 v[76:79], a[64:67], a[24:27], 0\n"
  "ds_read_b64_tr_b16 a[124:125], v197 offset:4096\n"
  "v_mfma_f32_16x16x32_bf16 v[76:79], a[68:71], a[28:31], v[76:79]\n"
  "ds_read_b64_tr_b16 a[126:127], v197 offset:6144\n"
  "v_mfma_f32_16x16x32_bf16 v[92:95], a[72:75], a[24:27], 0\n"
  "buffer_load_dwordx4 v242, s[16:19], s54 offen offset:2048 lds\n"
  "v_mfma_f32_16x16x32_bf16 v[92:95], a[76:79], a[28:31], v[92:95]\n"
  "v_mfma_f32_16x16x32_bf16 v[108:111], a[80:83], a[24:27], 0\n"
  "v_mfma_f32_16x16x32_bf16 v[108:111], a[84:87], a[28:31], v[108:111]\n"
  "v_mfma_f32_16x16x32_bf16 v[124:127], a[88:91], a[24:27], 0\n"
  "buffer_load_dwordx4 v243, s[16:19], s54 offen offset:3072 lds\n"
  "v_mfma_f32_16x16x32_bf16 v[124:127], a[92:95], a[28:31], v[124:127]\n"
  "s_waitcnt lgkmcnt(3)\n"
  "v_mfma_f32_16x16x32_bf16 v[128:131], a[96:99], a[0:3], v[128:131]\n"
  "s_add_u32 m0, s51, 0\n"
  "ds_read_b128 a[16:19], v215 offset:8192\n"
  "v_mfma_f32_16x16x32_bf16 v[128:131], a[100:103], a[4:7], v[128:131]\n"
  "s_mov_b32 s53, 458752\n"
  "ds_read_b128 a[20:23], v215 offset:12288\n"
  "v_mfma_f32_16x16x32_bf16 v[144:147], a[104:107], a[0:3], v[144:147]\n"
  "buffer_load_dwordx4 v244, s[20:23], s53 offen offset:0 lds\n"
  "v_mfma_f32_16x16x32_bf16 v[144:147], a[108:111], a[4:7], v[144:147]\n"
  "ds_read_b128 a[24:27], v215 offset:9216\n"
  "v_mfma_f32_16x16x32_bf16 v[160:163], a[112:115], a[0:3], v[160:163]\n"
  "s_waitcnt lgkmcnt(3)\n"
  "ds_read_b128 a[28:31], v215 offset:13312\n"
  "v_mfma_f32_16x16x32_bf16 v[160:163], a[116:119], a[4:7], v[160:163]\n"
  "s_waitcnt vmcnt(13)\n"
  "ds_read_b64_tr_b16 a[64:65], v194 offset:8192\n"
  "v_mfma_f32_16x16x32_bf16 v[176:179], a[120:123], a[0:3], v[176:179]\n"
  "buffer_load_dwordx4 v245, s[20:23], s53 offen offset:1024 lds\n"
  "v_mfma_f32_16x16x32_bf16 v[176:179], a[124:127], a[4:7], v[176:179]\n"
  "ds_read_b64_tr_b16 a[66:67], v194 offset:10240\n"
  "v_mfma_f32_16x16x32_bf16 v[132:135], a[96:99], a[8:11], v[132:135]\n"
  "ds_read_b64_tr_b16 a[72:73], v195 offset:8192\n"
  "v_mfma_f32_16x16x32_bf16 v[132:135], a[100:103], a[12:15], v[132:135]\n"
  "ds_read_b64_tr_b16 a[74:75], v195 offset:10240\n"
  "v_mfma_f32_16x16x32_bf16 v[148:151], a[104:107], a[8:11], v[148:151]\n"
  "buffer_load_dwordx4 v246, s[20:23], s53 offen offset:2048 lds\n"
  "v_mfma_f32_16x16x32_bf16 v[148:151], a[108:111], a[12:15], v[148:151]\n"
  "ds_read_b64_tr_b16 a[80:81], v196 offset:8192\n"
  "v_mfma_f32_16x16x32_bf16 v[164:167], a[112:115], a[8:11], v[164:167]\n"
  "ds_read_b64_tr_b16 a[82:83], v196 offset:10240\n"
  "v_mfma_f32_16x16x32_bf16 v[164:167], a[116:119], a[12:15], v[164:167]\n"
  "ds_read_b64_tr_b16 a[88:89], v197 offset:8192\n"
  "v_mfma_f32_16x16x32_bf16 v[180:183], a[120:123], a[8:11], v[180:183]\n"
  "buffer_load_dwordx4 v247, s[20:23], s53 offen offset:3072 lds\n"
  "v_mfma_f32_16x16x32_bf16 v[180:183], a[124:127], a[12:15], v[180:183]\n"
  "s_waitcnt lgkmcnt(6)\n"
  "ds_read_b64_tr_b16 a[90:91], v197 offset:10240\n"
  "v_mfma_f32_16x16x32_bf16 v[136:139], a[96:99], a[16:19], v[136:139]\n"
  "s_waitcnt vmcnt(12)\n"
  "s_add_u32 m0, s51, 4096\n"
  "ds_read_b64_tr_b16 a[68:69], v194 offset:12288\n"
  "v_mfma_f32_16x16x32_bf16 v[136:139], a[100:103], a[20:23], v[136:139]\n"
  "s_mov_b32 s54, 573440\n"
  "ds_read_b64_tr_b16 a[70:71], v194 offset:14336\n"
  "v_mfma_f32_16x16x32_bf16 v[152:155], a[104:107], a[16:19], v[152:155]\n"
  "buffer_load_dwordx4 v244, s[20:23], s54 offen offset:0 lds\n"
  "v_mfma_f32_16x16x32_bf16 v[152:155], a[108:111], a[20:23], v[152:155]\n"
  "ds_read_b64_tr_b16 a[76:77], v195 offset:12288\n"
  "v_mfma_f32_16x16x32_bf16 v[168:171], a[112:115], a[16:19], v[168:171]\n"
  "ds_read_b64_tr_b16 a[78:79], v195 offset:14336\n"
  "v_mfma_f32_16x16x32_bf16 v[168:171], a[116:119], a[20:23], v[168:171]\n"
  "ds_read_b64_tr_b16 a[84:85], v196 offset:12288\n"
  "v_mfma_f32_16x16x32_bf16 v[184:187], a[120:123], a[16:19], v[184:187]\n"
  "buffer_load_dwordx4 v245, s[20:23], s54 offen offset:1024 lds\n"
  "v_mfma_f32_16x16x32_bf16 v[184:187], a[124:127], a[20:23], v[184:187]\n"
  "ds_read_b64_tr_b16 a[86:87], v196 offset:14336\n"
  "v_mfma_f32_16x16x32_bf16 v[140:143], a[96:99], a[24:27], v[140:143]\n"
  "ds_read_b64_tr_b16 a[92:93], v197 offset:12288\n"
  "v_mfma_f32_16x16x32_bf16 v[140:143], a[100:103], a[28:31], v[140:143]\n"
  "ds_read_b64_tr_b16 a[94:95], v197 offset:14336\n"
  "v_mfma_f32_16x16x32_bf16 v[156:159], a[104:107], a[24:27], v[156:159]\n"
  "buffer_load_dwordx4 v246, s[20:23], s54 offen offset:2048 lds\n"
  "v_mfma_f32_16x16x32_bf16 v[156:159], a[108:111], a[28:31], v[156:159]\n"
  "v_mfma_f32_16x16x32_bf16 v[172:175], a[112:115], a[24:27], v[172:175]\n"
  "v_mfma_f32_16x16x32_bf16 v[172:175], a[116:119], a[28:31], v[172:175]\n"
  "v_mfma_f32_16x16x32_bf16 v[188:191], a[120:123], a[24:27], v[188:191]\n"
  "buffer_load_dwordx4 v247, s[20:23], s54 offen offset:3072 lds\n"
  "v_mfma_f32_16x16x32_bf16 v[188:191], a[124:127], a[28:31], v[188:191]\n"
  "s_waitcnt lgkmcnt(3)\n"
  "v_mfma_f32_16x16x32_bf16 v[0:3], a[64:67], a[0:3], v[0:3]\n"
  "s_waitcnt vmcnt(12)\n"
  "s_add_u32 m0, s51, 8192\n"
  "ds_read_b64_tr_b16 a[96:97], v194 offset:16384\n"
  "v_mfma_f32_16x16x32_bf16 v[0:3], a[68:71], a[4:7], v[0:3]\n"
  "s_xor_b32 s53, s62, 1048576\n"
  "ds_read_b64_tr_b16 a[98:99], v194 offset:18432\n"
  "v_mfma_f32_16x16x32_bf16 v[16:19], a[72:75], a[0:3], v[16:19]\n"
  "buffer_load_dwordx4 v240, s[16:19], s53 offen offset:0 lds\n"
  "v_mfma_f32_16x16x32_bf16 v[16:19], a[76:79], a[4:7], v[16:19]\n"
  "ds_read_b64_tr_b16 a[104:105], v195 offset:16384\n"
  "v_mfma_f32_16x16x32_bf16 v[32:35], a[80:83], a[0:3], v[32:35]\n"
  "s_waitcnt lgkmcnt(3)\n"
  "ds_read_b64_tr_b16 a[106:107], v195 offset:18432\n"
  "v_mfma_f32_16x16x32_bf16 v[32:35], a[84:87], a[4:7], v[32:35]\n"
  "ds_read_b64_tr_b16 a[112:113], v196 offset:16384\n"
  "v_mfma_f32_16x16x32_bf16 v[48:51], a[88:91], a[0:3], v[48:51]\n"
  "buffer_load_dwordx4 v241, s[16:19], s53 offen offset:1024 lds\n"
  "v_mfma_f32_16x16x32_bf16 v[48:51], a[92:95], a[4:7], v[48:51]\n"
  "ds_read_b64_tr_b16 a[114:115], v196 offset:18432\n"
  "v_mfma_f32_16x16x32_bf16 v[4:7], a[64:67], a[8:11], v[4:7]\n"
  "ds_read_b64_tr_b16 a[120:121], v197 offset:16384\n"
  "v_mfma_f32_16x16x32_bf16 v[4:7], a[68:71], a[12:15], v[4:7]\n"
  "ds_read_b64_tr_b16 a[122:123], v197 offset:18432\n"
  "v_mfma_f32_16x16x32_bf16 v[20:23], a[72:75], a[8:11], v[20:23]\n"
  "buffer_load_dwordx4 v242, s[16:19], s53 offen offset:2048 lds\n"
  "v_mfma_f32_16x16x32_bf16 v[20:23], a[76:79], a[12:15], v[20:23]\n"
  "v_mfma_f32_16x16x32_bf16 v[36:39], a[80:83], a[8:11], v[36:39]\n"
  "v_mfma_f32_16x16x32_bf16 v[36:39], a[84:87], a[12:15], v[36:39]\n"
  "s_waitcnt vmcnt(11)\n"
  "ds_read_b64_tr_b16 a[100:101], v194 offset:20480\n"
  "v_mfma_f32_16x16x32_bf16 v[52:55], a[88:91], a[8:11], v[52:55]\n"
  "buffer_load_dwordx4 v243, s[16:19], s53 offen offset:3072 lds\n"
  "v_mfma_f32_16x16x32_bf16 v[52:55], a[92:95], a[12:15], v[52:55]\n"
  "ds_read_b64_tr_b16 a[102:103], v194 offset:22528\n"
  "v_mfma_f32_16x16x32_bf16 v[8:11], a[64:67], a[16:19], v[8:11]\n"
  "s_add_u32 m0, s51, 12288\n"
  "ds_read_b64_tr_b16 a[108:109], v195 offset:20480\n"
  "v_mfma_f32_16x16x32_bf16 v[8:11], a[68:71], a[20:23], v[8:11]\n"
  "s_xor_b32 s54, s62, 1310720\n"
  "ds_read_b64_tr_b16 a[110:111], v195 offset:22528\n"
  "v_mfma_f32_16x16x32_bf16 v[24:27], a[72:75], a[16:19], v[24:27]\n"
  "buffer_load_dwordx4 v240, s[16:19], s54 offen offset:0 lds\n"
  "v_mfma_f32_16x16x32_bf16 v[24:27], a[76:79], a[20:23], v[24:27]\n"
  "ds_read_b64_tr_b16 a[116:117], v196 offset:20480\n"
  "v_mfma_f32_16x16x32_bf16 v[40:43], a[80:83], a[16:19], v[40:43]\n"
  "ds_read_b64_tr_b16 a[118:119], v196 offset:22528\n"
  "v_mfma_f32_16x16x32_bf16 v[40:43], a[84:87], a[20:23], v[40:43]\n"
  "ds_read_b64_tr_b16 a[124:125], v197 offset:20480\n"
  "v_mfma_f32_16x16x32_bf16 v[56:59], a[88:91], a[16:19], v[56:59]\n"
  "buffer_load_dwordx4 v241, s[16:19], s54 offen offset:1024 lds\n"
  "v_mfma_f32_16x16x32_bf16 v[56:59], a[92:95], a[20:23], v[56:59]\n"
  "ds_read_b64_tr_b16 a[126:127], v197 offset:22528\n"
  "v_mfma_f32_16x16x32_bf16 v[12:15], a[64:67], a[24:27], v[12:15]\n"
  "v_mfma_f32_16x16x32_bf16 v[12:15], a[68:71], a[28:31], v[12:15]\n"
  "v_mfma_f32_16x16x32_bf16 v[28:31], a[72:75], a[24:27], v[28:31]\n"
  "buffer_load_dwordx4 v242, s[16:19], s54 offen offset:2048 lds\n"
  "v_mfma_f32_16x16x32_bf16 v[28:31], a[76:79], a[28:31], v[28:31]\n"
  "v_mfma_f32_16x16x32_bf16 v[44:47], a[80:83], a[24:27], v[44:47]\n"
  "v_mfma_f32_16x16x32_bf16 v[44:47], a[84:87], a[28:31], v[44:47]\n"
  "v_mfma_f32_16x16x32_bf16 v[60:63], a[88:91], a[24:27], v[60:63]\n"
  "buffer_load_dwordx4 v243, s[16:19], s54 offen offset:3072 lds\n"
  "v_mfma_f32_16x16x32_bf16 v[60:63], a[92:95], a[28:31], v[60:63]\n"
  "s_waitcnt lgkmcnt(1)\n"
  "v_mfma_f32_16x16x32_bf16 v[64:67], a[96:99], a[0:3], v[64:67]\n"
  "s_waitcnt vmcnt(12)\n"
  "s_add_u32 m0, s51, 16384\n"
  "ds_read_b64_tr_b16 a[64:65], v194 offset:0\n"
  "v_mfma_f32_16x16x32_bf16 v[64:67], a[100:103], a[4:7], v[64:67]\n"
  "s_xor_b32 s53, s62, 1048704\n"
  "ds_read_b64_tr_b16 a[66:67], v194 offset:2048\n"
  "v_mfma_f32_16x16x32_bf16 v[80:83], a[104:107], a[0:3], v[80:83]\n"
  "buffer_load_dwordx4 v240, s[16:19], s53 offen offset:0 lds\n"
  "v_mfma_f32_16x16x32_bf16 v[80:83], a[108:111], a[4:7], v[80:83]\n"
  "ds_read_b64_tr_b16 a[72:73], v195 offset:0\n"
  "v_mfma_f32_16x16x32_bf16 v[96:99], a[112:115], a[0:3], v[96:99]\n"
  "ds_read_b64_tr_b16 a[74:75], v195 offset:2048\n"
  "v_mfma_f32_16x16x32_bf16 v[96:99], a[116:119], a[4:7], v[96:99]\n"
  "ds_read_b64_tr_b16 a[80:81], v196 offset:0\n"
  "v_mfma_f32_16x16x32_bf16 v[112:115], a[120:123], a[0:3], v[112:115]\n"
  "s_waitcnt lgkmcnt(5)\n"
  "buffer_load_dwordx4 v241, s[16:19], s53 offen offset:1024 lds\n"
  "v_mfma_f32_16x16x32_bf16 v[112:115], a[124:127], a[4:7], v[112:115]\n"
  "ds_read_b64_tr_b16 a[82:83], v196 offset:2048\n"
  "v_mfma_f32_16x16x32_bf16 v[68:71], a[96:99], a[8:11], v[68:71]\n"
  "ds_read_b128 a[0:3], v192 offset:16384\n"
  "v_mfma_f32_16x16x32_bf16 v[68:71], a[100:103], a[12:15], v[68:71]\n"
  "ds_read_b128 a[4:7], v192 offset:20480\n"
  "v_mfma_f32_16x16x32_bf16 v[84:87], a[104:107], a[8:11], v[84:87]\n"
  "buffer_load_dwordx4 v242, s[16:19], s53 offen offset:2048 lds\n"
  "v_mfma_f32_16x16x32_bf16 v[84:87], a[108:111], a[12:15], v[84:87]\n"
  "ds_read_b64_tr_b16 a[88:89], v197 offset:0\n"
  "v_mfma_f32_16x16x32_bf16 v[100:103], a[112:115], a[8:11], v[100:103]\n"
  "ds_read_b64_tr_b16 a[90:91], v197 offset:2048\n"
  "v_mfma_f32_16x16x32_bf16 v[100:103], a[116:119], a[12:15], v[100:103]\n"
  "s_waitcnt vmcnt(11)\n"
  "ds_read_b64_tr_b16 a[68:69], v194 offset:4096\n"
  "v_mfma_f32_16x16x32_bf16 v[116:119], a[120:123], a[8:11], v[116:119]\n"
  "buffer_load_dwordx4 v243, s[16:19], s53 offen offset:3072 lds\n"
  "v_mfma_f32_16x16x32_bf16 v[116:119], a[124:127], a[12:15], v[116:119]\n"
  "ds_read_b64_tr_b16 a[70:71], v194 offset:6144\n"
  "v_mfma_f32_16x16x32_bf16 v[72:75], a[96:99], a[16:19], v[72:75]\n"
  "s_add_u32 m0, s51, 20480\n"
  "ds_read_b128 a[8:11], v192 offset:17408\n"
  "v_mfma_f32_16x16x32_bf16 v[72:75], a[100:103], a[20:23], v[72:75]\n"
  "s_xor_b32 s54, s62, 1310848\n"
  "ds_read_b128 a[12:15], v192 offset:21504\n"
  "v_mfma_f32_16x16x32_bf16 v[88:91], a[104:107], a[16:19], v[88:91]\n"
  "buffer_load_dwordx4 v240, s[16:19], s54 offen offset:0 lds\n"
  "v_mfma_f32_16x16x32_bf16 v[88:91], a[108:111], a[20:23], v[88:91]\n"
  "ds_read_b64_tr_b16 a[76:77], v195 offset:4096\n"
  "v_mfma_f32_16x16x32_bf16 v[104:107], a[112:115], a[16:19], v[104:107]\n"
  "ds_read_b64_tr_b16 a[78:79], v195 offset:6144\n"
  "v_mfma_f32_16x16x32_bf16 v[104:107], a[116:119], a[20:23], v[104:107]\n"
  "ds_read_b64_tr_b16 a[84:85], v196 offset:4096\n"
  "v_mfma_f32_16x16x32_bf16 v[120:123], a[120:123], a[16:19], v[120:123]\n"
  "buffer_load_dwordx4 v241, s[16:19], s54 offen offset:1024 lds\n"
  "v_mfma_f32_16x16x32_bf16 v[120:123], a[124:127], a[20:23], v[120:123]\n"
  "ds_read_b64_tr_b16 a[86:87], v196 offset:6144\n"
  "v_mfma_f32_16x16x32_bf16 v[76:79], a[96:99], a[24:27], v[76:79]\n"
  "ds_read_b64_tr_b16 a[92:93], v197 offset:4096\n"
  "v_mfma_f32_16x16x32_bf16 v[76:79], a[100:103], a[28:31], v[76:79]\n"
  "ds_read_b64_tr_b16 a[94:95], v197 offset:6144\n"
  "v_mfma_f32_16x16x32_bf16 v[92:95], a[104:107], a[24:27], v[92:95]\n"
  "buffer_load_dwordx4 v242, s[16:19], s54 offen offset:2048 lds\n"
  "v_mfma_f32_16x16x32_bf16 v[92:95], a[108:111], a[28:31], v[92:95]\n"
  "v_mfma_f32_16x16x32_bf16 v[108:111], a[112:115], a[24:27], v[108:111]\n"
  "v_mfma_f32_16x16x32_bf16 v[108:111], a[116:119], a[28:31], v[108:111]\n"
  "v_mfma_f32_16x16x32_bf16 v[124:127], a[120:123], a[24:27], v[124:127]\n"
  "buffer_load_dwordx4 v243, s[16:19], s54 offen offset:3072 lds\n"
  "v_mfma_f32_16x16x32_bf16 v[124:127], a[124:127], a[28:31], v[124:127]\n"
  "s_waitcnt lgkmcnt(3)\n"
  "v_mfma_f32_16x16x32_bf16 v[128:131], a[64:67], a[0:3], v[128:131]\n"
  "s_add_u32 m0, s51, 0\n"
  "ds_read_b128 a[16:19], v215 offset:16384\n"
  "v_mfma_f32_16x16x32_bf16 v[128:131], a[68:71], a[4:7], v[128:131]\n"
  "s_mov_b32 s53, 688128\n"
  "ds_read_b128 a[20:23], v215 offset:20480\n"
  "v_mfma_f32_16x16x32_bf16 v[144:147], a[72:75], a[0:3], v[144:147]\n"
  "buffer_load_dwordx4 v244, s[20:23], s53 offen offset:0 lds\n"
  "v_mfma_f32_16x16x32_bf16 v[144:147], a[76:79], a[4:7], v[144:147]\n"
  "ds_read_b128 a[24:27], v215 offset:17408\n"
  "v_bfe_u32 v206, v192, 4, 4\n"
  "v_mfma_f32_16x16x32_bf16 v[160:163], a[80:83], a[0:3], v[160:163]\n"
  "s_waitcnt lgkmcnt(3)\n"
  "ds_read_b128 a[28:31], v215 offset:21504\n"
  "v_lshrrev_b32 v209, 8, v192\n"
  "v_mfma_f32_16x16x32_bf16 v[160:163], a[84:87], a[4:7], v[160:163]\n"
  "s_waitcnt vmcnt(13)\n"
  "ds_read_b64_tr_b16 a[96:97], v194 offset:8192\n"
  "v_lshlrev_b32 v213, 2, v209\n"
  "v_mfma_f32_16x16x32_bf16 v[176:179], a[88:91], a[0:3], v[176:179]\n"
  "buffer_load_dwordx4 v245, s[20:23], s53 offen offset:1024 lds\n"
  "v_mfma_f32_16x16x32_bf16 v[176:179], a[92:95], a[4:7], v[176:179]\n"
  "ds_read_b64_tr_b16 a[98:99], v194 offset:10240\n"
  "v_sub_u32 v213, v206, v213\n"
  "v_mfma_f32_16x16x32_bf16 v[132:135], a[64:67], a[8:11], v[132:135]\n"
  "ds_read_b64_tr_b16 a[104:105], v195 offset:8192\n"
  "v_lshlrev_b32 v212, 2, v206\n"
  "v_mfma_f32_16x16x32_bf16 v[132:135], a[68:71], a[12:15], v[132:135]\n"
  "ds_read_b64_tr_b16 a[106:107], v195 offset:10240\n"
  "v_bfrev_b32 v214, v209\n"
  "v_mfma_f32_16x16x32_bf16 v[148:151], a[72:75], a[8:11], v[148:151]\n"
  "buffer_load_dwordx4 v246, s[20:23], s53 offen offset:2048 lds\n"
  "v_mfma_f32_16x16x32_bf16 v[148:151], a[76:79], a[12:15], v[148:151]\n"
  "ds_read_b64_tr_b16 a[112:113], v196 offset:8192\n"
  "v_lshrrev_b32 v214, 28, v214\n"
  "v_mfma_f32_16x16x32_bf16 v[164:167], a[80:83], a[8:11], v[164:167]\n"
  "ds_read_b64_tr_b16 a[114:115], v196 offset:10240\n"
  "v_lshl_add_u32 v214, v206, 4, v214\n"
  "v_mfma_f32_16x16x32_bf16 v[164:167], a[84:87], a[12:15], v[164:167]\n"
  "ds_read_b64_tr_b16 a[120:121], v197 offset:8192\n"
  "v_lshrrev_b32 v206, 6, v192\n"
  "v_mfma_f32_16x16x32_bf16 v[180:183], a[88:91], a[8:11], v[180:183]\n"
  "buffer_load_dwordx4 v247, s[20:23], s53 offen offset:3072 lds\n"
  "v_mfma_f32_16x16x32_bf16 v[180:183], a[92:95], a[12:15], v[180:183]\n"
  "s_waitcnt lgkmcnt(6)\n"
  "ds_read_b64_tr_b16 a[122:123], v197 offset:10240\n"
  "v_and_b32 v209, 48, v192\n"
  "v_mfma_f32_16x16x32_bf16 v[136:139], a[64:67], a[16:19], v[136:139]\n"
  "s_waitcnt vmcnt(12)\n"
  "s_add_u32 m0, s51, 4096\n"
  "ds_read_b64_tr_b16 a[100:101], v194 offset:12288\n"
  "v_mfma_f32_16x16x32_bf16 v[136:139], a[68:71], a[20:23], v[136:139]\n"
  "L_dry0_1:\n"
  "L_pad1:\n"
  "s_cbranch_execz L_dryr0\n"
  "s_mov_b32 s54, 802816\n"
  "ds_read_b64_tr_b16 a[102:103], v194 offset:14336\n"
  "v_mfma_f32_16x16x32_bf16 v[152:155], a[72:75], a[16:19], v[152:155]\n"
  "buffer_load_dwordx4 v244, s[20:23], s54 offen offset:0 lds\n"
  "v_mfma_f32_16x16x32_bf16 v[152:155], a[76:79], a[20:23], v[152:155]\n"
  "ds_read_b64_tr_b16 a[108:109], v195 offset:12288\n"
  "v_bfrev_b32 v204, v209\n"
  "v_mfma_f32_16x16x32_bf16 v[168:171], a[80:83], a[16:19], v[168:171]\n"
  "ds_read_b64_tr_b16 a[110:111], v195 offset:14336\n"
  "v_lshrrev_b32 v204, 20, v204\n"
  "v_mfma_f32_16x16x32_bf16 v[168:171], a[84:87], a[20:23], v[168:171]\n"
  "ds_read_b64_tr_b16 a[116:117], v196 offset:12288\n"
  "v_lshl_add_u32 v204, v206, 2, v204\n"
  "v_mfma_f32_16x16x32_bf16 v[184:187], a[88:91], a[16:19], v[184:187]\n"
  "buffer_load_dwordx4 v245, s[20:23], s54 offen offset:1024 lds\n"
  "v_mfma_f32_16x16x32_bf16 v[184:187], a[92:95], a[20:23], v[184:187]\n"
  "ds_read_b64_tr_b16 a[118:119], v196 offset:14336\n"
  "v_lshl_add_u32 v206, v206, 10, v209\n"
  "v_mfma_f32_16x16x32_bf16 v[140:143], a[64:67], a[24:27], v[140:143]\n"
  "ds_read_b64_tr_b16 a[124:125], v197 offset:12288\n"
  "v_add_u32 v207, 0x4000, v206\n"
  "v_mfma_f32_16x16x32_bf16 v[140:143], a[68:71], a[28:31], v[140:143]\n"
  "ds_read_b64_tr_b16 a[126:127], v197 offset:14336\n"
  "v_bfe_u32 v208, v192, 5, 4\n"
  "v_mfma_f32_16x16x32_bf16 v[156:159], a[72:75], a[24:27], v[156:159]\n"
  "buffer_load_dwordx4 v246, s[20:23], s54 offen offset:2048 lds\n"
  "v_mfma_f32_16x16x32_bf16 v[156:159], a[76:79], a[28:31], v[156:159]\n"
  "v_bfe_u32 v209, v192, 4, 1\n"
  "v_lshlrev_b32 v208, 10, v208\n"
  "v_mfma_f32_16x16x32_bf16 v[172:175], a[80:83], a[24:27], v[172:175]\n"
  "v_lshl_add_u32 v208, v209, 7, v208\n"
  "v_lshl_add_u32 v210, s50, 14, v192\n"
  "v_mfma_f32_16x16x32_bf16 v[172:175], a[84:87], a[28:31], v[172:175]\n"
  "v_add_u32 v210, 65536, v210\n"
  "v_xor_b32 v211, 0x800, v210\n"
  "v_mfma_f32_16x16x32_bf16 v[188:191], a[88:91], a[24:27], v[188:191]\n"
  "buffer_load_dwordx4 v247, s[20:23], s54 offen offset:3072 lds\n"
  "v_mfma_f32_16x16x32_bf16 v[188:191], a[92:95], a[28:31], v[188:191]\n"
  "s_waitcnt lgkmcnt(3)\n"
  "v_lshlrev_b32 v205, 3, v192\n"
  "v_lshl_add_u32 v205, s50, 13, v205\n"
  "v_mfma_f32_16x16x32_bf16 v[0:3], a[96:99], a[0:3], v[0:3]\n"
  "s_waitcnt vmcnt(12)\n"
  "s_add_u32 m0, s51, 8192\n"
  "ds_read_b64_tr_b16 a[64:65], v194 offset:16384\n"
  "v_mfma_f32_16x16x32_bf16 v[0:3], a[100:103], a[4:7], v[0:3]\n"
  "s_xor_b32 s53, s62, 1572864\n"
  "ds_read_b64_tr_b16 a[66:67], v194 offset:18432\n"
  "v_mfma_f32_16x16x32_bf16 v[16:19], a[104:107], a[0:3], v[16:19]\n"
  "buffer_load_dwordx4 v240, s[16:19], s53 offen offset:0 lds\n"
  "v_mfma_f32_16x16x32_bf16 v[16:19], a[108:111], a[4:7], v[16:19]\n"
  "ds_read_b64_tr_b16 a[72:73], v195 offset:16384\n"
  "v_min_u32 v205, L_cend-L_h14, v205\n"
  "v_mfma_f32_16x16x32_bf16 v[32:35], a[112:115], a[0:3], v[32:35]\n"
  "s_waitcnt lgkmcnt(3)\n"
  "ds_read_b64_tr_b16 a[74:75], v195 offset:18432\n"
  "v_mfma_f32_16x16x32_bf16 v[32:35], a[116:119], a[4:7], v[32:35]\n"
  "ds_read_b64_tr_b16 a[80:81], v196 offset:16384\n"
  "v_mfma_f32_16x16x32_bf16 v[48:51], a[120:123], a[0:3], v[48:51]\n"
  "buffer_load_dwordx4 v241, s[16:19], s53 offen offset:1024 lds\n"
  "v_mfma_f32_16x16x32_bf16 v[48:51], a[124:127], a[4:7], v[48:51]\n"
  "ds_read_b64_tr_b16 a[82:83], v196 offset:18432\n"
  "v_mfma_f32_16x16x32_bf16 v[4:7], a[96:99], a[8:11], v[4:7]\n"
  "ds_read_b64_tr_b16 a[88:89], v197 offset:16384\n"
  "v_mfma_f32_16x16x32_bf16 v[4:7], a[100:103], a[12:15], v[4:7]\n"
  "ds_read_b64_tr_b16 a[90:91], v197 offset:18432\n"
  "v_mfma_f32_16x16x32_bf16 v[20:23], a[104:107], a[8:11], v[20:23]\n"
  "buffer_load_dwordx4 v242, s[16:19], s53 offen offset:2048 lds\n"
  "v_mfma_f32_16x16x32_bf16 v[20:23], a[108:111], a[12:15], v[20:23]\n"
  "v_mfma_f32_16x16x32_bf16 v[36:39], a[112:115], a[8:11], v[36:39]\n"
  "v_mfma_f32_16x16x32_bf16 v[36:39], a[116:119], a[12:15], v[36:39]\n"
  "s_waitcnt vmcnt(11)\n"
  "ds_read_b64_tr_b16 a[68:69], v194 offset:20480\n"
  "v_mfma_f32_16x16x32_bf16 v[52:55], a[120:123], a[8:11], v[52:55]\n"
  "buffer_load_dwordx4 v243, s[16:19], s53 offen offset:3072 lds\n"
  "v_mfma_f32_16x16x32_bf16 v[52:55], a[124:127], a[12:15], v[52:55]\n"
  "ds_read_b64_tr_b16 a[70:71], v194 offset:22528\n"
  "v_mfma_f32_16x16x32_bf16 v[8:11], a[96:99], a[16:19], v[8:11]\n"
  "s_add_u32 m0, s51, 12288\n"
  "ds_read_b64_tr_b16 a[76:77], v195 offset:20480\n"
  "v_mfma_f32_16x16x32_bf16 v[8:11], a[100:103], a[20:23], v[8:11]\n"
  "s_xor_b32 s54, s62, 1835008\n"
  "ds_read_b64_tr_b16 a[78:79], v195 offset:22528\n"
  "v_mfma_f32_16x16x32_bf16 v[24:27], a[104:107], a[16:19], v[24:27]\n"
  "buffer_load_dwordx4 v240, s[16:19], s54 offen offset:0 lds\n"
  "v_mfma_f32_16x16x32_bf16 v[24:27], a[108:111], a[20:23], v[24:27]\n"
  "ds_read_b64_tr_b16 a[84:85], v196 offset:20480\n"
  "v_mfma_f32_16x16x32_bf16 v[40:43], a[112:115], a[16:19], v[40:43]\n"
  "ds_read_b64_tr_b16 a[86:87], v196 offset:22528\n"
  "v_mfma_f32_16x16x32_bf16 v[40:43], a[116:119], a[20:23], v[40:43]\n"
  "ds_read_b64_tr_b16 a[92:93], v197 offset:20480\n"
  "v_mfma_f32_16x16x32_bf16 v[56:59], a[120:123], a[16:19], v[56:59]\n"
  "buffer_load_dwordx4 v241, s[16:19], s54 offen offset:1024 lds\n"
  "v_mfma_f32_16x16x32_bf16 v[56:59], a[124:127], a[20:23], v[56:59]\n"
  "ds_read_b64_tr_b16 a[94:95], v197 offset:22528\n"
  "v_mfma_f32_16x16x32_bf16 v[12:15], a[96:99], a[24:27], v[12:15]\n"
  "v_mfma_f32_16x16x32_bf16 v[12:15], a[100:103], a[28:31], v[12:15]\n"
  "v_mfma_f32_16x16x32_bf16 v[28:31], a[104:107], a[24:27], v[28:31]\n"
  "buffer_load_dwordx4 v242, s[16:19], s54 offen offset:2048 lds\n"
  "v_mfma_f32_16x16x32_bf16 v[28:31], a[108:111], a[28:31], v[28:31]\n"
  "v_mfma_f32_16x16x32_bf16 v[44:47], a[112:115], a[24:27], v[44:47]\n"
  "v_mfma_f32_16x16x32_bf16 v[44:47], a[116:119], a[28:31], v[44:47]\n"
  "v_mfma_f32_16x16x32_bf16 v[60:63], a[120:123], a[24:27], v[60:63]\n"
  "buffer_load_dwordx4 v243, s[16:19], s54 offen offset:3072 lds\n"
  "v_mfma_f32_16x16x32_bf16 v[60:63], a[124:127], a[28:31], v[60:63]\n"
  "s_waitcnt lgkmcnt(1)\n"
  "v_mfma_f32_16x16x32_bf16 v[64:67], a[64:67], a[0:3], v[64:67]\n"
  "s_waitcnt vmcnt(12)\n"
  "s_add_u32 m0, s51, 16384\n"
  "ds_read_b64_tr_b16 a[96:97], v194 offset:0\n"
  "v_mfma_f32_16x16x32_bf16 v[64:67], a[68:71], a[4:7], v[64:67]\n"
  "s_xor_b32 s53, s62, 1572992\n"
  "ds_read_b64_tr_b16 a[98:99], v194 offset:2048\n"
  "v_mfma_f32_16x16x32_bf16 v[80:83], a[72:75], a[0:3], v[80:83]\n"
  "buffer_load_dwordx4 v240, s[16:19], s53 offen offset:0 lds\n"
  "v_mfma_f32_16x16x32_bf16 v[80:83], a[76:79], a[4:7], v[80:83]\n"
  "ds_read_b64_tr_b16 a[104:105], v195 offset:0\n"
  "v_mfma_f32_16x16x32_bf16 v[96:99], a[80:83], a[0:3], v[96:99]\n"
  "ds_read_b64_tr_b16 a[106:107], v195 offset:2048\n"
  "v_mfma_f32_16x16x32_bf16 v[96:99], a[84:87], a[4:7], v[96:99]\n"
  "ds_read_b64_tr_b16 a[112:113], v196 offset:0\n"
  "v_mfma_f32_16x16x32_bf16 v[112:115], a[88:91], a[0:3], v[112:115]\n"
  "s_waitcnt lgkmcnt(5)\n"
  "buffer_load_dwordx4 v241, s[16:19], s53 offen offset:1024 lds\n"
  "v_mfma_f32_16x16x32_bf16 v[112:115], a[92:95], a[4:7], v[112:115]\n"
  "ds_read_b64_tr_b16 a[114:115], v196 offset:2048\n"
  "v_mfma_f32_16x16x32_bf16 v[68:71], a[64:67], a[8:11], v[68:71]\n"
  "ds_read_b128 a[0:3], v192 offset:24576\n"
  "v_mfma_f32_16x16x32_bf16 v[68:71], a[68:71], a[12:15], v[68:71]\n"
  "ds_read_b128 a[4:7], v192 offset:28672\n"
  "v_mfma_f32_16x16x32_bf16 v[84:87], a[72:75], a[8:11], v[84:87]\n"
  "buffer_load_dwordx4 v242, s[16:19], s53 offen offset:2048 lds\n"
  "v_mfma_f32_16x16x32_bf16 v[84:87], a[76:79], a[12:15], v[84:87]\n"
  "ds_read_b64_tr_b16 a[120:121], v197 offset:0\n"
  "v_mfma_f32_16x16x32_bf16 v[100:103], a[80:83], a[8:11], v[100:103]\n"
  "ds_read_b64_tr_b16 a[122:123], v197 offset:2048\n"
  "v_mfma_f32_16x16x32_bf16 v[100:103], a[84:87], a[12:15], v[100:103]\n"
  "s_waitcnt vmcnt(11)\n"
  "ds_read_b64_tr_b16 a[100:101], v194 offset:4096\n"
  "v_mfma_f32_16x16x32_bf16 v[116:119], a[88:91], a[8:11], v[116:119]\n"
  "buffer_load_dwordx4 v243, s[16:19], s53 offen offset:3072 lds\n"
  "v_mfma_f32_16x16x32_bf16 v[116:119], a[92:95], a[12:15], v[116:119]\n"
  "ds_read_b64_tr_b16 a[102:103], v194 offset:6144\n"
  "v_mfma_f32_16x16x32_bf16 v[72:75], a[64:67], a[16:19], v[72:75]\n"
  "s_add_u32 m0, s51, 20480\n"
  "ds_read_b128 a[8:11], v192 offset:25600\n"
  "v_mfma_f32_16x16x32_bf16 v[72:75], a[68:71], a[20:23], v[72:75]\n"
  "s_xor_b32 s54, s62, 1835136\n"
  "ds_read_b128 a[12:15], v192 offset:29696\n"
  "v_mfma_f32_16x16x32_bf16 v[88:91], a[72:75], a[16:19], v[88:91]\n"
  "buffer_load_dwordx4 v240, s[16:19], s54 offen offset:0 lds\n"
  "v_mfma_f32_16x16x32_bf16 v[88:91], a[76:79], a[20:23], v[88:91]\n"
  "ds_read_b64_tr_b16 a[108:109], v195 offset:4096\n"
  "v_mfma_f32_16x16x32_bf16 v[104:107], a[80:83], a[16:19], v[104:107]\n"
  "ds_read_b64_tr_b16 a[110:111], v195 offset:6144\n"
  "v_mfma_f32_16x16x32_bf16 v[104:107], a[84:87], a[20:23], v[104:107]\n"
  "ds_read_b64_tr_b16 a[116:117], v196 offset:4096\n"
  "v_mfma_f32_16x16x32_bf16 v[120:123], a[88:91], a[16:19], v[120:123]\n"
  "buffer_load_dwordx4 v241, s[16:19], s54 offen offset:1024 lds\n"
  "v_mfma_f32_16x16x32_bf16 v[120:123], a[92:95], a[20:23], v[120:123]\n"
  "ds_read_b64_tr_b16 a[118:119], v196 offset:6144\n"
  "v_mfma_f32_16x16x32_bf16 v[76:79], a[64:67], a[24:27], v[76:79]\n"
  "ds_read_b64_tr_b16 a[124:125], v197 offset:4096\n"
  "v_mfma_f32_16x16x32_bf16 v[76:79], a[68:71], a[28:31], v[76:79]\n"
  "ds_read_b64_tr_b16 a[126:127], v197 offset:6144\n"
  "v_mfma_f32_16x16x32_bf16 v[92:95], a[72:75], a[24:27], v[92:95]\n"
  "buffer_load_dwordx4 v242, s[16:19], s54 offen offset:2048 lds\n"
  "v_mfma_f32_16x16x32_bf16 v[92:95], a[76:79], a[28:31], v[92:95]\n"
  "v_mfma_f32_16x16x32_bf16 v[108:111], a[80:83], a[24:27], v[108:111]\n"
  "v_mfma_f32_16x16x32_bf16 v[108:111], a[84:87], a[28:31], v[108:111]\n"
  "v_mfma_f32_16x16x32_bf16 v[124:127], a[88:91], a[24:27], v[124:127]\n"
  "buffer_load_dwordx4 v243, s[16:19], s54 offen offset:3072 lds\n"
  "v_mfma_f32_16x16x32_bf16 v[124:127], a[92:95], a[28:31], v[124:127]\n"
  "s_waitcnt lgkmcnt(3)\n"
  "v_mfma_f32_16x16x32_bf16 v[128:131], a[96:99], a[0:3], v[128:131]\n"
  "s_add_u32 m0, s51, 0\n"
  "ds_read_b128 a[16:19], v215 offset:24576\n"
  "v_mfma_f32_16x16x32_bf16 v[128:131], a[100:103], a[4:7], v[128:131]\n"
  "s_mov_b32 s53, 917504\n"
  "ds_read_b128 a[20:23], v215 offset:28672\n"
  "v_mfma_f32_16x16x32_bf16 v[144:147], a[104:107], a[0:3], v[144:147]\n"
  "buffer_load_dwordx4 v244, s[20:23], s53 offen offset:0 lds\n"
  "v_mfma_f32_16x16x32_bf16 v[144:147], a[108:111], a[4:7], v[144:147]\n"
  "ds_read_b128 a[24:27], v215 offset:25600\n"
  "v_mfma_f32_16x16x32_bf16 v[160:163], a[112:115], a[0:3], v[160:163]\n"
  "s_waitcnt lgkmcnt(3)\n"
  "ds_read_b128 a[28:31], v215 offset:29696\n"
  "v_mfma_f32_16x16x32_bf16 v[160:163], a[116:119], a[4:7], v[160:163]\n"
  "s_waitcnt vmcnt(13)\n"
  "ds_read_b64_tr_b16 a[64:65], v194 offset:8192\n"
  "v_mfma_f32_16x16x32_bf16 v[176:179], a[120:123], a[0:3], v[176:179]\n"
  "buffer_load_dwordx4 v245, s[20:23], s53 offen offset:1024 lds\n"
  "v_mfma_f32_16x16x32_bf16 v[176:179], a[124:127], a[4:7], v[176:179]\n"
  "ds_read_b64_tr_b16 a[66:67], v194 offset:10240\n"
  "v_mfma_f32_16x16x32_bf16 v[132:135], a[96:99], a[8:11], v[132:135]\n"
  "ds_read_b64_tr_b16 a[72:73], v195 offset:8192\n"
  "v_mfma_f32_16x16x32_bf16 v[132:135], a[100:103], a[12:15], v[132:135]\n"
  "ds_read_b64_tr_b16 a[74:75], v195 offset:10240\n"
  "v_mfma_f32_16x16x32_bf16 v[148:151], a[104:107], a[8:11], v[148:151]\n"
  "buffer_load_dwordx4 v246, s[20:23], s53 offen offset:2048 lds\n"
  "v_mfma_f32_16x16x32_bf16 v[148:151], a[108:111], a[12:15], v[148:151]\n"
  "ds_read_b64_tr_b16 a[80:81], v196 offset:8192\n"
  "v_mfma_f32_16x16x32_bf16 v[164:167], a[112:115], a[8:11], v[164:167]\n"
  "ds_read_b64_tr_b16 a[82:83], v196 offset:10240\n"
  "v_mfma_f32_16x16x32_bf16 v[164:167], a[116:119], a[12:15], v[164:167]\n"
  "ds_read_b64_tr_b16 a[88:89], v197 offset:8192\n"
  "v_mfma_f32_16x16x32_bf16 v[180:183], a[120:123], a[8:11], v[180:183]\n"
  "buffer_load_dwordx4 v247, s[20:23], s53 offen offset:3072 lds\n"
  "v_mfma_f32_16x16x32_bf16 v[180:183], a[124:127], a[12:15], v[180:183]\n"
  "s_waitcnt lgkmcnt(6)\n"
  "ds_read_b64_tr_b16 a[90:91], v197 offset:10240\n"
  "v_mfma_f32_16x16x32_bf16 v[136:139], a[96:99], a[16:19], v[136:139]\n"
  "s_waitcnt vmcnt(12)\n"
  "s_add_u32 m0, s51, 4096\n"
  "ds_read_b64_tr_b16 a[68:69], v194 offset:12288\n"
  "v_mfma_f32_16x16x32_bf16 v[136:139], a[100:103], a[20:23], v[136:139]\n"
  "s_mov_b32 s54, 1032192\n"
  "ds_read_b64_tr_b16 a[70:71], v194 offset:14336\n"
  "v_mfma_f32_16x16x32_bf16 v[152:155], a[104:107], a[16:19], v[152:155]\n"
  "buffer_load_dwordx4 v244, s[20:23], s54 offen offset:0 lds\n"
  "v_mfma_f32_16x16x32_bf16 v[152:155], a[108:111], a[20:23], v[152:155]\n"
  "ds_read_b64_tr_b16 a[76:77], v195 offset:12288\n"
  "v_mfma_f32_16x16x32_bf16 v[168:171], a[112:115], a[16:19], v[168:171]\n"
  "ds_read_b64_tr_b16 a[78:79], v195 offset:14336\n"
  "v_mfma_f32_16x16x32_bf16 v[168:171], a[116:119], a[20:23], v[168:171]\n"
  "ds_read_b64_tr_b16 a[84:85], v196 offset:12288\n"
  "v_mfma_f32_16x16x32_bf16 v[184:187], a[120:123], a[16:19], v[184:187]\n"
  "buffer_load_dwordx4 v245, s[20:23], s54 offen offset:1024 lds\n"
  "v_mfma_f32_16x16x32_bf16 v[184:187], a[124:127], a[20:23], v[184:187]\n"
  "ds_read_b64_tr_b16 a[86:87], v196 offset:14336\n"
  "v_mfma_f32_16x16x32_bf16 v[140:143], a[96:99], a[24:27], v[140:143]\n"
  "ds_read_b64_tr_b16 a[92:93], v197 offset:12288\n"
  "v_mfma_f32_16x16x32_bf16 v[140:143], a[100:103], a[28:31], v[140:143]\n"
  "ds_read_b64_tr_b16 a[94:95], v197 offset:14336\n"
  "v_mfma_f32_16x16x32_bf16 v[156:159], a[104:107], a[24:27], v[156:159]\n"
  "buffer_load_dwordx4 v246, s[20:23], s54 offen offset:2048 lds\n"
  "v_mfma_f32_16x16x32_bf16 v[156:159], a[108:111], a[28:31], v[156:159]\n"
  "v_mfma_f32_16x16x32_bf16 v[172:175], a[112:115], a[24:27], v[172:175]\n"
  "v_mfma_f32_16x16x32_bf16 v[172:175], a[116:119], a[28:31], v[172:175]\n"
  "v_mfma_f32_16x16x32_bf16 v[188:191], a[120:123], a[24:27], v[188:191]\n"
  "buffer_load_dwordx4 v247, s[20:23], s54 offen offset:3072 lds\n"
  "v_mfma_f32_16x16x32_bf16 v[188:191], a[124:127], a[28:31], v[188:191]\n"
  "s_waitcnt lgkmcnt(3)\n"
  "v_mfma_f32_16x16x32_bf16 v[0:3], a[64:67], a[0:3], v[0:3]\n"
  "s_waitcnt vmcnt(12)\n"
  "s_add_u32 m0, s51, 8192\n"
  "ds_read_b64_tr_b16 a[96:97], v194 offset:16384\n"
  "v_mfma_f32_16x16x32_bf16 v[0:3], a[68:71], a[4:7], v[0:3]\n"
  "s_xor_b32 s53, s62, 2097152\n"
  "ds_read_b64_tr_b16 a[98:99], v194 offset:18432\n"
  "v_mfma_f32_16x16x32_bf16 v[16:19], a[72:75], a[0:3], v[16:19]\n"
  "buffer_load_dwordx4 v240, s[16:19], s53 offen offset:0 lds\n"
  "v_mfma_f32_16x16x32_bf16 v[16:19], a[76:79], a[4:7], v[16:19]\n"
  "ds_read_b64_tr_b16 a[104:105], v195 offset:16384\n"
  "v_mfma_f32_16x16x32_bf16 v[32:35], a[80:83], a[0:3], v[32:35]\n"
  "s_waitcnt lgkmcnt(3)\n"
  "ds_read_b64_tr_b16 a[106:107], v195 offset:18432\n"
  "v_mfma_f32_16x16x32_bf16 v[32:35], a[84:87], a[4:7], v[32:35]\n"
  "ds_read_b64_tr_b16 a[112:113], v196 offset:16384\n"
  "v_mfma_f32_16x16x32_bf16 v[48:51], a[88:91], a[0:3], v[48:51]\n"
  "buffer_load_dwordx4 v241, s[16:19], s53 offen offset:1024 lds\n"
  "v_mfma_f32_16x16x32_bf16 v[48:51], a[92:95], a[4:7], v[48:51]\n"
  "ds_read_b64_tr_b16 a[114:115], v196 offset:18432\n"
  "v_mfma_f32_16x16x32_bf16 v[4:7], a[64:67], a[8:11], v[4:7]\n"
  "ds_read_b64_tr_b16 a[120:121], v197 offset:16384\n"
  "v_mfma_f32_16x16x32_bf16 v[4:7], a[68:71], a[12:15], v[4:7]\n"
  "ds_read_b64_tr_b16 a[122:123], v197 offset:18432\n"
  "v_mfma_f32_16x16x32_bf16 v[20:23], a[72:75], a[8:11], v[20:23]\n"
  "buffer_load_dwordx4 v242, s[16:19], s53 offen offset:2048 lds\n"
  "v_mfma_f32_16x16x32_bf16 v[20:23], a[76:79], a[12:15], v[20:23]\n"
  "v_mfma_f32_16x16x32_bf16 v[36:39], a[80:83], a[8:11], v[36:39]\n"
  "v_mfma_f32_16x16x32_bf16 v[36:39], a[84:87], a[12:15], v[36:39]\n"
  "s_waitcnt vmcnt(11)\n"
  "ds_read_b64_tr_b16 a[100:101], v194 offset:20480\n"
  "v_mfma_f32_16x16x32_bf16 v[52:55], a[88:91], a[8:11], v[52:55]\n"
  "buffer_load_dwordx4 v243, s[16:19], s53 offen offset:3072 lds\n"
  "v_mfma_f32_16x16x32_bf16 v[52:55], a[92:95], a[12:15], v[52:55]\n"
  "ds_read_b64_tr_b16 a[102:103], v194 offset:22528\n"
  "v_mfma_f32_16x16x32_bf16 v[8:11], a[64:67], a[16:19], v[8:11]\n"
  "s_add_u32 m0, s51, 12288\n"
  "ds_read_b64_tr_b16 a[108:109], v195 offset:20480\n"
  "v_mfma_f32_16x16x32_bf16 v[8:11], a[68:71], a[20:23], v[8:11]\n"
  "s_xor_b32 s54, s62, 2359296\n"
  "ds_read_b64_tr_b16 a[110:111], v195 offset:22528\n"
  "v_mfma_f32_16x16x32_bf16 v[24:27], a[72:75], a[16:19], v[24:27]\n"
  "buffer_load_dwordx4 v240, s[16:19], s54 offen offset:0 lds\n"
  "v_mfma_f32_16x16x32_bf16 v[24:27], a[76:79], a[20:23], v[24:27]\n"
  "ds_read_b64_tr_b16 a[116:117], v196 offset:20480\n"
  "v_mfma_f32_16x16x32_bf16 v[40:43], a[80:83], a[16:19], v[40:43]\n"
  "ds_read_b64_tr_b16 a[118:119], v196 offset:22528\n"
  "v_mfma_f32_16x16x32_bf16 v[40:43], a[84:87], a[20:23], v[40:43]\n"
  "ds_read_b64_tr_b16 a[124:125], v197 offset:20480\n"
  "v_mfma_f32_16x16x32_bf16 v[56:59], a[88:91], a[16:19], v[56:59]\n"
  "buffer_load_dwordx4 v241, s[16:19], s54 offen offset:1024 lds\n"
  "v_mfma_f32_16x16x32_bf16 v[56:59], a[92:95], a[20:23], v[56:59]\n"
  "ds_read_b64_tr_b16 a[126:127], v197 offset:22528\n"
  "v_mfma_f32_16x16x32_bf16 v[12:15], a[64:67], a[24:27], v[12:15]\n"
  "v_mfma_f32_16x16x32_bf16 v[12:15], a[68:71], a[28:31], v[12:15]\n"
  "v_mfma_f32_16x16x32_bf16 v[28:31], a[72:75], a[24:27], v[28:31]\n"
  "buffer_load_dwordx4 v242, s[16:19], s54 offen offset:2048 lds\n"
  "v_mfma_f32_16x16x32_bf16 v[28:31], a[76:79], a[28:31], v[28:31]\n"
  "v_mfma_f32_16x16x32_bf16 v[44:47], a[80:83], a[24:27], v[44:47]\n"
  "v_mfma_f32_16x16x32_bf16 v[44:47], a[84:87], a[28:31], v[44:47]\n"
  "v_mfma_f32_16x16x32_bf16 v[60:63], a[88:91], a[24:27], v[60:63]\n"
  "buffer_load_dwordx4 v243, s[16:19], s54 offen offset:3072 lds\n"
  "v_mfma_f32_16x16x32_bf16 v[60:63], a[92:95], a[28:31], v[60:63]\n"
  "s_waitcnt lgkmcnt(1)\n"
  "v_mfma_f32_16x16x32_bf16 v[64:67], a[96:99], a[0:3], v[64:67]\n"
  "s_waitcnt vmcnt(12)\n"
  "s_add_u32 m0, s51, 16384\n"
  "ds_read_b64_tr_b16 a[64:65], v194 offset:0\n"
  "v_mfma_f32_16x16x32_bf16 v[64:67], a[100:103], a[4:7], v[64:67]\n"
  "s_xor_b32 s53, s62, 2097280\n"
  "ds_read_b64_tr_b16 a[66:67], v194 offset:2048\n"
  "v_mfma_f32_16x16x32_bf16 v[80:83], a[104:107], a[0:3], v[80:83]\n"
  "buffer_load_dwordx4 v240, s[16:19], s53 offen offset:0 lds\n"
  "v_mfma_f32_16x16x32_bf16 v[80:83], a[108:111], a[4:7], v[80:83]\n"
  "ds_read_b64_tr_b16 a[72:73], v195 offset:0\n"
  "v_mfma_f32_16x16x32_bf16 v[96:99], a[112:115], a[0:3], v[96:99]\n"
  "ds_read_b64_tr_b16 a[74:75], v195 offset:2048\n"
  "v_mfma_f32_16x16x32_bf16 v[96:99], a[116:119], a[4:7], v[96:99]\n"
  "ds_read_b64_tr_b16 a[80:81], v196 offset:0\n"
  "v_mfma_f32_16x16x32_bf16 v[112:115], a[120:123], a[0:3], v[112:115]\n"
  "s_waitcnt lgkmcnt(5)\n"
  "buffer_load_dwordx4 v241, s[16:19], s53 offen offset:1024 lds\n"
  "v_mfma_f32_16x16x32_bf16 v[112:115], a[124:127], a[4:7], v[112:115]\n"
  "ds_read_b64_tr_b16 a[82:83], v196 offset:2048\n"
  "v_mfma_f32_16x16x32_bf16 v[68:71], a[96:99], a[8:11], v[68:71]\n"
  "ds_read_b128 a[0:3], v192 offset:32768\n"
  "v_mfma_f32_16x16x32_bf16 v[68:71], a[100:103], a[12:15], v[68:71]\n"
  "ds_read_b128 a[4:7], v192 offset:36864\n"
  "v_mfma_f32_16x16x32_bf16 v[84:87], a[104:107], a[8:11], v[84:87]\n"
  "buffer_load_dwordx4 v242, s[16:19], s53 offen offset:2048 lds\n"
  "v_mfma_f32_16x16x32_bf16 v[84:87], a[108:111], a[12:15], v[84:87]\n"
  "ds_read_b64_tr_b16 a[88:89], v197 offset:0\n"
  "v_mfma_f32_16x16x32_bf16 v[100:103], a[112:115], a[8:11], v[100:103]\n"
  "ds_read_b64_tr_b16 a[90:91], v197 offset:2048\n"
  "v_mfma_f32_16x16x32_bf16 v[100:103], a[116:119], a[12:15], v[100:103]\n"
  "s_waitcnt vmcnt(11)\n"
  "ds_read_b64_tr_b16 a[68:69], v194 offset:4096\n"
  "v_mfma_f32_16x16x32_bf16 v[116:119], a[120:123], a[8:11], v[116:119]\n"
  "buffer_load_dwordx4 v243, s[16:19], s53 offen offset:3072 lds\n"
  "v_mfma_f32_16x16x32_bf16 v[116:119], a[124:127], a[12:15], v[116:119]\n"
  "ds_read_b64_tr_b16 a[70:71], v194 offset:6144\n"
  "v_mfma_f32_16x16x32_bf16 v[72:75], a[96:99], a[16:19], v[72:75]\n"
  "s_add_u32 m0, s51, 20480\n"
  "ds_read_b128 a[8:11], v192 offset:33792\n"
  "v_mfma_f32_16x16x32_bf16 v[72:75], a[100:103], a[20:23], v[72:75]\n"
  "s_xor_b32 s54, s62, 2359424\n"
  "ds_read_b128 a[12:15], v192 offset:37888\n"
  "v_mfma_f32_16x16x32_bf16 v[88:91], a[104:107], a[16:19], v[88:91]\n"
  "buffer_load_dwordx4 v240, s[16:19], s54 offen offset:0 lds\n"
  "v_mfma_f32_16x16x32_bf16 v[88:91], a[108:111], a[20:23], v[88:91]\n"
  "ds_read_b64_tr_b16 a[76:77], v195 offset:4096\n"
  "v_mfma_f32_16x16x32_bf16 v[104:107], a[112:115], a[16:19], v[104:107]\n"
  "ds_read_b64_tr_b16 a[78:79], v195 offset:6144\n"
  "v_mfma_f32_16x16x32_bf16 v[104:107], a[116:119], a[20:23], v[104:107]\n"
  "ds_read_b64_tr_b16 a[84:85], v196 offset:4096\n"
  "v_mfma_f32_16x16x32_bf16 v[120:123], a[120:123], a[16:19], v[120:123]\n"
  "buffer_load_dwordx4 v241, s[16:19], s54 offen offset:1024 lds\n"
  "v_mfma_f32_16x16x32_bf16 v[120:123], a[124:127], a[20:23], v[120:123]\n"
  "ds_read_b64_tr_b16 a[86:87], v196 offset:6144\n"
  "v_mfma_f32_16x16x32_bf16 v[76:79], a[96:99], a[24:27], v[76:79]\n"
  "ds_read_b64_tr_b16 a[92:93], v197 offset:4096\n"
  "v_mfma_f32_16x16x32_bf16 v[76:79], a[100:103], a[28:31], v[76:79]\n"
  "ds_read_b64_tr_b16 a[94:95], v197 offset:6144\n"
  "v_mfma_f32_16x16x32_bf16 v[92:95], a[104:107], a[24:27], v[92:95]\n"
  "buffer_load_dwordx4 v242, s[16:19], s54 offen offset:2048 lds\n"
  "v_mfma_f32_16x16x32_bf16 v[92:95], a[108:111], a[28:31], v[92:95]\n"
  "v_mfma_f32_16x16x32_bf16 v[108:111], a[112:115], a[24:27], v[108:111]\n"
  "v_mfma_f32_16x16x32_bf16 v[108:111], a[116:119], a[28:31], v[108:111]\n"
  "v_mfma_f32_16x16x32_bf16 v[124:127], a[120:123], a[24:27], v[124:127]\n"
  "buffer_load_dwordx4 v243, s[16:19], s54 offen offset:3072 lds\n"
  "v_mfma_f32_16x16x32_bf16 v[124:127], a[124:127], a[28:31], v[124:127]\n"
  "s_waitcnt lgkmcnt(3)\n"
  "v_mfma_f32_16x16x32_bf16 v[128:131], a[64:67], a[0:3], v[128:131]\n"
  "s_add_u32 m0, s51, 0\n"
  "ds_read_b128 a[16:19], v215 offset:32768\n"
  "v_mfma_f32_16x16x32_bf16 v[128:131], a[68:71], a[4:7], v[128:131]\n"
  "s_mov_b32 s53, 1146880\n"
  "ds_read_b128 a[20:23], v215 offset:36864\n"
  "v_mfma_f32_16x16x32_bf16 v[144:147], a[72:75], a[0:3], v[144:147]\n"
  "buffer_load_dwordx4 v244, s[20:23], s53 offen offset:0 lds\n"
  "v_mfma_f32_16x16x32_bf16 v[144:147], a[76:79], a[4:7], v[144:147]\n"
  "ds_read_b128 a[24:27], v215 offset:33792\n"
  "v_bfe_u32 v253, v192, 4, 4\n"
  "v_mfma_f32_16x16x32_bf16 v[160:163], a[80:83], a[0:3], v[160:163]\n"
  "s_waitcnt lgkmcnt(3)\n"
  "ds_read_b128 a[28:31], v215 offset:37888\n"
  "v_lshrrev_b32 v254, 8, v192\n"
  "v_mfma_f32_16x16x32_bf16 v[160:163], a[84:87], a[4:7], v[160:163]\n"
  "s_waitcnt vmcnt(13)\n"
  "ds_read_b64_tr_b16 a[96:97], v194 offset:8192\n"
  "v_lshlrev_b32 v251, 8, v254\n"
  "v_mfma_f32_16x16x32_bf16 v[176:179], a[88:91], a[0:3], v[176:179]\n"
  "buffer_load_dwordx4 v245, s[20:23], s53 offen offset:1024 lds\n"
  "v_mfma_f32_16x16x32_bf16 v[176:179], a[92:95], a[4:7], v[176:179]\n"
  "ds_read_b64_tr_b16 a[98:99], v194 offset:10240\n"
  "v_add_u32 v251, s63, v251\n"
  "v_mfma_f32_16x16x32_bf16 v[132:135], a[64:67], a[8:11], v[132:135]\n"
  "ds_read_b64_tr_b16 a[104:105], v195 offset:8192\n"
  "v_xor_b32 v249, v253, v254\n"
  "v_mfma_f32_16x16x32_bf16 v[132:135], a[68:71], a[12:15], v[132:135]\n"
  "ds_read_b64_tr_b16 a[106:107], v195 offset:10240\n"
  "v_xor_b32 v228, 0, v249\n"
  "v_mfma_f32_16x16x32_bf16 v[148:151], a[72:75], a[8:11], v[148:151]\n"
  "buffer_load_dwordx4 v246, s[20:23], s53 offen offset:2048 lds\n"
  "v_mfma_f32_16x16x32_bf16 v[148:151], a[76:79], a[12:15], v[148:151]\n"
  "ds_read_b64_tr_b16 a[112:113], v196 offset:8192\n"
  "v_lshl_add_u32 v228, v228, 4, v251\n"
  "v_mfma_f32_16x16x32_bf16 v[164:167], a[80:83], a[8:11], v[164:167]\n"
  "ds_read_b64_tr_b16 a[114:115], v196 offset:10240\n"
  "v_xor_b32 v229, 4, v249\n"
  "v_mfma_f32_16x16x32_bf16 v[164:167], a[84:87], a[12:15], v[164:167]\n"
  "ds_read_b64_tr_b16 a[120:121], v197 offset:8192\n"
  "v_lshl_add_u32 v229, v229, 4, v251\n"
  "v_mfma_f32_16x16x32_bf16 v[180:183], a[88:91], a[8:11], v[180:183]\n"
  "buffer_load_dwordx4 v247, s[20:23], s53 offen offset:3072 lds\n"
  "v_mfma_f32_16x16x32_bf16 v[180:183], a[92:95], a[12:15], v[180:183]\n"
  "s_waitcnt lgkmcnt(6)\n"
  "ds_read_b64_tr_b16 a[122:123], v197 offset:10240\n"
  "v_xor_b32 v230, 8, v249\n"
  "v_mfma_f32_16x16x32_bf16 v[136:139], a[64:67], a[16:19], v[136:139]\n"
  "s_waitcnt vmcnt(12)\n"
  "s_add_u32 m0, s51, 4096\n"
  "ds_read_b64_tr_b16 a[100:101], v194 offset:12288\n"
  "v_mfma_f32_16x16x32_bf16 v[136:139], a[68:71], a[20:23], v[136:139]\n"
  "s_mov_b32 s54, 1261568\n"
  "ds_read_b64_tr_b16 a[102:103], v194 offset:14336\n"
  "v_mfma_f32_16x16x32_bf16 v[152:155], a[72:75], a[16:19], v[152:155]\n"
  "buffer_load_dwordx4 v244, s[20:23], s54 offen offset:0 lds\n"
  "v_mfma_f32_16x16x32_bf16 v[152:155], a[76:79], a[20:23], v[152:155]\n"
  "ds_read_b64_tr_b16 a[108:109], v195 offset:12288\n"
  "v_lshl_add_u32 v230, v230, 4, v251\n"
  "v_mfma_f32_16x16x32_bf16 v[168:171], a[80:83], a[16:19], v[168:171]\n"
  "ds_read_b64_tr_b16 a[110:111], v195 offset:14336\n"
  "v_xor_b32 v231, 12, v249\n"
  "v_mfma_f32_16x16x32_bf16 v[168:171], a[84:87], a[20:23], v[168:171]\n"
  "ds_read_b64_tr_b16 a[116:117], v196 offset:12288\n"
  "v_lshl_add_u32 v231, v231, 4, v251\n"
  "v_mfma_f32_16x16x32_bf16 v[184:187], a[88:91], a[16:19], v[184:187]\n"
  "buffer_load_dwordx4 v245, s[20:23], s54 offen offset:1024 lds\n"
  "v_mfma_f32_16x16x32_bf16 v[184:187], a[92:95], a[20:23], v[184:187]\n"
  "ds_read_b64_tr_b16 a[118:119], v196 offset:14336\n"
  "v_lshlrev_b32 v249, 1, v254\n"
  "v_mfma_f32_16x16x32_bf16 v[140:143], a[64:67], a[24:27], v[140:143]\n"
  "ds_read_b64_tr_b16 a[124:125], v197 offset:12288\n"
  "v_xor_b32 v249, v253, v249\n"
  "v_mfma_f32_16x16x32_bf16 v[140:143], a[68:71], a[28:31], v[140:143]\n"
  "ds_read_b64_tr_b16 a[126:127], v197 offset:14336\n"
  "v_xor_b32 v232, 0, v249\n"
  "v_mfma_f32_16x16x32_bf16 v[156:159], a[72:75], a[24:27], v[156:159]\n"
  "buffer_load_dwordx4 v246, s[20:23], s54 offen offset:2048 lds\n"
  "v_mfma_f32_16x16x32_bf16 v[156:159], a[76:79], a[28:31], v[156:159]\n"
  "v_lshl_add_u32 v232, v232, 4, v251\n"
  "v_xor_b32 v233, 8, v249\n"
  "v_mfma_f32_16x16x32_bf16 v[172:175], a[80:83], a[24:27], v[172:175]\n"
  "v_lshl_add_u32 v233, v233, 4, v251\n"
  "v_xor_b32 v234, 0, v249\n"
  "v_mfma_f32_16x16x32_bf16 v[172:175], a[84:87], a[28:31], v[172:175]\n"
  "v_lshl_add_u32 v234, v234, 4, v251\n"
  "v_xor_b32 v235, 8, v249\n"
  "v_mfma_f32_16x16x32_bf16 v[188:191], a[88:91], a[24:27], v[188:191]\n"
  "buffer_load_dwordx4 v247, s[20:23], s54 offen offset:3072 lds\n"
  "v_mfma_f32_16x16x32_bf16 v[188:191], a[92:95], a[28:31], v[188:191]\n"
  "s_waitcnt lgkmcnt(3)\n"
  "v_lshl_add_u32 v235, v235, 4, v251\n"
  "v_lshrrev_b32 v249, 1, v254\n"
  "v_mfma_f32_16x16x32_bf16 v[0:3], a[96:99], a[0:3], v[0:3]\n"
  "s_waitcnt vmcnt(12)\n"
  "s_add_u32 m0, s51, 8192\n"
  "ds_read_b64_tr_b16 a[64:65], v194 offset:16384\n"
  "v_mfma_f32_16x16x32_bf16 v[0:3], a[100:103], a[4:7], v[0:3]\n"
  "s_xor_b32 s53, s62, 2621440\n"
  "ds_read_b64_tr_b16 a[66:67], v194 offset:18432\n"
  "v_mfma_f32_16x16x32_bf16 v[16:19], a[104:107], a[0:3], v[16:19]\n"
  "buffer_load_dwordx4 v240, s[16:19], s53 offen offset:0 lds\n"
  "v_mfma_f32_16x16x32_bf16 v[16:19], a[108:111], a[4:7], v[16:19]\n"
  "ds_read_b64_tr_b16 a[72:73], v195 offset:16384\n"
  "v_and_b32 v250, 1, v254\n"
  "v_mfma_f32_16x16x32_bf16 v[32:35], a[112:115], a[0:3], v[32:35]\n"
  "s_waitcnt lgkmcnt(3)\n"
  "ds_read_b64_tr_b16 a[74:75], v195 offset:18432\n"
  "v_lshl_add_u32 v249, v250, 1, v249\n"
  "v_mfma_f32_16x16x32_bf16 v[32:35], a[116:119], a[4:7], v[32:35]\n"
  "ds_read_b64_tr_b16 a[80:81], v196 offset:16384\n"
  "v_xor_b32 v249, v249, v253\n"
  "v_mfma_f32_16x16x32_bf16 v[48:51], a[120:123], a[0:3], v[48:51]\n"
  "buffer_load_dwordx4 v241, s[16:19], s53 offen offset:1024 lds\n"
  "v_mfma_f32_16x16x32_bf16 v[48:51], a[124:127], a[4:7], v[48:51]\n"
  "ds_read_b64_tr_b16 a[82:83], v196 offset:18432\n"
  "v_lshlrev_b32 v249, 4, v249\n"
  "v_mfma_f32_16x16x32_bf16 v[4:7], a[96:99], a[8:11], v[4:7]\n"
  "ds_read_b64_tr_b16 a[88:89], v197 offset:16384\n"
  "v_lshl_add_u32 v249, v253, 8, v249\n"
  "v_mfma_f32_16x16x32_bf16 v[4:7], a[100:103], a[12:15], v[4:7]\n"
  "ds_read_b64_tr_b16 a[90:91], v197 offset:18432\n"
  "v_xor_b32 v216, s62, v249\n"
  "v_mfma_f32_16x16x32_bf16 v[20:23], a[104:107], a[8:11], v[20:23]\n"
  "buffer_load_dwordx4 v242, s[16:19], s53 offen offset:2048 lds\n"
  "v_mfma_f32_16x16x32_bf16 v[20:23], a[108:111], a[12:15], v[20:23]\n"
  "v_xor_b32 v217, 64, v216\n"
  "v_xor_b32 v218, 128, v216\n"
  "v_mfma_f32_16x16x32_bf16 v[36:39], a[112:115], a[8:11], v[36:39]\n"
  "v_xor_b32 v219, 192, v216\n"
  "v_lshrrev_b32 v249, 6, v192\n"
  "v_mfma_f32_16x16x32_bf16 v[36:39], a[116:119], a[12:15], v[36:39]\n"
  "s_waitcnt vmcnt(11)\n"
  "ds_read_b64_tr_b16 a[68:69], v194 offset:20480\n"
  "v_bfe_u32 v250, v192, 4, 2\n"
  "v_mfma_f32_16x16x32_bf16 v[52:55], a[120:123], a[8:11], v[52:55]\n"
  "buffer_load_dwordx4 v243, s[16:19], s53 offen offset:3072 lds\n"
  "v_mfma_f32_16x16x32_bf16 v[52:55], a[124:127], a[12:15], v[52:55]\n"
  "ds_read_b64_tr_b16 a[70:71], v194 offset:22528\n"
  "v_lshlrev_b32 v251, 8, v249\n"
  "v_mfma_f32_16x16x32_bf16 v[8:11], a[96:99], a[16:19], v[8:11]\n"
  "s_add_u32 m0, s51, 12288\n"
  "ds_read_b64_tr_b16 a[76:77], v195 offset:20480\n"
  "v_mfma_f32_16x16x32_bf16 v[8:11], a[100:103], a[20:23], v[8:11]\n"
  "s_xor_b32 s54, s62, 2883584\n"
  "ds_read_b64_tr_b16 a[78:79], v195 offset:22528\n"
  "v_mfma_f32_16x16x32_bf16 v[24:27], a[104:107], a[16:19], v[24:27]\n"
  "buffer_load_dwordx4 v240, s[16:19], s54 offen offset:0 lds\n"
  "v_mfma_f32_16x16x32_bf16 v[24:27], a[108:111], a[20:23], v[24:27]\n"
  "ds_read_b64_tr_b16 a[84:85], v196 offset:20480\n"
  "v_lshl_add_u32 v251, v250, 3, v251\n"
  "v_mfma_f32_16x16x32_bf16 v[40:43], a[112:115], a[16:19], v[40:43]\n"
  "ds_read_b64_tr_b16 a[86:87], v196 offset:22528\n"
  "v_and_b32 v252, 7, v249\n"
  "v_mfma_f32_16x16x32_bf16 v[40:43], a[116:119], a[20:23], v[40:43]\n"
  "ds_read_b64_tr_b16 a[92:93], v197 offset:20480\n"
  "v_lshl_add_u32 v220, v252, 5, v251\n"
  "v_mfma_f32_16x16x32_bf16 v[56:59], a[120:123], a[16:19], v[56:59]\n"
  "buffer_load_dwordx4 v241, s[16:19], s54 offen offset:1024 lds\n"
  "v_mfma_f32_16x16x32_bf16 v[56:59], a[124:127], a[20:23], v[56:59]\n"
  "ds_read_b64_tr_b16 a[94:95], v197 offset:22528\n"
  "v_xor_b32 v221, 32, v220\n"
  "v_mfma_f32_16x16x32_bf16 v[12:15], a[96:99], a[24:27], v[12:15]\n"
  "L_dry0_2:\n"
  "L_pad2:\n"
  "s_cbranch_execz L_dryr0\n"
  "v_xor_b32 v222, 64, v220\n"
  "v_xor_b32 v223, 96, v220\n"
  "v_mfma_f32_16x16x32_bf16 v[12:15], a[100:103], a[28:31], v[12:15]\n"
  "v_xor_b32 v224, 128, v220\n"
  "v_xor_b32 v225, 160, v220\n"
  "v_mfma_f32_16x16x32_bf16 v[28:31], a[104:107], a[24:27], v[28:31]\n"
  "buffer_load_dwordx4 v242, s[16:19], s54 offen offset:2048 lds\n"
  "v_mfma_f32_16x16x32_bf16 v[28:31], a[108:111], a[28:31], v[28:31]\n"
  "v_xor_b32 v226, 192, v220\n"
  "v_xor_b32 v227, 224, v220\n"
  "v_mfma_f32_16x16x32_bf16 v[44:47], a[112:115], a[24:27], v[44:47]\n"
  "v_lshrrev_b32 v249, 4, v192\n"
  "v_lshrrev_b32 v250, v249, v193\n"
  "v_mfma_f32_16x16x32_bf16 v[44:47], a[116:119], a[28:31], v[44:47]\n"
  "v_and_b32 v250, 1, v250\n"
  "v_cmp_eq_u32 vcc, 0, v250\n"
  "v_mfma_f32_16x16x32_bf16 v[60:63], a[120:123], a[24:27], v[60:63]\n"
  "buffer_load_dwordx4 v243, s[16:19], s54 offen offset:3072 lds\n"
  "v_mfma_f32_16x16x32_bf16 v[60:63], a[124:127], a[28:31], v[60:63]\n"
  "s_waitcnt lgkmcnt(1)\n"
  "v_lshlrev_b32 v249, 14, v249\n"
  "v_add_lshl_u32 v249, s48, v249, 2\n"
  "v_mfma_f32_16x16x32_bf16 v[64:67], a[64:67], a[0:3], v[64:67]\n"
  "s_waitcnt vmcnt(12)\n"
  "s_add_u32 m0, s51, 16384\n"
  "ds_read_b64_tr_b16 a[96:97], v194 offset:0\n"
  "v_mfma_f32_16x16x32_bf16 v[64:67], a[68:71], a[4:7], v[64:67]\n"
  "s_xor_b32 s53, s62, 2621568\n"
  "ds_read_b64_tr_b16 a[98:99], v194 offset:2048\n"
  "v_mfma_f32_16x16x32_bf16 v[80:83], a[72:75], a[0:3], v[80:83]\n"
  "buffer_load_dwordx4 v240, s[16:19], s53 offen offset:0 lds\n"
  "v_mfma_f32_16x16x32_bf16 v[80:83], a[76:79], a[4:7], v[80:83]\n"
  "ds_read_b64_tr_b16 a[104:105], v195 offset:0\n"
  "v_mov_b32 v250, vcc_lo\n"
  "v_mfma_f32_16x16x32_bf16 v[96:99], a[80:83], a[0:3], v[96:99]\n"
  "s_mov_b32 s65, vcc_lo\n"
  "ds_read_b64_tr_b16 a[106:107], v195 offset:2048\n"
  "v_mfma_f32_16x16x32_bf16 v[96:99], a[84:87], a[4:7], v[96:99]\n"
  "ds_read_b64_tr_b16 a[112:113], v196 offset:0\n"
  "v_mfma_f32_16x16x32_bf16 v[112:115], a[88:91], a[0:3], v[112:115]\n"
  "s_waitcnt lgkmcnt(5)\n"
  "buffer_load_dwordx4 v241, s[16:19], s53 offen offset:1024 lds\n"
  "v_mfma_f32_16x16x32_bf16 v[112:115], a[92:95], a[4:7], v[112:115]\n"
  "ds_read_b64_tr_b16 a[114:115], v196 offset:2048\n"
  "v_mfma_f32_16x16x32_bf16 v[68:71], a[64:67], a[8:11], v[68:71]\n"
  "ds_read_b128 a[0:3], v192 offset:40960\n"
  "v_mfma_f32_16x16x32_bf16 v[68:71], a[68:71], a[12:15], v[68:71]\n"
  "ds_read_b128 a[4:7], v192 offset:45056\n"
  "v_mfma_f32_16x16x32_bf16 v[84:87], a[72:75], a[8:11], v[84:87]\n"
  "buffer_load_dwordx4 v242, s[16:19], s53 offen offset:2048 lds\n"
  "v_mfma_f32_16x16x32_bf16 v[84:87], a[76:79], a[12:15], v[84:87]\n"
  "ds_read_b64_tr_b16 a[120:121], v197 offset:0\n"
  "v_mfma_f32_16x16x32_bf16 v[100:103], a[80:83], a[8:11], v[100:103]\n"
  "ds_read_b64_tr_b16 a[122:123], v197 offset:2048\n"
  "v_mfma_f32_16x16x32_bf16 v[100:103], a[84:87], a[12:15], v[100:103]\n"
  "s_waitcnt vmcnt(11)\n"
  "ds_read_b64_tr_b16 a[100:101], v194 offset:4096\n"
  "v_mfma_f32_16x16x32_bf16 v[116:119], a[88:91], a[8:11], v[116:119]\n"
  "buffer_load_dwordx4 v243, s[16:19], s53 offen offset:3072 lds\n"
  "v_mfma_f32_16x16x32_bf16 v[116:119], a[92:95], a[12:15], v[116:119]\n"
  "ds_read_b64_tr_b16 a[102:103], v194 offset:6144\n"
  "v_mfma_f32_16x16x32_bf16 v[72:75], a[64:67], a[16:19], v[72:75]\n"
  "s_add_u32 m0, s51, 20480\n"
  "ds_read_b128 a[8:11], v192 offset:41984\n"
  "v_mfma_f32_16x16x32_bf16 v[72:75], a[68:71], a[20:23], v[72:75]\n"
  "s_xor_b32 s54, s62, 2883712\n"
  "ds_read_b128 a[12:15], v192 offset:46080\n"
  "v_mfma_f32_16x16x32_bf16 v[88:91], a[72:75], a[16:19], v[88:91]\n"
  "buffer_load_dwordx4 v240, s[16:19], s54 offen offset:0 lds\n"
  "v_mfma_f32_16x16x32_bf16 v[88:91], a[76:79], a[20:23], v[88:91]\n"
  "ds_read_b64_tr_b16 a[108:109], v195 offset:4096\n"
  "v_mfma_f32_16x16x32_bf16 v[104:107], a[80:83], a[16:19], v[104:107]\n"
  "ds_read_b64_tr_b16 a[110:111], v195 offset:6144\n"
  "v_mfma_f32_16x16x32_bf16 v[104:107], a[84:87], a[20:23], v[104:107]\n"
  "ds_read_b64_tr_b16 a[116:117], v196 offset:4096\n"
  "v_mfma_f32_16x16x32_bf16 v[120:123], a[88:91], a[16:19], v[120:123]\n"
  "buffer_load_dwordx4 v241, s[16:19], s54 offen offset:1024 lds\n"
  "v_mfma_f32_16x16x32_bf16 v[120:123], a[92:95], a[20:23], v[120:123]\n"
  "ds_read_b64_tr_b16 a[118:119], v196 offset:6144\n"
  "v_mfma_f32_16x16x32_bf16 v[76:79], a[64:67], a[24:27], v[76:79]\n"
  "ds_read_b64_tr_b16 a[124:125], v197 offset:4096\n"
  "v_mfma_f32_16x16x32_bf16 v[76:79], a[68:71], a[28:31], v[76:79]\n"
  "ds_read_b64_tr_b16 a[126:127], v197 offset:6144\n"
  "v_mfma_f32_16x16x32_bf16 v[92:95], a[72:75], a[24:27], v[92:95]\n"
  "buffer_load_dwordx4 v242, s[16:19], s54 offen offset:2048 lds\n"
  "v_mfma_f32_16x16x32_bf16 v[92:95], a[76:79], a[28:31], v[92:95]\n"
  "v_mfma_f32_16x16x32_bf16 v[108:111], a[80:83], a[24:27], v[108:111]\n"
  "v_mfma_f32_16x16x32_bf16 v[108:111], a[84:87], a[28:31], v[108:111]\n"
  "v_mfma_f32_16x16x32_bf16 v[124:127], a[88:91], a[24:27], v[124:127]\n"
  "buffer_load_dwordx4 v243, s[16:19], s54 offen offset:3072 lds\n"
  "v_mfma_f32_16x16x32_bf16 v[124:127], a[92:95], a[28:31], v[124:127]\n"
  "s_waitcnt lgkmcnt(3)\n"
  "v_mfma_f32_16x16x32_bf16 v[128:131], a[96:99], a[0:3], v[128:131]\n"
  "s_add_u32 m0, s51, 0\n"
  "ds_read_b128 a[16:19], v215 offset:40960\n"
  "v_mfma_f32_16x16x32_bf16 v[128:131], a[100:103], a[4:7], v[128:131]\n"
  "s_mov_b32 s53, 1376256\n"
  "ds_read_b128 a[20:23], v215 offset:45056\n"
  "v_mfma_f32_16x16x32_bf16 v[144:147], a[104:107], a[0:3], v[144:147]\n"
  "buffer_load_dwordx4 v244, s[20:23], s53 offen offset:0 lds\n"
  "v_mfma_f32_16x16x32_bf16 v[144:147], a[108:111], a[4:7], v[144:147]\n"
  "ds_read_b128 a[24:27], v215 offset:41984\n"
  "v_mfma_f32_16x16x32_bf16 v[160:163], a[112:115], a[0:3], v[160:163]\n"
  "s_waitcnt lgkmcnt(3)\n"
  "ds_read_b128 a[28:31], v215 offset:46080\n"
  "v_mfma_f32_16x16x32_bf16 v[160:163], a[116:119], a[4:7], v[160:163]\n"
  "s_waitcnt vmcnt(13)\n"
  "ds_read_b64_tr_b16 a[64:65], v194 offset:8192\n"
  "v_mfma_f32_16x16x32_bf16 v[176:179], a[120:123], a[0:3], v[176:179]\n"
  "buffer_load_dwordx4 v245, s[20:23], s53 offen offset:1024 lds\n"
  "v_mfma_f32_16x16x32_bf16 v[176:179], a[124:127], a[4:7], v[176:179]\n"
  "ds_read_b64_tr_b16 a[66:67], v194 offset:10240\n"
  "v_mfma_f32_16x16x32_bf16 v[132:135], a[96:99], a[8:11], v[132:135]\n"
  "ds_read_b64_tr_b16 a[72:73], v195 offset:8192\n"
  "v_mfma_f32_16x16x32_bf16 v[132:135], a[100:103], a[12:15], v[132:135]\n"
  "ds_read_b64_tr_b16 a[74:75], v195 offset:10240\n"
  "v_mfma_f32_16x16x32_bf16 v[148:151], a[104:107], a[8:11], v[148:151]\n"
  "buffer_load_dwordx4 v246, s[20:23], s53 offen offset:2048 lds\n"
  "v_mfma_f32_16x16x32_bf16 v[148:151], a[108:111], a[12:15], v[148:151]\n"
  "ds_read_b64_tr_b16 a[80:81], v196 offset:8192\n"
  "v_mfma_f32_16x16x32_bf16 v[164:167], a[112:115], a[8:11], v[164:167]\n"
  "ds_read_b64_tr_b16 a[82:83], v196 offset:10240\n"
  "v_mfma_f32_16x16x32_bf16 v[164:167], a[116:119], a[12:15], v[164:167]\n"
  "ds_read_b64_tr_b16 a[88:89], v197 offset:8192\n"
  "v_mfma_f32_16x16x32_bf16 v[180:183], a[120:123], a[8:11], v[180:183]\n"
  "buffer_load_dwordx4 v247, s[20:23], s53 offen offset:3072 lds\n"
  "v_mfma_f32_16x16x32_bf16 v[180:183], a[124:127], a[12:15], v[180:183]\n"
  "s_waitcnt lgkmcnt(6)\n"
  "ds_read_b64_tr_b16 a[90:91], v197 offset:10240\n"
  "v_mfma_f32_16x16x32_bf16 v[136:139], a[96:99], a[16:19], v[136:139]\n"
  "s_waitcnt vmcnt(12)\n"
  "s_add_u32 m0, s51, 4096\n"
  "ds_read_b64_tr_b16 a[68:69], v194 offset:12288\n"
  "v_mfma_f32_16x16x32_bf16 v[136:139], a[100:103], a[20:23], v[136:139]\n"
  "s_mov_b32 s54, 1490944\n"
  "ds_read_b64_tr_b16 a[70:71], v194 offset:14336\n"
  "v_mfma_f32_16x16x32_bf16 v[152:155], a[104:107], a[16:19], v[152:155]\n"
  "buffer_load_dwordx4 v244, s[20:23], s54 offen offset:0 lds\n"
  "v_mfma_f32_16x16x32_bf16 v[152:155], a[108:111], a[20:23], v[152:155]\n"
  "ds_read_b64_tr_b16 a[76:77], v195 offset:12288\n"
  "v_mfma_f32_16x16x32_bf16 v[168:171], a[112:115], a[16:19], v[168:171]\n"
  "ds_read_b64_tr_b16 a[78:79], v195 offset:14336\n"
  "v_mfma_f32_16x16x32_bf16 v[168:171], a[116:119], a[20:23], v[168:171]\n"
  "ds_read_b64_tr_b16 a[84:85], v196 offset:12288\n"
  "v_mfma_f32_16x16x32_bf16 v[184:187], a[120:123], a[16:19], v[184:187]\n"
  "buffer_load_dwordx4 v245, s[20:23], s54 offen offset:1024 lds\n"
  "v_mfma_f32_16x16x32_bf16 v[184:187], a[124:127], a[20:23], v[184:187]\n"
  "ds_read_b64_tr_b16 a[86:87], v196 offset:14336\n"
  "v_mfma_f32_16x16x32_bf16 v[140:143], a[96:99], a[24:27], v[140:143]\n"
  "ds_read_b64_tr_b16 a[92:93], v197 offset:12288\n"
  "v_mfma_f32_16x16x32_bf16 v[140:143], a[100:103], a[28:31], v[140:143]\n"
  "ds_read_b64_tr_b16 a[94:95], v197 offset:14336\n"
  "v_mfma_f32_16x16x32_bf16 v[156:159], a[104:107], a[24:27], v[156:159]\n"
  "buffer_load_dwordx4 v246, s[20:23], s54 offen offset:2048 lds\n"
  "v_mfma_f32_16x16x32_bf16 v[156:159], a[108:111], a[28:31], v[156:159]\n"
  "v_mfma_f32_16x16x32_bf16 v[172:175], a[112:115], a[24:27], v[172:175]\n"
  "v_mfma_f32_16x16x32_bf16 v[172:175], a[116:119], a[28:31], v[172:175]\n"
  "v_mfma_f32_16x16x32_bf16 v[188:191], a[120:123], a[24:27], v[188:191]\n"
  "buffer_load_dwordx4 v247, s[20:23], s54 offen offset:3072 lds\n"
  "v_mfma_f32_16x16x32_bf16 v[188:191], a[124:127], a[28:31], v[188:191]\n"
  "s_waitcnt lgkmcnt(3)\n"
  "v_mfma_f32_16x16x32_bf16 v[0:3], a[64:67], a[0:3], v[0:3]\n"
  "s_waitcnt vmcnt(12)\n"
  "s_add_u32 m0, s51, 8192\n"
  "ds_read_b64_tr_b16 a[96:97], v194 offset:16384\n"
  "v_mfma_f32_16x16x32_bf16 v[0:3], a[68:71], a[4:7], v[0:3]\n"
  "s_xor_b32 s53, s62, 3145728\n"
  "ds_read_b64_tr_b16 a[98:99], v194 offset:18432\n"
  "v_mfma_f32_16x16x32_bf16 v[16:19], a[72:75], a[0:3], v[16:19]\n"
  "buffer_load_dwordx4 v240, s[16:19], s53 offen offset:0 lds\n"
  "v_mfma_f32_16x16x32_bf16 v[16:19], a[76:79], a[4:7], v[16:19]\n"
  "ds_read_b64_tr_b16 a[104:105], v195 offset:16384\n"
  "v_mfma_f32_16x16x32_bf16 v[32:35], a[80:83], a[0:3], v[32:35]\n"
  "s_waitcnt lgkmcnt(3)\n"
  "ds_read_b64_tr_b16 a[106:107], v195 offset:18432\n"
  "v_mfma_f32_16x16x32_bf16 v[32:35], a[84:87], a[4:7], v[32:35]\n"
  "ds_read_b64_tr_b16 a[112:113], v196 offset:16384\n"
  "v_mfma_f32_16x16x32_bf16 v[48:51], a[88:91], a[0:3], v[48:51]\n"
  "buffer_load_dwordx4 v241, s[16:19], s53 offen offset:1024 lds\n"
  "v_mfma_f32_16x16x32_bf16 v[48:51], a[92:95], a[4:7], v[48:51]\n"
  "ds_read_b64_tr_b16 a[114:115], v196 offset:18432\n"
  "v_mfma_f32_16x16x32_bf16 v[4:7], a[64:67], a[8:11], v[4:7]\n"
  "ds_read_b64_tr_b16 a[120:121], v197 offset:16384\n"
  "v_mfma_f32_16x16x32_bf16 v[4:7], a[68:71], a[12:15], v[4:7]\n"
  "ds_read_b64_tr_b16 a[122:123], v197 offset:18432\n"
  "v_mfma_f32_16x16x32_bf16 v[20:23], a[72:75], a[8:11], v[20:23]\n"
  "buffer_load_dwordx4 v242, s[16:19], s53 offen offset:2048 lds\n"
  "v_mfma_f32_16x16x32_bf16 v[20:23], a[76:79], a[12:15], v[20:23]\n"
  "v_mfma_f32_16x16x32_bf16 v[36:39], a[80:83], a[8:11], v[36:39]\n"
  "v_mfma_f32_16x16x32_bf16 v[36:39], a[84:87], a[12:15], v[36:39]\n"
  "s_waitcnt vmcnt(11)\n"
  "ds_read_b64_tr_b16 a[100:101], v194 offset:20480\n"
  "v_mfma_f32_16x16x32_bf16 v[52:55], a[88:91], a[8:11], v[52:55]\n"
  "buffer_load_dwordx4 v243, s[16:19], s53 offen offset:3072 lds\n"
  "v_mfma_f32_16x16x32_bf16 v[52:55], a[92:95], a[12:15], v[52:55]\n"
  "ds_read_b64_tr_b16 a[102:103], v194 offset:22528\n"
  "v_mfma_f32_16x16x32_bf16 v[8:11], a[64:67], a[16:19], v[8:11]\n"
  "s_add_u32 m0, s51, 12288\n"
  "ds_read_b64_tr_b16 a[108:109], v195 offset:20480\n"
  "v_mfma_f32_16x16x32_bf16 v[8:11], a[68:71], a[20:23], v[8:11]\n"
  "s_xor_b32 s54, s62, 3407872\n"
  "ds_read_b64_tr_b16 a[110:111], v195 offset:22528\n"
  "v_mfma_f32_16x16x32_bf16 v[24:27], a[72:75], a[16:19], v[24:27]\n"
  "buffer_load_dwordx4 v240, s[16:19], s54 offen offset:0 lds\n"
  "v_mfma_f32_16x16x32_bf16 v[24:27], a[76:79], a[20:23], v[24:27]\n"
  "ds_read_b64_tr_b16 a[116:117], v196 offset:20480\n"
  "v_mfma_f32_16x16x32_bf16 v[40:43], a[80:83], a[16:19], v[40:43]\n"
  "ds_read_b64_tr_b16 a[118:119], v196 offset:22528\n"
  "v_mfma_f32_16x16x32_bf16 v[40:43], a[84:87], a[20:23], v[40:43]\n"
  "ds_read_b64_tr_b16 a[124:125], v197 offset:20480\n"
  "v_mfma_f32_16x16x32_bf16 v[56:59], a[88:91], a[16:19], v[56:59]\n"
  "buffer_load_dwordx4 v241, s[16:19], s54 offen offset:1024 lds\n"
  "v_mfma_f32_16x16x32_bf16 v[56:59], a[92:95], a[20:23], v[56:59]\n"
  "ds_read_b64_tr_b16 a[126:127], v197 offset:22528\n"
  "v_mfma_f32_16x16x32_bf16 v[12:15], a[64:67], a[24:27], v[12:15]\n"
  "v_mfma_f32_16x16x32_bf16 v[12:15], a[68:71], a[28:31], v[12:15]\n"
  "v_mfma_f32_16x16x32_bf16 v[28:31], a[72:75], a[24:27], v[28:31]\n"
  "buffer_load_dwordx4 v242, s[16:19], s54 offen offset:2048 lds\n"
  "v_mfma_f32_16x16x32_bf16 v[28:31], a[76:79], a[28:31], v[28:31]\n"
  "v_mfma_f32_16x16x32_bf16 v[44:47], a[80:83], a[24:27], v[44:47]\n"
  "v_mfma_f32_16x16x32_bf16 v[44:47], a[84:87], a[28:31], v[44:47]\n"
  "v_mfma_f32_16x16x32_bf16 v[60:63], a[88:91], a[24:27], v[60:63]\n"
  "buffer_load_dwordx4 v243, s[16:19], s54 offen offset:3072 lds\n"
  "v_mfma_f32_16x16x32_bf16 v[60:63], a[92:95], a[28:31], v[60:63]\n"
  "s_waitcnt lgkmcnt(1)\n"
  "v_mfma_f32_16x16x32_bf16 v[64:67], a[96:99], a[0:3], v[64:67]\n"
  "s_waitcnt vmcnt(12)\n"
  "s_add_u32 m0, s51, 16384\n"
  "ds_read_b64_tr_b16 a[64:65], v194 offset:0\n"
  "v_mfma_f32_16x16x32_bf16 v[64:67], a[100:103], a[4:7], v[64:67]\n"
  "s_xor_b32 s53, s62, 3145856\n"
  "ds_read_b64_tr_b16 a[66:67], v194 offset:2048\n"
  "v_mfma_f32_16x16x32_bf16 v[80:83], a[104:107], a[0:3], v[80:83]\n"
  "buffer_load_dwordx4 v240, s[16:19], s53 offen offset:0 lds\n"
  "v_mfma_f32_16x16x32_bf16 v[80:83], a[108:111], a[4:7], v[80:83]\n"
  "ds_read_b64_tr_b16 a[72:73], v195 offset:0\n"
  "v_mfma_f32_16x16x32_bf16 v[96:99], a[112:115], a[0:3], v[96:99]\n"
  "ds_read_b64_tr_b16 a[74:75], v195 offset:2048\n"
  "v_mfma_f32_16x16x32_bf16 v[96:99], a[116:119], a[4:7], v[96:99]\n"
  "ds_read_b64_tr_b16 a[80:81], v196 offset:0\n"
  "v_mfma_f32_16x16x32_bf16 v[112:115], a[120:123], a[0:3], v[112:115]\n"
  "s_waitcnt lgkmcnt(5)\n"
  "buffer_load_dwordx4 v241, s[16:19], s53 offen offset:1024 lds\n"
  "v_mfma_f32_16x16x32_bf16 v[112:115], a[124:127], a[4:7], v[112:115]\n"
  "ds_read_b64_tr_b16 a[82:83], v196 offset:2048\n"
  "v_mfma_f32_16x16x32_bf16 v[68:71], a[96:99], a[8:11], v[68:71]\n"
  "ds_read_b128 a[0:3], v192 offset:49152\n"
  "v_mfma_f32_16x16x32_bf16 v[68:71], a[100:103], a[12:15], v[68:71]\n"
  "ds_read_b128 a[4:7], v192 offset:53248\n"
  "v_mfma_f32_16x16x32_bf16 v[84:87], a[104:107], a[8:11], v[84:87]\n"
  "buffer_load_dwordx4 v242, s[16:19], s53 offen offset:2048 lds\n"
  "v_mfma_f32_16x16x32_bf16 v[84:87], a[108:111], a[12:15], v[84:87]\n"
  "ds_read_b64_tr_b16 a[88:89], v197 offset:0\n"
  "v_mfma_f32_16x16x32_bf16 v[100:103], a[112:115], a[8:11], v[100:103]\n"
  "ds_read_b64_tr_b16 a[90:91], v197 offset:2048\n"
  "v_mfma_f32_16x16x32_bf16 v[100:103], a[116:119], a[12:15], v[100:103]\n"
  "s_waitcnt vmcnt(11)\n"
  "ds_read_b64_tr_b16 a[68:69], v194 offset:4096\n"
  "v_mfma_f32_16x16x32_bf16 v[116:119], a[120:123], a[8:11], v[116:119]\n"
  "buffer_load_dwordx4 v243, s[16:19], s53 offen offset:3072 lds\n"
  "v_mfma_f32_16x16x32_bf16 v[116:119], a[124:127], a[12:15], v[116:119]\n"
  "ds_read_b64_tr_b16 a[70:71], v194 offset:6144\n"
  "v_mfma_f32_16x16x32_bf16 v[72:75], a[96:99], a[16:19], v[72:75]\n"
  "s_add_u32 m0, s51, 20480\n"
  "ds_read_b128 a[8:11], v192 offset:50176\n"
  "v_mfma_f32_16x16x32_bf16 v[72:75], a[100:103], a[20:23], v[72:75]\n"
  "s_xor_b32 s54, s62, 3408000\n"
  "ds_read_b128 a[12:15], v192 offset:54272\n"
  "v_mfma_f32_16x16x32_bf16 v[88:91], a[104:107], a[16:19], v[88:91]\n"
  "buffer_load_dwordx4 v240, s[16:19], s54 offen offset:0 lds\n"
  "v_mfma_f32_16x16x32_bf16 v[88:91], a[108:111], a[20:23], v[88:91]\n"
  "ds_read_b64_tr_b16 a[76:77], v195 offset:4096\n"
  "v_mfma_f32_16x16x32_bf16 v[104:107], a[112:115], a[16:19], v[104:107]\n"
  "ds_read_b64_tr_b16 a[78:79], v195 offset:6144\n"
  "v_mfma_f32_16x16x32_bf16 v[104:107], a[116:119], a[20:23], v[104:107]\n"
  "ds_read_b64_tr_b16 a[84:85], v196 offset:4096\n"
  "v_mfma_f32_16x16x32_bf16 v[120:123], a[120:123], a[16:19], v[120:123]\n"
  "buffer_load_dwordx4 v241, s[16:19], s54 offen offset:1024 lds\n"
  "v_mfma_f32_16x16x32_bf16 v[120:123], a[124:127], a[20:23], v[120:123]\n"
  "ds_read_b64_tr_b16 a[86:87], v196 offset:6144\n"
  "v_mfma_f32_16x16x32_bf16 v[76:79], a[96:99], a[24:27], v[76:79]\n"
  "ds_read_b64_tr_b16 a[92:93], v197 offset:4096\n"
  "v_mfma_f32_16x16x32_bf16 v[76:79], a[100:103], a[28:31], v[76:79]\n"
  "ds_read_b64_tr_b16 a[94:95], v197 offset:6144\n"
  "v_mfma_f32_16x16x32_bf16 v[92:95], a[104:107], a[24:27], v[92:95]\n"
  "buffer_load_dwordx4 v242, s[16:19], s54 offen offset:2048 lds\n"
  "v_mfma_f32_16x16x32_bf16 v[92:95], a[108:111], a[28:31], v[92:95]\n"
  "v_mfma_f32_16x16x32_bf16 v[108:111], a[112:115], a[24:27], v[108:111]\n"
  "v_mfma_f32_16x16x32_bf16 v[108:111], a[116:119], a[28:31], v[108:111]\n"
  "v_mfma_f32_16x16x32_bf16 v[124:127], a[120:123], a[24:27], v[124:127]\n"
  "buffer_load_dwordx4 v243, s[16:19], s54 offen offset:3072 lds\n"
  "v_mfma_f32_16x16x32_bf16 v[124:127], a[124:127], a[28:31], v[124:127]\n"
  "s_waitcnt lgkmcnt(3)\n"
  "v_mfma_f32_16x16x32_bf16 v[128:131], a[64:67], a[0:3], v[128:131]\n"
  "s_add_u32 m0, s51, 0\n"
  "ds_read_b128 a[16:19], v215 offset:49152\n"
  "v_mfma_f32_16x16x32_bf16 v[128:131], a[68:71], a[4:7], v[128:131]\n"
  "s_mov_b32 s53, 1605632\n"
  "ds_read_b128 a[20:23], v215 offset:53248\n"
  "v_mfma_f32_16x16x32_bf16 v[144:147], a[72:75], a[0:3], v[144:147]\n"
  "buffer_load_dwordx4 v244, s[20:23], s53 offen offset:0 lds\n"
  "v_mfma_f32_16x16x32_bf16 v[144:147], a[76:79], a[4:7], v[144:147]\n"
  "ds_read_b128 a[24:27], v215 offset:50176\n"
  "v_mfma_f32_16x16x32_bf16 v[160:163], a[80:83], a[0:3], v[160:163]\n"
  "s_waitcnt lgkmcnt(3)\n"
  "ds_read_b128 a[28:31], v215 offset:54272\n"
  "v_mfma_f32_16x16x32_bf16 v[160:163], a[84:87], a[4:7], v[160:163]\n"
  "s_waitcnt vmcnt(13)\n"
  "ds_read_b64_tr_b16 a[96:97], v194 offset:8192\n"
  "v_mfma_f32_16x16x32_bf16 v[176:179], a[88:91], a[0:3], v[176:179]\n"
  "buffer_load_dwordx4 v245, s[20:23], s53 offen offset:1024 lds\n"
  "v_mfma_f32_16x16x32_bf16 v[176:179], a[92:95], a[4:7], v[176:179]\n"
  "ds_read_b64_tr_b16 a[98:99], v194 offset:10240\n"
  "v_mfma_f32_16x16x32_bf16 v[132:135], a[64:67], a[8:11], v[132:135]\n"
  "ds_read_b64_tr_b16 a[104:105], v195 offset:8192\n"
  "v_mfma_f32_16x16x32_bf16 v[132:135], a[68:71], a[12:15], v[132:135]\n"
  "ds_read_b64_tr_b16 a[106:107], v195 offset:10240\n"
  "v_mfma_f32_16x16x32_bf16 v[148:151], a[72:75], a[8:11], v[148:151]\n"
  "buffer_load_dwordx4 v246, s[20:23], s53 offen offset:2048 lds\n"
  "v_mfma_f32_16x16x32_bf16 v[148:151], a[76:79], a[12:15], v[148:151]\n"
  "ds_read_b64_tr_b16 a[112:113], v196 offset:8192\n"
  "v_mfma_f32_16x16x32_bf16 v[164:167], a[80:83], a[8:11], v[164:167]\n"
  "ds_read_b64_tr_b16 a[114:115], v196 offset:10240\n"
  "v_mfma_f32_16x16x32_bf16 v[164:167], a[84:87], a[12:15], v[164:167]\n"
  "ds_read_b64_tr_b16 a[120:121], v197 offset:8192\n"
  "v_mfma_f32_16x16x32_bf16 v[180:183], a[88:91], a[8:11], v[180:183]\n"
  "buffer_load_dwordx4 v247, s[20:23], s53 offen offset:3072 lds\n"
  "v_mfma_f32_16x16x32_bf16 v[180:183], a[92:95], a[12:15], v[180:183]\n"
  "s_waitcnt lgkmcnt(6)\n"
  "ds_read_b64_tr_b16 a[122:123], v197 offset:10240\n"
  "v_mfma_f32_16x16x32_bf16 v[136:139], a[64:67], a[16:19], v[136:139]\n"
  "s_waitcnt vmcnt(12)\n"
  "s_add_u32 m0, s51, 4096\n"
  "ds_read_b64_tr_b16 a[100:101], v194 offset:12288\n"
  "v_mfma_f32_16x16x32_bf16 v[136:139], a[68:71], a[20:23], v[136:139]\n"
  "s_mov_b32 s54, 1720320\n"
  "ds_read_b64_tr_b16 a[102:103], v194 offset:14336\n"
  "v_mfma_f32_16x16x32_bf16 v[152:155], a[72:75], a[16:19], v[152:155]\n"
  "buffer_load_dwordx4 v244, s[20:23], s54 offen offset:0 lds\n"
  "v_mfma_f32_16x16x32_bf16 v[152:155], a[76:79], a[20:23], v[152:155]\n"
  "ds_read_b64_tr_b16 a[108:109], v195 offset:12288\n"
  "v_mfma_f32_16x16x32_bf16 v[168:171], a[80:83], a[16:19], v[168:171]\n"
  "ds_read_b64_tr_b16 a[110:111], v195 offset:14336\n"
  "v_mfma_f32_16x16x32_bf16 v[168:171], a[84:87], a[20:23], v[168:171]\n"
  "ds_read_b64_tr_b16 a[116:117], v196 offset:12288\n"
  "v_mfma_f32_16x16x32_bf16 v[184:187], a[88:91], a[16:19], v[184:187]\n"
  "buffer_load_dwordx4 v245, s[20:23], s54 offen offset:1024 lds\n"
  "v_mfma_f32_16x16x32_bf16 v[184:187], a[92:95], a[20:23], v[184:187]\n"
  "ds_read_b64_tr_b16 a[118:119], v196 offset:14336\n"
  "v_mfma_f32_16x16x32_bf16 v[140:143], a[64:67], a[24:27], v[140:143]\n"
  "ds_read_b64_tr_b16 a[124:125], v197 offset:12288\n"
  "v_mfma_f32_16x16x32_bf16 v[140:143], a[68:71], a[28:31], v[140:143]\n"
  "ds_read_b64_tr_b16 a[126:127], v197 offset:14336\n"
  "v_mfma_f32_16x16x32_bf16 v[156:159], a[72:75], a[24:27], v[156:159]\n"
  "buffer_load_dwordx4 v246, s[20:23], s54 offen offset:2048 lds\n"
  "v_mfma_f32_16x16x32_bf16 v[156:159], a[76:79], a[28:31], v[156:159]\n"
  "v_mfma_f32_16x16x32_bf16 v[172:175], a[80:83], a[24:27], v[172:175]\n"
  "v_mfma_f32_16x16x32_bf16 v[172:175], a[84:87], a[28:31], v[172:175]\n"
  "v_mfma_f32_16x16x32_bf16 v[188:191], a[88:91], a[24:27], v[188:191]\n"
  "buffer_load_dwordx4 v247, s[20:23], s54 offen offset:3072 lds\n"
  "v_mfma_f32_16x16x32_bf16 v[188:191], a[92:95], a[28:31], v[188:191]\n"
  "s_waitcnt lgkmcnt(3)\n"
  "v_mfma_f32_16x16x32_bf16 v[0:3], a[96:99], a[0:3], v[0:3]\n"
  "s_waitcnt vmcnt(12)\n"
  "s_add_u32 m0, s51, 8192\n"
  "ds_read_b64_tr_b16 a[64:65], v194 offset:16384\n"
  "v_mfma_f32_16x16x32_bf16 v[0:3], a[100:103], a[4:7], v[0:3]\n"
  "s_xor_b32 s53, s62, 3670016\n"
  "ds_read_b64_tr_b16 a[66:67], v194 offset:18432\n"
  "v_mfma_f32_16x16x32_bf16 v[16:19], a[104:107], a[0:3], v[16:19]\n"
  "buffer_load_dwordx4 v240, s[16:19], s53 offen offset:0 lds\n"
  "v_mfma_f32_16x16x32_bf16 v[16:19], a[108:111], a[4:7], v[16:19]\n"
  "ds_read_b64_tr_b16 a[72:73], v195 offset:16384\n"
  "v_accvgpr_write_b32 a240, 0\n"
  "v_mfma_f32_16x16x32_bf16 v[32:35], a[112:115], a[0:3], v[32:35]\n"
  "s_waitcnt lgkmcnt(3)\n"
  "ds_read_b64_tr_b16 a[74:75], v195 offset:18432\n"
  "v_accvgpr_write_b32 a241, 0\n"
  "v_mfma_f32_16x16x32_bf16 v[32:35], a[116:119], a[4:7], v[32:35]\n"
  "ds_read_b64_tr_b16 a[80:81], v196 offset:16384\n"
  "v_accvgpr_write_b32 a242, 0\n"
  "v_mfma_f32_16x16x32_bf16 v[48:51], a[120:123], a[0:3], v[48:51]\n"
  "buffer_load_dwordx4 v241, s[16:19], s53 offen offset:1024 lds\n"
  "v_mfma_f32_16x16x32_bf16 v[48:51], a[124:127], a[4:7], v[48:51]\n"
  "ds_read_b64_tr_b16 a[82:83], v196 offset:18432\n"
  "v_accvgpr_write_b32 a243, 0\n"
  "v_mfma_f32_16x16x32_bf16 v[4:7], a[96:99], a[8:11], v[4:7]\n"
  "ds_read_b64_tr_b16 a[88:89], v197 offset:16384\n"
  "v_accvgpr_write_b32 a244, 0\n"
  "v_mfma_f32_16x16x32_bf16 v[4:7], a[100:103], a[12:15], v[4:7]\n"
  "ds_read_b64_tr_b16 a[90:91], v197 offset:18432\n"
  "v_accvgpr_write_b32 a245, 0\n"
  "v_mfma_f32_16x16x32_bf16 v[20:23], a[104:107], a[8:11], v[20:23]\n"
  "buffer_load_dwordx4 v242, s[16:19], s53 offen offset:2048 lds\n"
  "v_mfma_f32_16x16x32_bf16 v[20:23], a[108:111], a[12:15], v[20:23]\n"
  "v_accvgpr_write_b32 a246, 0\n"
  "v_accvgpr_write_b32 a247, 0\n"
  "v_mfma_f32_16x16x32_bf16 v[36:39], a[112:115], a[8:11], v[36:39]\n"
  "v_accvgpr_write_b32 a248, 0\n"
  "v_accvgpr_write_b32 a249, 0\n"
  "v_mfma_f32_16x16x32_bf16 v[36:39], a[116:119], a[12:15], v[36:39]\n"
  "s_waitcnt vmcnt(11)\n"
  "ds_read_b64_tr_b16 a[68:69], v194 offset:20480\n"
  "v_accvgpr_write_b32 a250, 0\n"
  "v_mfma_f32_16x16x32_bf16 v[52:55], a[120:123], a[8:11], v[52:55]\n"
  "buffer_load_dwordx4 v243, s[16:19], s53 offen offset:3072 lds\n"
  "v_mfma_f32_16x16x32_bf16 v[52:55], a[124:127], a[12:15], v[52:55]\n"
  "ds_read_b64_tr_b16 a[70:71], v194 offset:22528\n"
  "v_accvgpr_write_b32 a251, 0\n"
  "v_mfma_f32_16x16x32_bf16 v[8:11], a[96:99], a[16:19], v[8:11]\n"
  "s_add_u32 m0, s51, 12288\n"
  "ds_read_b64_tr_b16 a[76:77], v195 offset:20480\n"
  "v_mfma_f32_16x16x32_bf16 v[8:11], a[100:103], a[20:23], v[8:11]\n"
  "s_xor_b32 s54, s62, 3932160\n"
  "ds_read_b64_tr_b16 a[78:79], v195 offset:22528\n"
  "v_mfma_f32_16x16x32_bf16 v[24:27], a[104:107], a[16:19], v[24:27]\n"
  "buffer_load_dwordx4 v240, s[16:19], s54 offen offset:0 lds\n"
  "v_mfma_f32_16x16x32_bf16 v[24:27], a[108:111], a[20:23], v[24:27]\n"
  "ds_read_b64_tr_b16 a[84:85], v196 offset:20480\n"
  "v_accvgpr_write_b32 a252, 0\n"
  "v_mfma_f32_16x16x32_bf16 v[40:43], a[112:115], a[16:19], v[40:43]\n"
  "ds_read_b64_tr_b16 a[86:87], v196 offset:22528\n"
  "v_accvgpr_write_b32 a253, 0\n"
  "v_mfma_f32_16x16x32_bf16 v[40:43], a[116:119], a[20:23], v[40:43]\n"
  "ds_read_b64_tr_b16 a[92:93], v197 offset:20480\n"
  "v_accvgpr_write_b32 a254, 0\n"
  "v_mfma_f32_16x16x32_bf16 v[56:59], a[120:123], a[16:19], v[56:59]\n"
  "buffer_load_dwordx4 v241, s[16:19], s54 offen offset:1024 lds\n"
  "v_mfma_f32_16x16x32_bf16 v[56:59], a[124:127], a[20:23], v[56:59]\n"
  "ds_read_b64_tr_b16 a[94:95], v197 offset:22528\n"
  "v_accvgpr_write_b32 a255, 0\n"
  "v_mfma_f32_16x16x32_bf16 v[12:15], a[96:99], a[24:27], v[12:15]\n"
  "v_mfma_f32_16x16x32_bf16 v[12:15], a[100:103], a[28:31], v[12:15]\n"
  "v_mfma_f32_16x16x32_bf16 v[28:31], a[104:107], a[24:27], v[28:31]\n"
  "buffer_load_dwordx4 v242, s[16:19], s54 offen offset:2048 lds\n"
  "v_mfma_f32_16x16x32_bf16 v[28:31], a[108:111], a[28:31], v[28:31]\n"
  "v_mfma_f32_16x16x32_bf16 v[44:47], a[112:115], a[24:27], v[44:47]\n"
  "v_mfma_f32_16x16x32_bf16 v[44:47], a[116:119], a[28:31], v[44:47]\n"
  "v_mfma_f32_16x16x32_bf16 v[60:63], a[120:123], a[24:27], v[60:63]\n"
  "buffer_load_dwordx4 v243, s[16:19], s54 offen offset:3072 lds\n"
  "v_mfma_f32_16x16x32_bf16 v[60:63], a[124:127], a[28:31], v[60:63]\n"
  "s_waitcnt lgkmcnt(1)\n"
  "v_mfma_f32_16x16x32_bf16 v[64:67], a[64:67], a[0:3], v[64:67]\n"
  "s_waitcnt vmcnt(12)\n"
  "s_add_u32 m0, s51, 16384\n"
  "ds_read_b64_tr_b16 a[96:97], v194 offset:0\n"
  "v_mfma_f32_16x16x32_bf16 v[64:67], a[68:71], a[4:7], v[64:67]\n"
  "s_xor_b32 s53, s62, 3670144\n"
  "ds_read_b64_tr_b16 a[98:99], v194 offset:2048\n"
  "v_mfma_f32_16x16x32_bf16 v[80:83], a[72:75], a[0:3], v[80:83]\n"
  "buffer_load_dwordx4 v240, s[16:19], s53 offen offset:0 lds\n"
  "v_mfma_f32_16x16x32_bf16 v[80:83], a[76:79], a[4:7], v[80:83]\n"
  "ds_read_b64_tr_b16 a[104:105], v195 offset:0\n"
  "v_mfma_f32_16x16x32_bf16 v[96:99], a[80:83], a[0:3], v[96:99]\n"
  "ds_read_b64_tr_b16 a[106:107], v195 offset:2048\n"
  "v_mfma_f32_16x16x32_bf16 v[96:99], a[84:87], a[4:7], v[96:99]\n"
  "ds_read_b64_tr_b16 a[112:113], v196 offset:0\n"
  "v_mfma_f32_16x16x32_bf16 v[112:115], a[88:91], a[0:3], v[112:115]\n"
  "s_waitcnt lgkmcnt(5)\n"
  "buffer_load_dwordx4 v241, s[16:19], s53 offen offset:1024 lds\n"
  "v_mfma_f32_16x16x32_bf16 v[112:115], a[92:95], a[4:7], v[112:115]\n"
  "ds_read_b64_tr_b16 a[114:115], v196 offset:2048\n"
  "v_mfma_f32_16x16x32_bf16 v[68:71], a[64:67], a[8:11], v[68:71]\n"
  "ds_read_b128 a[0:3], v192 offset:57344\n"
  "v_mfma_f32_16x16x32_bf16 v[68:71], a[68:71], a[12:15], v[68:71]\n"
  "ds_read_b128 a[4:7], v192 offset:61440\n"
  "v_mfma_f32_16x16x32_bf16 v[84:87], a[72:75], a[8:11], v[84:87]\n"
  "buffer_load_dwordx4 v242, s[16:19], s53 offen offset:2048 lds\n"
  "v_mfma_f32_16x16x32_bf16 v[84:87], a[76:79], a[12:15], v[84:87]\n"
  "ds_read_b64_tr_b16 a[120:121], v197 offset:0\n"
  "v_mfma_f32_16x16x32_bf16 v[100:103], a[80:83], a[8:11], v[100:103]\n"
  "ds_read_b64_tr_b16 a[122:123], v197 offset:2048\n"
  "v_mfma_f32_16x16x32_bf16 v[100:103], a[84:87], a[12:15], v[100:103]\n"
  "s_waitcnt vmcnt(11)\n"
  "ds_read_b64_tr_b16 a[100:101], v194 offset:4096\n"
  "v_mfma_f32_16x16x32_bf16 v[116:119], a[88:91], a[8:11], v[116:119]\n"
  "buffer_load_dwordx4 v243, s[16:19], s53 offen offset:3072 lds\n"
  "v_mfma_f32_16x16x32_bf16 v[116:119], a[92:95], a[12:15], v[116:119]\n"
  "ds_read_b64_tr_b16 a[102:103], v194 offset:6144\n"
  "v_mfma_f32_16x16x32_bf16 v[72:75], a[64:67], a[16:19], v[72:75]\n"
  "s_add_u32 m0, s51, 20480\n"
  "ds_read_b128 a[8:11], v192 offset:58368\n"
  "v_mfma_f32_16x16x32_bf16 v[72:75], a[68:71], a[20:23], v[72:75]\n"
  "s_xor_b32 s54, s62, 3932288\n"
  "ds_read_b128 a[12:15], v192 offset:62464\n"
  "v_mfma_f32_16x16x32_bf16 v[88:91], a[72:75], a[16:19], v[88:91]\n"
  "buffer_load_dwordx4 v240, s[16:19], s54 offen offset:0 lds\n"
  "v_mfma_f32_16x16x32_bf16 v[88:91], a[76:79], a[20:23], v[88:91]\n"
  "ds_read_b64_tr_b16 a[108:109], v195 offset:4096\n"
  "v_mfma_f32_16x16x32_bf16 v[104:107], a[80:83], a[16:19], v[104:107]\n"
  "ds_read_b64_tr_b16 a[110:111], v195 offset:6144\n"
  "v_mfma_f32_16x16x32_bf16 v[104:107], a[84:87], a[20:23], v[104:107]\n"
  "ds_read_b64_tr_b16 a[116:117], v196 offset:4096\n"
  "v_mfma_f32_16x16x32_bf16 v[120:123], a[88:91], a[16:19], v[120:123]\n"
  "buffer_load_dwordx4 v241, s[16:19], s54 offen offset:1024 lds\n"
  "v_mfma_f32_16x16x32_bf16 v[120:123], a[92:95], a[20:23], v[120:123]\n"
  "ds_read_b64_tr_b16 a[118:119], v196 offset:6144\n"
  "v_mfma_f32_16x16x32_bf16 v[76:79], a[64:67], a[24:27], v[76:79]\n"
  "ds_read_b64_tr_b16 a[124:125], v197 offset:4096\n"
  "v_mfma_f32_16x16x32_bf16 v[76:79], a[68:71], a[28:31], v[76:79]\n"
  "ds_read_b64_tr_b16 a[126:127], v197 offset:6144\n"
  "v_mfma_f32_16x16x32_bf16 v[92:95], a[72:75], a[24:27], v[92:95]\n"
  "buffer_load_dwordx4 v242, s[16:19], s54 offen offset:2048 lds\n"
  "v_mfma_f32_16x16x32_bf16 v[92:95], a[76:79], a[28:31], v[92:95]\n"
  "v_mfma_f32_16x16x32_bf16 v[108:111], a[80:83], a[24:27], v[108:111]\n"
  "v_mfma_f32_16x16x32_bf16 v[108:111], a[84:87], a[28:31], v[108:111]\n"
  "v_mfma_f32_16x16x32_bf16 v[124:127], a[88:91], a[24:27], v[124:127]\n"
  "buffer_load_dwordx4 v243, s[16:19], s54 offen offset:3072 lds\n"
  "v_mfma_f32_16x16x32_bf16 v[124:127], a[92:95], a[28:31], v[124:127]\n"
  "s_waitcnt lgkmcnt(3)\n"
  "v_mfma_f32_16x16x32_bf16 v[128:131], a[96:99], a[0:3], v[128:131]\n"
  "s_add_u32 m0, s51, 0\n"
  "ds_read_b128 a[16:19], v215 offset:57344\n"
  "v_mfma_f32_16x16x32_bf16 v[128:131], a[100:103], a[4:7], v[128:131]\n"
  "s_add_u32 s53, s13, 0\n"
  "ds_read_b128 a[20:23], v215 offset:61440\n"
  "v_mfma_f32_16x16x32_bf16 v[144:147], a[104:107], a[0:3], v[144:147]\n"
  "buffer_load_dwordx4 v198, s[44:47], s53 offen offset:0 lds\n"
  "v_mfma_f32_16x16x32_bf16 v[144:147], a[108:111], a[4:7], v[144:147]\n"
  "ds_read_b128 a[24:27], v215 offset:58368\n"
  "v_mfma_f32_16x16x32_bf16 v[160:163], a[112:115], a[0:3], v[160:163]\n"
  "s_waitcnt lgkmcnt(3)\n"
  "ds_read_b128 a[28:31], v215 offset:62464\n"
  "v_mfma_f32_16x16x32_bf16 v[160:163], a[116:119], a[4:7], v[160:163]\n"
  "s_waitcnt vmcnt(13)\n"
  "ds_read_b64_tr_b16 a[64:65], v194 offset:8192\n"
  "v_mfma_f32_16x16x32_bf16 v[176:179], a[120:123], a[0:3], v[176:179]\n"
  "buffer_load_dwordx4 v199, s[44:47], s53 offen offset:1024 lds\n"
  "v_mfma_f32_16x16x32_bf16 v[176:179], a[124:127], a[4:7], v[176:179]\n"
  "ds_read_b64_tr_b16 a[66:67], v194 offset:10240\n"
  "v_mfma_f32_16x16x32_bf16 v[132:135], a[96:99], a[8:11], v[132:135]\n"
  "ds_read_b64_tr_b16 a[72:73], v195 offset:8192\n"
  "v_max_f32 v128, 0, v128\n"
  "v_max_f32 v129, 0, v129\n"
  "v_mfma_f32_16x16x32_bf16 v[132:135], a[100:103], a[12:15], v[132:135]\n"
  "L_dry0_3:\n"
  "L_pad3:\n"
  "s_cbranch_execz L_dryr0\n"
  "ds_read_b64_tr_b16 a[74:75], v195 offset:10240\n"
  "v_max_f32 v130, 0, v130\n"
  "v_max_f32 v131, 0, v131\n"
  "v_mfma_f32_16x16x32_bf16 v[148:151], a[104:107], a[8:11], v[148:151]\n"
  "buffer_load_dwordx4 v200, s[44:47], s53 offen offset:2048 lds\n"
  "v_max_f32 v144, 0, v144\n"
  "v_max_f32 v145, 0, v145\n"
  "v_mfma_f32_16x16x32_bf16 v[148:151], a[108:111], a[12:15], v[148:151]\n"
  "ds_read_b64_tr_b16 a[80:81], v196 offset:8192\n"
  "v_max_f32 v146, 0, v146\n"
  "v_max_f32 v147, 0, v147\n"
  "v_mfma_f32_16x16x32_bf16 v[164:167], a[112:115], a[8:11], v[164:167]\n"
  "ds_read_b64_tr_b16 a[82:83], v196 offset:10240\n"
  "v_cvt_pk_bf16_f32 v128, v128, v129\n"
  "v_cvt_pk_bf16_f32 v129, v130, v131\n"
  "s_waitcnt lgkmcnt(6)\n"
  "s_barrier\n"
  "v_mfma_f32_16x16x32_bf16 v[164:167], a[116:119], a[12:15], v[164:167]\n"
  "ds_read_b64_tr_b16 a[88:89], v197 offset:8192\n"
  "v_cvt_pk_bf16_f32 v130, v144, v145\n"
  "v_cvt_pk_bf16_f32 v131, v146, v147\n"
  "v_mfma_f32_16x16x32_bf16 v[180:183], a[120:123], a[8:11], v[180:183]\n"
  "buffer_load_dwordx4 v201, s[44:47], s53 offen offset:3072 lds\n"
  "v_max_f32 v160, 0, v160\n"
  "v_max_f32 v161, 0, v161\n"
  "v_mfma_f32_16x16x32_bf16 v[180:183], a[124:127], a[12:15], v[180:183]\n"
  "ds_read_b64_tr_b16 a[90:91], v197 offset:10240\n"
  "v_max_f32 v162, 0, v162\n"
  "v_max_f32 v163, 0, v163\n"
  "v_mfma_f32_16x16x32_bf16 v[136:139], a[96:99], a[16:19], v[136:139]\n"
  "s_waitcnt vmcnt(12)\n"
  "s_add_u32 m0, s51, 4096\n"
  "ds_read_b64_tr_b16 a[68:69], v194 offset:12288\n"
  "v_max_f32 v176, 0, v176\n"
  "v_max_f32 v177, 0, v177\n"
  "v_mfma_f32_16x16x32_bf16 v[136:139], a[100:103], a[20:23], v[136:139]\n"
  "s_add_u32 s54, s13, 32768\n"
  "ds_read_b64_tr_b16 a[70:71], v194 offset:14336\n"
  "v_max_f32 v178, 0, v178\n"
  "v_max_f32 v179, 0, v179\n"
  "v_mfma_f32_16x16x32_bf16 v[152:155], a[104:107], a[16:19], v[152:155]\n"
  "buffer_load_dwordx4 v198, s[44:47], s54 offen offset:0 lds\n"
  "v_cvt_pk_bf16_f32 v160, v160, v161\n"
  "v_cvt_pk_bf16_f32 v161, v162, v163\n"
  "v_mfma_f32_16x16x32_bf16 v[152:155], a[108:111], a[20:23], v[152:155]\n"
  "ds_read_b64_tr_b16 a[76:77], v195 offset:12288\n"
  "v_cvt_pk_bf16_f32 v162, v176, v177\n"
  "v_cvt_pk_bf16_f32 v163, v178, v179\n"
  "v_mfma_f32_16x16x32_bf16 v[168:171], a[112:115], a[16:19], v[168:171]\n"
  "ds_read_b64_tr_b16 a[78:79], v195 offset:14336\n"
  "v_max_f32 v132, 0, v132\n"
  "v_max_f32 v133, 0, v133\n"
  "v_mfma_f32_16x16x32_bf16 v[168:171], a[116:119], a[20:23], v[168:171]\n"
  "ds_read_b64_tr_b16 a[84:85], v196 offset:12288\n"
  "v_max_f32 v134, 0, v134\n"
  "v_max_f32 v135, 0, v135\n"
  "v_mfma_f32_16x16x32_bf16 v[184:187], a[120:123], a[16:19], v[184:187]\n"
  "buffer_load_dwordx4 v199, s[44:47], s54 offen offset:1024 lds\n"
  "v_max_f32 v148, 0, v148\n"
  "v_max_f32 v149, 0, v149\n"
  "v_mfma_f32_16x16x32_bf16 v[184:187], a[124:127], a[20:23], v[184:187]\n"
  "ds_read_b64_tr_b16 a[86:87], v196 offset:14336\n"
  "v_max_f32 v150, 0, v150\n"
  "v_max_f32 v151, 0, v151\n"
  "v_mfma_f32_16x16x32_bf16 v[140:143], a[96:99], a[24:27], v[140:143]\n"
  "ds_read_b64_tr_b16 a[92:93], v197 offset:12288\n"
  "v_cvt_pk_bf16_f32 v132, v132, v133\n"
  "v_cvt_pk_bf16_f32 v133, v134, v135\n"
  "v_mfma_f32_16x16x32_bf16 v[140:143], a[100:103], a[28:31], v[140:143]\n"
  "ds_read_b64_tr_b16 a[94:95], v197 offset:14336\n"
  "v_cvt_pk_bf16_f32 v134, v148, v149\n"
  "v_cvt_pk_bf16_f32 v135, v150, v151\n"
  "v_mfma_f32_16x16x32_bf16 v[156:159], a[104:107], a[24:27], v[156:159]\n"
  "buffer_load_dwordx4 v200, s[44:47], s54 offen offset:2048 lds\n"
  "v_max_f32 v164, 0, v164\n"
  "v_max_f32 v165, 0, v165\n"
  "v_mfma_f32_16x16x32_bf16 v[156:159], a[108:111], a[28:31], v[156:159]\n"
  "v_max_f32 v166, 0, v166\n"
  "v_max_f32 v167, 0, v167\n"
  "v_mfma_f32_16x16x32_bf16 v[172:175], a[112:115], a[24:27], v[172:175]\n"
  "v_max_f32 v180, 0, v180\n"
  "v_max_f32 v181, 0, v181\n"
  "v_mfma_f32_16x16x32_bf16 v[172:175], a[116:119], a[28:31], v[172:175]\n"
  "v_max_f32 v182, 0, v182\n"
  "v_max_f32 v183, 0, v183\n"
  "v_mfma_f32_16x16x32_bf16 v[188:191], a[120:123], a[24:27], v[188:191]\n"
  "buffer_load_dwordx4 v201, s[44:47], s54 offen offset:3072 lds\n"
  "v_cvt_pk_bf16_f32 v164, v164, v165\n"
  "v_cvt_pk_bf16_f32 v165, v166, v167\n"
  "v_mfma_f32_16x16x32_bf16 v[188:191], a[124:127], a[28:31], v[188:191]\n"
  "v_cvt_pk_bf16_f32 v166, v180, v181\n"
  "v_cvt_pk_bf16_f32 v167, v182, v183\n"
  "s_mov_b64 exec, 0xffff\n"
  "buffer_load_dword a224, v255, s[44:47], 0 offen\n"
  "s_mov_b64 exec, -1\n"
  "s_mov_b64 exec, 0xff\n"
  "buffer_load_dword a224, v236, s[24:27], s61 offen\n"
  "s_mov_b64 exec, -1\n"
  "s_waitcnt lgkmcnt(3)\n"
  "v_mfma_f32_16x16x32_bf16 v[0:3], a[64:67], a[0:3], v[0:3]\n"
  "s_waitcnt vmcnt(14)\n"
  "s_add_u32 m0, s51, 8192\n"
  "ds_read_b64_tr_b16 a[96:97], v194 offset:16384\n"
  "v_max_f32 v136, 0, v136\n"
  "v_max_f32 v137, 0, v137\n"
  "v_mfma_f32_16x16x32_bf16 v[0:3], a[68:71], a[4:7], v[0:3]\n"
  "s_add_u32 s53, s13, 262144\n"
  "ds_read_b64_tr_b16 a[98:99], v194 offset:18432\n"
  "v_max_f32 v138, 0, v138\n"
  "v_max_f32 v139, 0, v139\n"
  "v_mfma_f32_16x16x32_bf16 v[16:19], a[72:75], a[0:3], v[16:19]\n"
  "buffer_load_dwordx4 v198, s[44:47], s53 offen offset:0 lds\n"
  "v_max_f32 v152, 0, v152\n"
  "v_max_f32 v153, 0, v153\n"
  "v_mfma_f32_16x16x32_bf16 v[16:19], a[76:79], a[4:7], v[16:19]\n"
  "ds_read_b64_tr_b16 a[104:105], v195 offset:16384\n"
  "v_max_f32 v154, 0, v154\n"
  "v_max_f32 v155, 0, v155\n"
  "v_mfma_f32_16x16x32_bf16 v[32:35], a[80:83], a[0:3], v[32:35]\n"
  "s_waitcnt lgkmcnt(3)\n"
  "ds_read_b64_tr_b16 a[106:107], v195 offset:18432\n"
  "v_cvt_pk_bf16_f32 v136, v136, v137\n"
  "v_cvt_pk_bf16_f32 v137, v138, v139\n"
  "v_mfma_f32_16x16x32_bf16 v[32:35], a[84:87], a[4:7], v[32:35]\n"
  "ds_read_b64_tr_b16 a[112:113], v196 offset:16384\n"
  "v_cvt_pk_bf16_f32 v138, v152, v153\n"
  "v_cvt_pk_bf16_f32 v139, v154, v155\n"
  "v_mfma_f32_16x16x32_bf16 v[48:51], a[88:91], a[0:3], v[48:51]\n"
  "buffer_load_dwordx4 v199, s[44:47], s53 offen offset:1024 lds\n"
  "v_max_f32 v168, 0, v168\n"
  "v_max_f32 v169, 0, v169\n"
  "v_mfma_f32_16x16x32_bf16 v[48:51], a[92:95], a[4:7], v[48:51]\n"
  "ds_read_b64_tr_b16 a[114:115], v196 offset:18432\n"
  "v_max_f32 v170, 0, v170\n"
  "v_max_f32 v171, 0, v171\n"
  "v_mfma_f32_16x16x32_bf16 v[4:7], a[64:67], a[8:11], v[4:7]\n"
  "ds_read_b64_tr_b16 a[120:121], v197 offset:16384\n"
  "v_max_f32 v184, 0, v184\n"
  "v_max_f32 v185, 0, v185\n"
  "v_mfma_f32_16x16x32_bf16 v[4:7], a[68:71], a[12:15], v[4:7]\n"
  "ds_read_b64_tr_b16 a[122:123], v197 offset:18432\n"
  "v_max_f32 v186, 0, v186\n"
  "v_max_f32 v187, 0, v187\n"
  "v_mfma_f32_16x16x32_bf16 v[20:23], a[72:75], a[8:11], v[20:23]\n"
  "buffer_load_dwordx4 v200, s[44:47], s53 offen offset:2048 lds\n"
  "v_cvt_pk_bf16_f32 v168, v168, v169\n"
  "v_cvt_pk_bf16_f32 v169, v170, v171\n"
  "v_mfma_f32_16x16x32_bf16 v[20:23], a[76:79], a[12:15], v[20:23]\n"
  "v_cvt_pk_bf16_f32 v170, v184, v185\n"
  "v_cvt_pk_bf16_f32 v171, v186, v187\n"
  "v_mfma_f32_16x16x32_bf16 v[36:39], a[80:83], a[8:11], v[36:39]\n"
  "v_max_f32 v140, 0, v140\n"
  "v_max_f32 v141, 0, v141\n"
  "v_mfma_f32_16x16x32_bf16 v[36:39], a[84:87], a[12:15], v[36:39]\n"
  "s_waitcnt vmcnt(13)\n"
  "ds_read_b64_tr_b16 a[100:101], v194 offset:20480\n"
  "v_max_f32 v142, 0, v142\n"
  "v_max_f32 v143, 0, v143\n"
  "v_mfma_f32_16x16x32_bf16 v[52:55], a[88:91], a[8:11], v[52:55]\n"
  "buffer_load_dwordx4 v201, s[44:47], s53 offen offset:3072 lds\n"
  "v_max_f32 v156, 0, v156\n"
  "v_max_f32 v157, 0, v157\n"
  "v_mfma_f32_16x16x32_bf16 v[52:55], a[92:95], a[12:15], v[52:55]\n"
  "ds_read_b64_tr_b16 a[102:103], v194 offset:22528\n"
  "v_max_f32 v158, 0, v158\n"
  "v_max_f32 v159, 0, v159\n"
  "v_mfma_f32_16x16x32_bf16 v[8:11], a[64:67], a[16:19], v[8:11]\n"
  "s_add_u32 m0, s51, 12288\n"
  "ds_read_b64_tr_b16 a[108:109], v195 offset:20480\n"
  "v_cvt_pk_bf16_f32 v140, v140, v141\n"
  "v_cvt_pk_bf16_f32 v141, v142, v143\n"
  "v_mfma_f32_16x16x32_bf16 v[8:11], a[68:71], a[20:23], v[8:11]\n"
  "s_add_u32 s54, s13, 294912\n"
  "ds_read_b64_tr_b16 a[110:111], v195 offset:22528\n"
  "v_cvt_pk_bf16_f32 v142, v156, v157\n"
  "v_cvt_pk_bf16_f32 v143, v158, v159\n"
  "v_mfma_f32_16x16x32_bf16 v[24:27], a[72:75], a[16:19], v[24:27]\n"
  "buffer_load_dwordx4 v198, s[44:47], s54 offen offset:0 lds\n"
  "v_max_f32 v172, 0, v172\n"
  "v_max_f32 v173, 0, v173\n"
  "v_mfma_f32_16x16x32_bf16 v[24:27], a[76:79], a[20:23], v[24:27]\n"
  "ds_read_b64_tr_b16 a[116:117], v196 offset:20480\n"
  "v_max_f32 v174, 0, v174\n"
  "v_max_f32 v175, 0, v175\n"
  "v_mfma_f32_16x16x32_bf16 v[40:43], a[80:83], a[16:19], v[40:43]\n"
  "ds_read_b64_tr_b16 a[118:119], v196 offset:22528\n"
  "v_max_f32 v188, 0, v188\n"
  "v_max_f32 v189, 0, v189\n"
  "v_mfma_f32_16x16x32_bf16 v[40:43], a[84:87], a[20:23], v[40:43]\n"
  "ds_read_b64_tr_b16 a[124:125], v197 offset:20480\n"
  "v_max_f32 v190, 0, v190\n"
  "v_max_f32 v191, 0, v191\n"
  "v_mfma_f32_16x16x32_bf16 v[56:59], a[88:91], a[16:19], v[56:59]\n"
  "buffer_load_dwordx4 v199, s[44:47], s54 offen offset:1024 lds\n"
  "v_cvt_pk_bf16_f32 v172, v172, v173\n"
  "v_cvt_pk_bf16_f32 v173, v174, v175\n"
  "v_mfma_f32_16x16x32_bf16 v[56:59], a[92:95], a[20:23], v[56:59]\n"
  "ds_read_b64_tr_b16 a[126:127], v197 offset:22528\n"
  "v_cvt_pk_bf16_f32 v174, v188, v189\n"
  "v_cvt_pk_bf16_f32 v175, v190, v191\n"
  "v_mfma_f32_16x16x32_bf16 v[12:15], a[64:67], a[24:27], v[12:15]\n"
  "ds_write_b128 v248, v[128:131] offset:32768\n"
  "ds_write_b128 v248, v[160:163] offset:36864\n"
  "ds_write_b128 v248, v[132:135] offset:33792\n"
  "ds_write_b128 v248, v[164:167] offset:37888\n"
  "ds_write_b128 v248, v[136:139] offset:34816\n"
  "ds_write_b128 v248, v[168:171] offset:38912\n"
  "ds_write_b128 v248, v[140:143] offset:35840\n"
  "ds_write_b128 v248, v[172:175] offset:39936\n"
  "v_cvt_pk_bf16_f32 v0, v0, v1\n"
  "v_cvt_pk_bf16_f32 v1, v2, v3\n"
  "v_mfma_f32_16x16x32_bf16 v[12:15], a[68:71], a[28:31], v[12:15]\n"
  "v_cvt_pk_bf16_f32 v2, v16, v17\n"
  "v_cvt_pk_bf16_f32 v3, v18, v19\n"
  "v_mfma_f32_16x16x32_bf16 v[28:31], a[72:75], a[24:27], v[28:31]\n"
  "buffer_load_dwordx4 v200, s[44:47], s54 offen offset:2048 lds\n"
  "v_cvt_pk_bf16_f32 v32, v32, v33\n"
  "v_cvt_pk_bf16_f32 v33, v34, v35\n"
  "v_mfma_f32_16x16x32_bf16 v[28:31], a[76:79], a[28:31], v[28:31]\n"
  "v_cvt_pk_bf16_f32 v34, v48, v49\n"
  "v_cvt_pk_bf16_f32 v35, v50, v51\n"
  "v_mfma_f32_16x16x32_bf16 v[44:47], a[80:83], a[24:27], v[44:47]\n"
  "v_cvt_pk_bf16_f32 v4, v4, v5\n"
  "v_cvt_pk_bf16_f32 v5, v6, v7\n"
  "v_mfma_f32_16x16x32_bf16 v[44:47], a[84:87], a[28:31], v[44:47]\n"
  "v_cvt_pk_bf16_f32 v6, v20, v21\n"
  "v_cvt_pk_bf16_f32 v7, v22, v23\n"
  "v_mfma_f32_16x16x32_bf16 v[60:63], a[88:91], a[24:27], v[60:63]\n"
  "buffer_load_dwordx4 v201, s[44:47], s54 offen offset:3072 lds\n"
  "v_cvt_pk_bf16_f32 v36, v36, v37\n"
  "v_cvt_pk_bf16_f32 v37, v38, v39\n"
  "v_mfma_f32_16x16x32_bf16 v[60:63], a[92:95], a[28:31], v[60:63]\n"
  "s_waitcnt lgkmcnt(9)\n"
  "v_cvt_pk_bf16_f32 v38, v52, v53\n"
  "v_cvt_pk_bf16_f32 v39, v54, v55\n"
  "v_mfma_f32_16x16x32_bf16 v[64:67], a[96:99], a[0:3], v[64:67]\n"
  "s_waitcnt vmcnt(14)\n"
  "s_add_u32 m0, s51, 16384\n"
  "ds_read_b64_tr_b16 a[64:65], v194 offset:0\n"
  "v_cvt_pk_bf16_f32 v8, v8, v9\n"
  "v_cvt_pk_bf16_f32 v9, v10, v11\n"
  "v_mfma_f32_16x16x32_bf16 v[64:67], a[100:103], a[4:7], v[64:67]\n"
  "s_add_u32 s53, s13, 524288\n"
  "ds_read_b64_tr_b16 a[66:67], v194 offset:2048\n"
  "v_cvt_pk_bf16_f32 v10, v24, v25\n"
  "v_cvt_pk_bf16_f32 v11, v26, v27\n"
  "v_mfma_f32_16x16x32_bf16 v[80:83], a[104:107], a[0:3], v[80:83]\n"
  "buffer_load_dwordx4 v198, s[44:47], s53 offen offset:0 lds\n"
  "v_cvt_pk_bf16_f32 v40, v40, v41\n"
  "v_cvt_pk_bf16_f32 v41, v42, v43\n"
  "v_mfma_f32_16x16x32_bf16 v[80:83], a[108:111], a[4:7], v[80:83]\n"
  "ds_read_b64_tr_b16 a[72:73], v195 offset:0\n"
  "v_cvt_pk_bf16_f32 v42, v56, v57\n"
  "v_cvt_pk_bf16_f32 v43, v58, v59\n"
  "v_mfma_f32_16x16x32_bf16 v[96:99], a[112:115], a[0:3], v[96:99]\n"
  "ds_read_b64_tr_b16 a[74:75], v195 offset:2048\n"
  "v_cvt_pk_bf16_f32 v12, v12, v13\n"
  "v_cvt_pk_bf16_f32 v13, v14, v15\n"
  "v_mfma_f32_16x16x32_bf16 v[96:99], a[116:119], a[4:7], v[96:99]\n"
  "ds_read_b64_tr_b16 a[80:81], v196 offset:0\n"
  "v_cvt_pk_bf16_f32 v14, v28, v29\n"
  "v_cvt_pk_bf16_f32 v15, v30, v31\n"
  "v_mfma_f32_16x16x32_bf16 v[112:115], a[120:123], a[0:3], v[112:115]\n"
  "s_waitcnt lgkmcnt(5)\n"
  "buffer_load_dwordx4 v199, s[44:47], s53 offen offset:1024 lds\n"
  "v_cvt_pk_bf16_f32 v44, v44, v45\n"
  "v_cvt_pk_bf16_f32 v45, v46, v47\n"
  "v_mfma_f32_16x16x32_bf16 v[112:115], a[124:127], a[4:7], v[112:115]\n"
  "ds_read_b64_tr_b16 a[82:83], v196 offset:2048\n"
  "v_cvt_pk_bf16_f32 v46, v60, v61\n"
  "v_cvt_pk_bf16_f32 v47, v62, v63\n"
  "v_mfma_f32_16x16x32_bf16 v[68:71], a[96:99], a[8:11], v[68:71]\n"
  "ds_read_b64_tr_b16 a[88:89], v197 offset:0\n"
  "v_cvt_pk_bf16_f32 v64, v64, v65\n"
  "v_mfma_f32_16x16x32_bf16 v[68:71], a[100:103], a[12:15], v[68:71]\n"
  "ds_read_b64_tr_b16 a[90:91], v197 offset:2048\n"
  "v_cvt_pk_bf16_f32 v65, v66, v67\n"
  "v_mfma_f32_16x16x32_bf16 v[84:87], a[104:107], a[8:11], v[84:87]\n"
  "buffer_load_dwordx4 v200, s[44:47], s53 offen offset:2048 lds\n"
  "v_mfma_f32_16x16x32_bf16 v[84:87], a[108:111], a[12:15], v[84:87]\n"
  "v_cvt_pk_bf16_f32 v66, v80, v81\n"
  "v_cvt_pk_bf16_f32 v67, v82, v83\n"
  "v_mfma_f32_16x16x32_bf16 v[100:103], a[112:115], a[8:11], v[100:103]\n"
  "v_cvt_pk_bf16_f32 v96, v96, v97\n"
  "v_cvt_pk_bf16_f32 v97, v98, v99\n"
  "s_barrier\n"
  "v_mfma_f32_16x16x32_bf16 v[100:103], a[116:119], a[12:15], v[100:103]\n"
  "ds_read_b128 a[32:35], v202 offset:32768\n"
  "v_cvt_pk_bf16_f32 v98, v112, v113\n"
  "v_mfma_f32_16x16x32_bf16 v[116:119], a[120:123], a[8:11], v[116:119]\n"
  "buffer_load_dwordx4 v201, s[44:47], s53 offen offset:3072 lds\n"
  "v_mfma_f32_16x16x32_bf16 v[116:119], a[124:127], a[12:15], v[116:119]\n"
  "ds_read_b128 a[36:39], v202 offset:36864\n"
  "v_cvt_pk_bf16_f32 v99, v114, v115\n"
  "v_mfma_f32_16x16x32_bf16 v[72:75], a[96:99], a[16:19], v[72:75]\n"
  "s_add_u32 m0, s51, 20480\n"
  "ds_read_b128 a[40:43], v202 offset:33792\n"
  "v_mfma_f32_16x16x32_bf16 v[72:75], a[100:103], a[20:23], v[72:75]\n"
  "s_add_u32 s54, s13, 557056\n"
  "ds_read_b128 a[44:47], v202 offset:37888\n"
  "v_mfma_f32_16x16x32_bf16 v[88:91], a[104:107], a[16:19], v[88:91]\n"
  "buffer_load_dwordx4 v198, s[44:47], s54 offen offset:0 lds\n"
  "v_mfma_f32_16x16x32_bf16 v[88:91], a[108:111], a[20:23], v[88:91]\n"
  "ds_read_b128 a[48:51], v203 offset:32768\n"
  "v_cvt_pk_bf16_f32 v68, v68, v69\n"
  "v_mfma_f32_16x16x32_bf16 v[104:107], a[112:115], a[16:19], v[104:107]\n"
  "ds_read_b128 a[52:55], v203 offset:36864\n"
  "v_cvt_pk_bf16_f32 v69, v70, v71\n"
  "v_mfma_f32_16x16x32_bf16 v[104:107], a[116:119], a[20:23], v[104:107]\n"
  "s_waitcnt vmcnt(15)\n"
  "ds_read_b64_tr_b16 a[68:69], v194 offset:4096\n"
  "v_cvt_pk_bf16_f32 v70, v84, v85\n"
  "v_mfma_f32_16x16x32_bf16 v[120:123], a[120:123], a[16:19], v[120:123]\n"
  "buffer_load_dwordx4 v199, s[44:47], s54 offen offset:1024 lds\n"
  "v_mfma_f32_16x16x32_bf16 v[120:123], a[124:127], a[20:23], v[120:123]\n"
  "ds_read_b64_tr_b16 a[70:71], v194 offset:6144\n"
  "v_cvt_pk_bf16_f32 v71, v86, v87\n"
  "v_mfma_f32_16x16x32_bf16 v[76:79], a[96:99], a[24:27], v[76:79]\n"
  "ds_read_b64_tr_b16 a[76:77], v195 offset:4096\n"
  "v_cvt_pk_bf16_f32 v100, v100, v101\n"
  "v_mfma_f32_16x16x32_bf16 v[76:79], a[100:103], a[28:31], v[76:79]\n"
  "ds_read_b64_tr_b16 a[78:79], v195 offset:6144\n"
  "v_cvt_pk_bf16_f32 v101, v102, v103\n"
  "v_mfma_f32_16x16x32_bf16 v[92:95], a[104:107], a[24:27], v[92:95]\n"
  "buffer_load_dwordx4 v200, s[44:47], s54 offen offset:2048 lds\n"
  "v_mfma_f32_16x16x32_bf16 v[92:95], a[108:111], a[28:31], v[92:95]\n"
  "ds_read_b64_tr_b16 a[84:85], v196 offset:4096\n"
  "v_cvt_pk_bf16_f32 v102, v116, v117\n"
  "v_mfma_f32_16x16x32_bf16 v[108:111], a[112:115], a[24:27], v[108:111]\n"
  "ds_read_b64_tr_b16 a[86:87], v196 offset:6144\n"
  "v_cvt_pk_bf16_f32 v103, v118, v119\n"
  "v_mfma_f32_16x16x32_bf16 v[108:111], a[116:119], a[28:31], v[108:111]\n"
  "ds_read_b64_tr_b16 a[92:93], v197 offset:4096\n"
  "v_cvt_pk_bf16_f32 v72, v72, v73\n"
  "v_mfma_f32_16x16x32_bf16 v[124:127], a[120:123], a[24:27], v[124:127]\n"
  "buffer_load_dwordx4 v201, s[44:47], s54 offen offset:3072 lds\n"
  "v_mfma_f32_16x16x32_bf16 v[124:127], a[124:127], a[28:31], v[124:127]\n"
  "ds_read_b64_tr_b16 a[94:95], v197 offset:6144\n"
  "v_cvt_pk_bf16_f32 v73, v74, v75\n"
  "s_cmp_eq_u32 s56, 0\n"
  "s_cbranch_scc1 L_even\n"
  "s_waitcnt lgkmcnt(7)\n"
  "v_mfma_f32_16x16x32_bf16 v[16:19], a[64:67], a[32:35], 0\n"
  "s_waitcnt lgkmcnt(6)\n"
  "s_add_u32 m0, s51, 0\n"
  "ds_read_b128 a[56:59], v203 offset:33792\n"
  "v_mfma_f32_16x16x32_bf16 v[16:19], a[68:71], a[36:39], v[16:19]\n"
  "s_add_u32 s53, s55, 524288\n"
  "ds_read_b128 a[60:63], v203 offset:37888\n"
  "v_mfma_f32_16x16x32_bf16 v[128:131], a[72:75], a[32:35], 0\n"
  "s_waitcnt lgkmcnt(6)\n"
  "buffer_load_dwordx4 v198, s[44:47], s53 offen offset:0 lds\n"
  "v_mfma_f32_16x16x32_bf16 v[128:131], a[76:79], a[36:39], v[128:131]\n"
  "ds_read_b128 a[160:163], v202 offset:40960\n"
  "v_cvt_pk_bf16_f32 v74, v88, v89\n"
  "v_mfma_f32_16x16x32_bf16 v[48:51], a[80:83], a[32:35], 0\n"
  "s_waitcnt lgkmcnt(5)\n"
  "ds_read_b128 a[164:167], v202 offset:45056\n"
  "v_cvt_pk_bf16_f32 v75, v90, v91\n"
  "v_mfma_f32_16x16x32_bf16 v[48:51], a[84:87], a[36:39], v[48:51]\n"
  "s_waitcnt vmcnt(13)\n"
  "ds_read_b64_tr_b16 a[96:97], v194 offset:8192\n"
  "v_cvt_pk_bf16_f32 v104, v104, v105\n"
  "v_mfma_f32_16x16x32_bf16 v[144:147], a[88:91], a[32:35], 0\n"
  "s_waitcnt lgkmcnt(5)\n"
  "buffer_load_dwordx4 v199, s[44:47], s53 offen offset:1024 lds\n"
  "v_mfma_f32_16x16x32_bf16 v[144:147], a[92:95], a[36:39], v[144:147]\n"
  "ds_read_b128 a[168:171], v202 offset:41984\n"
  "v_cvt_pk_bf16_f32 v105, v106, v107\n"
  "v_mfma_f32_16x16x32_bf16 v[20:23], a[64:67], a[40:43], 0\n"
  "ds_read_b128 a[172:175], v202 offset:46080\n"
  "v_cvt_pk_bf16_f32 v106, v120, v121\n"
  "v_mfma_f32_16x16x32_bf16 v[20:23], a[68:71], a[44:47], v[20:23]\n"
  "ds_read_b64_tr_b16 a[98:99], v194 offset:10240\n"
  "v_cvt_pk_bf16_f32 v107, v122, v123\n"
  "v_mfma_f32_16x16x32_bf16 v[132:135], a[72:75], a[40:43], 0\n"
  "buffer_load_dwordx4 v200, s[44:47], s53 offen offset:2048 lds\n"
  "v_mfma_f32_16x16x32_bf16 v[132:135], a[76:79], a[44:47], v[132:135]\n"
  "ds_read_b64_tr_b16 a[104:105], v195 offset:8192\n"
  "v_cvt_pk_bf16_f32 v76, v76, v77\n"
  "v_mfma_f32_16x16x32_bf16 v[52:55], a[80:83], a[40:43], 0\n"
  "ds_read_b128 a[176:179], v203 offset:40960\n"
  "v_cvt_pk_bf16_f32 v77, v78, v79\n"
  "v_mfma_f32_16x16x32_bf16 v[52:55], a[84:87], a[44:47], v[52:55]\n"
  "ds_read_b128 a[180:183], v203 offset:45056\n"
  "v_cvt_pk_bf16_f32 v78, v92, v93\n"
  "v_mfma_f32_16x16x32_bf16 v[148:151], a[88:91], a[40:43], 0\n"
  "buffer_load_dwordx4 v201, s[44:47], s53 offen offset:3072 lds\n"
  "v_mfma_f32_16x16x32_bf16 v[148:151], a[92:95], a[44:47], v[148:151]\n"
  "ds_read_b64_tr_b16 a[106:107], v195 offset:10240\n"
  "v_cvt_pk_bf16_f32 v79, v94, v95\n"
  "v_mfma_f32_16x16x32_bf16 v[24:27], a[64:67], a[48:51], 0\n"
  "s_add_u32 m0, s51, 4096\n"
  "ds_read_b64_tr_b16 a[112:113], v196 offset:8192\n"
  "v_mfma_f32_16x16x32_bf16 v[24:27], a[68:71], a[52:55], v[24:27]\n"
  "s_add_u32 s54, s55, 557056\n"
  "ds_read_b64_tr_b16 a[114:115], v196 offset:10240\n"
  "v_mfma_f32_16x16x32_bf16 v[136:139], a[72:75], a[48:51], 0\n"
  "buffer_load_dwordx4 v198, s[44:47], s54 offen offset:0 lds\n"
  "v_mfma_f32_16x16x32_bf16 v[136:139], a[76:79], a[52:55], v[136:139]\n"
  "ds_read_b128 a[184:187], v203 offset:41984\n"
  "v_cvt_pk_bf16_f32 v108, v108, v109\n"
  "v_mfma_f32_16x16x32_bf16 v[56:59], a[80:83], a[48:51], 0\n"
  "ds_read_b128 a[188:191], v203 offset:46080\n"
  "v_cvt_pk_bf16_f32 v109, v110, v111\n"
  "v_mfma_f32_16x16x32_bf16 v[56:59], a[84:87], a[52:55], v[56:59]\n"
  "ds_read_b64_tr_b16 a[120:121], v197 offset:8192\n"
  "v_cvt_pk_bf16_f32 v110, v124, v125\n"
  "v_mfma_f32_16x16x32_bf16 v[152:155], a[88:91], a[48:51], 0\n"
  "buffer_load_dwordx4 v199, s[44:47], s54 offen offset:1024 lds\n"
  "v_mfma_f32_16x16x32_bf16 v[152:155], a[92:95], a[52:55], v[152:155]\n"
  "s_waitcnt lgkmcnt(6)\n"
  "ds_read_b64_tr_b16 a[122:123], v197 offset:10240\n"
  "v_cvt_pk_bf16_f32 v111, v126, v127\n"
  "v_mfma_f32_16x16x32_bf16 v[28:31], a[64:67], a[56:59], 0\n"
  "s_waitcnt vmcnt(14)\n"
  "ds_read_b64_tr_b16 a[100:101], v194 offset:12288\n"
  "v_permlane16_swap_b32 v0, v2\n"
  "v_mfma_f32_16x16x32_bf16 v[28:31], a[68:71], a[60:63], v[28:31]\n"
  "ds_read_b64_tr_b16 a[102:103], v194 offset:14336\n"
  "v_permlane16_swap_b32 v1, v3\n"
  "v_mfma_f32_16x16x32_bf16 v[140:143], a[72:75], a[56:59], 0\n"
  "buffer_load_dwordx4 v200, s[44:47], s54 offen offset:2048 lds\n"
  "v_mfma_f32_16x16x32_bf16 v[140:143], a[76:79], a[60:63], v[140:143]\n"
  "ds_read_b64_tr_b16 a[108:109], v195 offset:12288\n"
  "v_permlane16_swap_b32 v32, v34\n"
  "v_mfma_f32_16x16x32_bf16 v[60:63], a[80:83], a[56:59], 0\n"
  "ds_read_b64_tr_b16 a[110:111], v195 offset:14336\n"
  "v_permlane16_swap_b32 v33, v35\n"
  "v_mfma_f32_16x16x32_bf16 v[60:63], a[84:87], a[60:63], v[60:63]\n"
  "ds_read_b64_tr_b16 a[116:117], v196 offset:12288\n"
  "v_permlane16_swap_b32 v64, v66\n"
  "v_mfma_f32_16x16x32_bf16 v[156:159], a[88:91], a[56:59], 0\n"
  "buffer_load_dwordx4 v201, s[44:47], s54 offen offset:3072 lds\n"
  "ds_read_b64_tr_b16 a[124:125], v197 offset:12288\n"
  "v_mfma_f32_16x16x32_bf16 v[156:159], a[92:95], a[60:63], v[156:159]\n"
  "ds_read_b64_tr_b16 a[118:119], v196 offset:14336\n"
  "ds_read_b64_tr_b16 a[126:127], v197 offset:14336\n"
  "v_mfma_f32_16x16x32_bf16 v[16:19], a[96:99], a[160:163], v[16:19]\n"
  "s_waitcnt lgkmcnt(6)\n"
  "s_add_u32 m0, s51, 8192\n"
  "ds_read_b128 a[0:3], v202 offset:49152\n"
  "v_mfma_f32_16x16x32_bf16 v[16:19], a[100:103], a[164:167], v[16:19]\n"
  "s_add_u32 s53, s55, 262144\n"
  "ds_read_b128 a[4:7], v202 offset:53248\n"
  "v_mfma_f32_16x16x32_bf16 v[128:131], a[104:107], a[160:163], v[128:131]\n"
  "s_waitcnt lgkmcnt(6)\n"
  "s_mov_b32 s16, s42\n"
  "buffer_load_dwordx4 v198, s[44:47], s53 offen offset:0 lds\n"
  "v_mfma_f32_16x16x32_bf16 v[128:131], a[108:111], a[164:167], v[128:131]\n"
  "s_waitcnt vmcnt(13)\n"
  "s_mov_b32 s17, s43\n"
  "ds_read_b64_tr_b16 a[64:65], v194 offset:16384\n"
  "v_mfma_f32_16x16x32_bf16 v[48:51], a[112:115], a[160:163], v[48:51]\n"
  "s_waitcnt lgkmcnt(4)\n"
  "s_lshl_b32 s57, s50, 8\n"
  "ds_read_b64_tr_b16 a[66:67], v194 offset:18432\n"
  "v_mfma_f32_16x16x32_bf16 v[48:51], a[116:119], a[164:167], v[48:51]\n"
  "s_sub_u32 s18, 0x80000, s57\n"
  "ds_read_b64_tr_b16 a[72:73], v195 offset:16384\n"
  "v_mfma_f32_16x16x32_bf16 v[144:147], a[120:123], a[160:163], v[144:147]\n"
  "s_waitcnt lgkmcnt(5)\n"
  "buffer_load_dwordx4 v199, s[44:47], s53 offen offset:1024 lds\n"
  "v_mfma_f32_16x16x32_bf16 v[144:147], a[124:127], a[164:167], v[144:147]\n"
  "ds_read_b128 a[8:11], v202 offset:50176\n"
  "v_permlane16_swap_b32 v65, v67\n"
  "v_mfma_f32_16x16x32_bf16 v[20:23], a[96:99], a[168:171], v[20:23]\n"
  "ds_read_b128 a[12:15], v202 offset:54272\n"
  "v_permlane16_swap_b32 v96, v98\n"
  "v_mfma_f32_16x16x32_bf16 v[20:23], a[100:103], a[172:175], v[20:23]\n"
  "ds_read_b64_tr_b16 a[74:75], v195 offset:18432\n"
  "v_permlane16_swap_b32 v97, v99\n"
  "v_mfma_f32_16x16x32_bf16 v[132:135], a[104:107], a[168:171], v[132:135]\n"
  "buffer_load_dwordx4 v200, s[44:47], s53 offen offset:2048 lds\n"
  "v_mfma_f32_16x16x32_bf16 v[132:135], a[108:111], a[172:175], v[132:135]\n"
  "ds_read_b64_tr_b16 a[80:81], v196 offset:16384\n"
  "v_permlane16_swap_b32 v4, v6\n"
  "v_mfma_f32_16x16x32_bf16 v[52:55], a[112:115], a[168:171], v[52:55]\n"
  "ds_read_b128 a[16:19], v203 offset:49152\n"
  "v_permlane16_swap_b32 v5, v7\n"
  "v_mfma_f32_16x16x32_bf16 v[52:55], a[116:119], a[172:175], v[52:55]\n"
  "ds_read_b128 a[20:23], v203 offset:53248\n"
  "v_permlane16_swap_b32 v36, v38\n"
  "v_mfma_f32_16x16x32_bf16 v[148:151], a[120:123], a[168:171], v[148:151]\n"
  "buffer_load_dwordx4 v201, s[44:47], s53 offen offset:3072 lds\n"
  "v_mfma_f32_16x16x32_bf16 v[148:151], a[124:127], a[172:175], v[148:151]\n"
  "ds_read_b64_tr_b16 a[82:83], v196 offset:18432\n"
  "v_permlane16_swap_b32 v37, v39\n"
  "v_mfma_f32_16x16x32_bf16 v[24:27], a[96:99], a[176:179], v[24:27]\n"
  "s_add_u32 m0, s51, 12288\n"
  "ds_read_b64_tr_b16 a[88:89], v197 offset:16384\n"
  "v_mfma_f32_16x16x32_bf16 v[24:27], a[100:103], a[180:183], v[24:27]\n"
  "s_add_u32 s54, s55, 294912\n"
  "ds_read_b64_tr_b16 a[90:91], v197 offset:18432\n"
  "v_mfma_f32_16x16x32_bf16 v[136:139], a[104:107], a[176:179], v[136:139]\n"
  "buffer_load_dwordx4 v198, s[44:47], s54 offen offset:0 lds\n"
  "v_mfma_f32_16x16x32_bf16 v[136:139], a[108:111], a[180:183], v[136:139]\n"
  "ds_read_b128 a[24:27], v203 offset:50176\n"
  "v_permlane16_swap_b32 v68, v70\n"
  "v_mfma_f32_16x16x32_bf16 v[56:59], a[112:115], a[176:179], v[56:59]\n"
  "ds_read_b128 a[28:31], v203 offset:54272\n"
  "v_permlane16_swap_b32 v69, v71\n"
  "v_mfma_f32_16x16x32_bf16 v[56:59], a[116:119], a[180:183], v[56:59]\n"
  "s_waitcnt vmcnt(13)\n"
  "ds_read_b64_tr_b16 a[68:69], v194 offset:20480\n"
  "v_permlane16_swap_b32 v100, v102\n"
  "v_mfma_f32_16x16x32_bf16 v[152:155], a[120:123], a[176:179], v[152:155]\n"
  "buffer_load_dwordx4 v199, s[44:47], s54 offen offset:1024 lds\n"
  "v_mfma_f32_16x16x32_bf16 v[152:155], a[124:127], a[180:183], v[152:155]\n"
  "ds_read_b64_tr_b16 a[70:71], v194 offset:22528\n"
  "v_permlane16_swap_b32 v101, v103\n"
  "v_mfma_f32_16x16x32_bf16 v[28:31], a[96:99], a[184:187], v[28:31]\n"
  "ds_read_b64_tr_b16 a[76:77], v195 offset:20480\n"
  "v_permlane16_swap_b32 v8, v10\n"
  "v_mfma_f32_16x16x32_bf16 v[28:31], a[100:103], a[188:191], v[28:31]\n"
  "ds_read_b64_tr_b16 a[78:79], v195 offset:22528\n"
  "v_permlane16_swap_b32 v9, v11\n"
  "v_mfma_f32_16x16x32_bf16 v[140:143], a[104:107], a[184:187], v[140:143]\n"
  "buffer_load_dwordx4 v200, s[44:47], s54 offen offset:2048 lds\n"
  "v_mfma_f32_16x16x32_bf16 v[140:143], a[108:111], a[188:191], v[140:143]\n"
  "ds_read_b64_tr_b16 a[84:85], v196 offset:20480\n"
  "v_permlane16_swap_b32 v40, v42\n"
  "v_mfma_f32_16x16x32_bf16 v[60:63], a[112:115], a[184:187], v[60:63]\n"
  "ds_read_b64_tr_b16 a[86:87], v196 offset:22528\n"
  "v_permlane16_swap_b32 v41, v43\n"
  "v_mfma_f32_16x16x32_bf16 v[60:63], a[116:119], a[188:191], v[60:63]\n"
  "ds_read_b64_tr_b16 a[92:93], v197 offset:20480\n"
  "v_permlane16_swap_b32 v72, v74\n"
  "v_mfma_f32_16x16x32_bf16 v[156:159], a[120:123], a[184:187], v[156:159]\n"
  "buffer_load_dwordx4 v201, s[44:47], s54 offen offset:3072 lds\n"
  "s_mov_b32 m0, s63\n"
  "v_mfma_f32_16x16x32_bf16 v[156:159], a[124:127], a[188:191], v[156:159]\n"
  "buffer_load_dwordx4 v228, s[24:27], s61 offen offset:0 lds\n"
  "buffer_load_dwordx4 v229, s[24:27], s61 offen offset:1024 lds\n"
  "buffer_load_dwordx4 v230, s[24:27], s61 offen offset:2048 lds\n"
  "buffer_load_dwordx4 v231, s[24:27], s61 offen offset:3072 lds\n"
  "ds_read_b64_tr_b16 a[94:95], v197 offset:22528\n"
  "s_waitcnt lgkmcnt(7)\n"
  "v_mfma_f32_16x16x32_bf16 v[16:19], a[64:67], a[0:3], v[16:19]\n"
  "s_waitcnt lgkmcnt(6)\n"
  "s_waitcnt vmcnt(16)\n"
  "s_add_u32 m0, s51, 16384\n"
  "ds_read_b64_tr_b16 a[96:97], v194 offset:0\n"
  "v_mfma_f32_16x16x32_bf16 v[16:19], a[68:71], a[4:7], v[16:19]\n"
  "s_add_u32 s53, s55, 0\n"
  "ds_read_b64_tr_b16 a[98:99], v194 offset:2048\n"
  "v_mfma_f32_16x16x32_bf16 v[128:131], a[72:75], a[0:3], v[128:131]\n"
  "s_waitcnt lgkmcnt(6)\n"
  "buffer_load_dwordx4 v198, s[44:47], s53 offen offset:0 lds\n"
  "v_mfma_f32_16x16x32_bf16 v[128:131], a[76:79], a[4:7], v[128:131]\n"
  "ds_read_b64_tr_b16 a[104:105], v195 offset:0\n"
  "v_permlane16_swap_b32 v73, v75\n"
  "v_mfma_f32_16x16x32_bf16 v[48:51], a[80:83], a[0:3], v[48:51]\n"
  "s_waitcnt lgkmcnt(5)\n"
  "ds_read_b64_tr_b16 a[106:107], v195 offset:2048\n"
  "v_permlane16_swap_b32 v104, v106\n"
  "v_mfma_f32_16x16x32_bf16 v[48:51], a[84:87], a[4:7], v[48:51]\n"
  "ds_read_b64_tr_b16 a[112:113], v196 offset:0\n"
  "v_permlane16_swap_b32 v105, v107\n"
  "v_mfma_f32_16x16x32_bf16 v[144:147], a[88:91], a[0:3], v[144:147]\n"
  "s_waitcnt lgkmcnt(5)\n"
  "buffer_load_dwordx4 v199, s[44:47], s53 offen offset:1024 lds\n"
  "v_mfma_f32_16x16x32_bf16 v[144:147], a[92:95], a[4:7], v[144:147]\n"
  "ds_read_b64_tr_b16 a[114:115], v196 offset:2048\n"
  "v_permlane16_swap_b32 v12, v14\n"
  "v_mfma_f32_16x16x32_bf16 v[20:23], a[64:67], a[8:11], v[20:23]\n"
  "ds_read_b64_tr_b16 a[120:121], v197 offset:0\n"
  "v_permlane16_swap_b32 v13, v15\n"
  "v_mfma_f32_16x16x32_bf16 v[20:23], a[68:71], a[12:15], v[20:23]\n"
  "ds_read_b64_tr_b16 a[122:123], v197 offset:2048\n"
  "v_permlane16_swap_b32 v44, v46\n"
  "v_mfma_f32_16x16x32_bf16 v[132:135], a[72:75], a[8:11], v[132:135]\n"
  "buffer_load_dwordx4 v200, s[44:47], s53 offen offset:2048 lds\n"
  "v_mfma_f32_16x16x32_bf16 v[132:135], a[76:79], a[12:15], v[132:135]\n"
  "v_permlane16_swap_b32 v45, v47\n"
  "v_permlane16_swap_b32 v76, v78\n"
  "v_mfma_f32_16x16x32_bf16 v[52:55], a[80:83], a[8:11], v[52:55]\n"
  "L_dry1_0:\n"
  "L_pad4:\n"
  "s_cbranch_execz L_dryr1\n"
  "v_permlane16_swap_b32 v77, v79\n"
  "v_permlane16_swap_b32 v108, v110\n"
  "v_mfma_f32_16x16x32_bf16 v[52:55], a[84:87], a[12:15], v[52:55]\n"
  "s_waitcnt vmcnt(15)\n"
  "ds_read_b64_tr_b16 a[100:101], v194 offset:4096\n"
  "v_permlane16_swap_b32 v109, v111\n"
  "v_mfma_f32_16x16x32_bf16 v[148:151], a[88:91], a[8:11], v[148:151]\n"
  "buffer_load_dwordx4 v201, s[44:47], s53 offen offset:3072 lds\n"
  "v_mfma_f32_16x16x32_bf16 v[148:151], a[92:95], a[12:15], v[148:151]\n"
  "ds_read_b64_tr_b16 a[102:103], v194 offset:6144\n"
  "v_cvt_pk_bf16_f32 v16, v16, v17\n"
  "v_mfma_f32_16x16x32_bf16 v[24:27], a[64:67], a[16:19], v[24:27]\n"
  "s_add_u32 m0, s51, 20480\n"
  "ds_read_b64_tr_b16 a[108:109], v195 offset:4096\n"
  "v_mfma_f32_16x16x32_bf16 v[24:27], a[68:71], a[20:23], v[24:27]\n"
  "s_add_u32 s54, s55, 32768\n"
  "ds_read_b64_tr_b16 a[110:111], v195 offset:6144\n"
  "v_mfma_f32_16x16x32_bf16 v[136:139], a[72:75], a[16:19], v[136:139]\n"
  "buffer_load_dwordx4 v198, s[44:47], s54 offen offset:0 lds\n"
  "v_mfma_f32_16x16x32_bf16 v[136:139], a[76:79], a[20:23], v[136:139]\n"
  "ds_read_b64_tr_b16 a[116:117], v196 offset:4096\n"
  "v_cvt_pk_bf16_f32 v17, v18, v19\n"
  "v_mfma_f32_16x16x32_bf16 v[56:59], a[80:83], a[16:19], v[56:59]\n"
  "ds_read_b64_tr_b16 a[118:119], v196 offset:6144\n"
  "v_cvt_pk_bf16_f32 v18, v128, v129\n"
  "v_mfma_f32_16x16x32_bf16 v[56:59], a[84:87], a[20:23], v[56:59]\n"
  "ds_read_b64_tr_b16 a[124:125], v197 offset:4096\n"
  "v_cvt_pk_bf16_f32 v19, v130, v131\n"
  "v_mfma_f32_16x16x32_bf16 v[152:155], a[88:91], a[16:19], v[152:155]\n"
  "buffer_load_dwordx4 v199, s[44:47], s54 offen offset:1024 lds\n"
  "v_mfma_f32_16x16x32_bf16 v[152:155], a[92:95], a[20:23], v[152:155]\n"
  "ds_read_b64_tr_b16 a[126:127], v197 offset:6144\n"
  "v_cvt_pk_bf16_f32 v48, v48, v49\n"
  "v_mfma_f32_16x16x32_bf16 v[28:31], a[64:67], a[24:27], v[28:31]\n"
  "v_cvt_pk_bf16_f32 v49, v50, v51\n"
  "v_cvt_pk_bf16_f32 v50, v144, v145\n"
  "v_mfma_f32_16x16x32_bf16 v[28:31], a[68:71], a[28:31], v[28:31]\n"
  "v_cvt_pk_bf16_f32 v51, v146, v147\n"
  "v_cvt_pk_bf16_f32 v20, v20, v21\n"
  "v_mfma_f32_16x16x32_bf16 v[140:143], a[72:75], a[24:27], v[140:143]\n"
  "buffer_load_dwordx4 v200, s[44:47], s54 offen offset:2048 lds\n"
  "v_mfma_f32_16x16x32_bf16 v[140:143], a[76:79], a[28:31], v[140:143]\n"
  "v_cvt_pk_bf16_f32 v21, v22, v23\n"
  "v_cvt_pk_bf16_f32 v22, v132, v133\n"
  "v_mfma_f32_16x16x32_bf16 v[60:63], a[80:83], a[24:27], v[60:63]\n"
  "v_cvt_pk_bf16_f32 v23, v134, v135\n"
  "v_cvt_pk_bf16_f32 v52, v52, v53\n"
  "v_mfma_f32_16x16x32_bf16 v[60:63], a[84:87], a[28:31], v[60:63]\n"
  "v_cvt_pk_bf16_f32 v53, v54, v55\n"
  "v_cvt_pk_bf16_f32 v54, v148, v149\n"
  "v_mfma_f32_16x16x32_bf16 v[156:159], a[88:91], a[24:27], v[156:159]\n"
  "buffer_load_dwordx4 v201, s[44:47], s54 offen offset:3072 lds\n"
  "v_mfma_f32_16x16x32_bf16 v[156:159], a[92:95], a[28:31], v[156:159]\n"
  "s_waitcnt lgkmcnt(1)\n"
  "v_cvt_pk_bf16_f32 v55, v150, v151\n"
  "v_cvt_pk_bf16_f32 v27, v26, v27\n"
  "v_mfma_f32_16x16x32_bf16 v[80:83], a[96:99], a[0:3], 0\n"
  "s_waitcnt vmcnt(16)\n"
  "s_add_u32 m0, s63, 16384\n"
  "ds_read_b64_tr_b16 a[64:65], v194 offset:8192\n"
  "v_mfma_f32_16x16x32_bf16 v[80:83], a[100:103], a[4:7], v[80:83]\n"
  "buffer_load_dwordx4 v232, s[28:31], s61 offen offset:0 lds\n"
  "v_mfma_f32_16x16x32_bf16 v[160:163], a[104:107], a[0:3], 0\n"
  "s_mov_b64 s[20:21], s[24:25]\n"
  "ds_read_b64_tr_b16 a[66:67], v194 offset:10240\n"
  "v_mfma_f32_16x16x32_bf16 v[160:163], a[108:111], a[4:7], v[160:163]\n"
  "s_mov_b64 s[22:23], s[28:29]\n"
  "ds_read_b64_tr_b16 a[72:73], v195 offset:8192\n"
  "v_mfma_f32_16x16x32_bf16 v[112:115], a[112:115], a[0:3], 0\n"
  "s_add_u32 s44, s42, s13\n"
  "s_addc_u32 s45, s43, 0\n"
  "buffer_load_dwordx4 v233, s[28:31], s61 offen offset:1024 lds\n"
  "v_mfma_f32_16x16x32_bf16 v[112:115], a[116:119], a[4:7], v[112:115]\n"
  "s_add_u32 s46, s44, 0x8000\n"
  "s_addc_u32 s47, s45, 0\n"
  "ds_read_b64_tr_b16 a[74:75], v195 offset:10240\n"
  "v_mfma_f32_16x16x32_bf16 v[176:179], a[120:123], a[0:3], 0\n"
  "s_waitcnt lgkmcnt(4)\n"
  "s_sub_u32 s51, s51, s63\n"
  "ds_read_b64_tr_b16 a[80:81], v196 offset:8192\n"
  "v_mfma_f32_16x16x32_bf16 v[176:179], a[124:127], a[4:7], v[176:179]\n"
  "buffer_load_dwordx4 v234, s[28:31], s61 offen offset:2048 lds\n"
  "v_mfma_f32_16x16x32_bf16 v[84:87], a[96:99], a[8:11], 0\n"
  "ds_read_b64_tr_b16 a[82:83], v196 offset:10240\n"
  "v_cvt_pk_bf16_f32 v26, v24, v25\n"
  "v_mfma_f32_16x16x32_bf16 v[84:87], a[100:103], a[12:15], v[84:87]\n"
  "ds_read_b64_tr_b16 a[88:89], v197 offset:8192\n"
  "v_cvt_pk_bf16_f32 v24, v136, v137\n"
  "v_mfma_f32_16x16x32_bf16 v[164:167], a[104:107], a[8:11], 0\n"
  "buffer_load_dwordx4 v235, s[28:31], s61 offen offset:3072 lds\n"
  "v_mfma_f32_16x16x32_bf16 v[164:167], a[108:111], a[12:15], v[164:167]\n"
  "ds_read_b64_tr_b16 a[90:91], v197 offset:10240\n"
  "v_cvt_pk_bf16_f32 v25, v138, v139\n"
  "v_mfma_f32_16x16x32_bf16 v[116:119], a[112:115], a[8:11], 0\n"
  "s_add_u32 s61, s61, 0x4000\n"
  "v_cvt_pk_bf16_f32 v59, v58, v59\n"
  "v_cvt_pk_bf16_f32 v58, v56, v57\n"
  "s_waitcnt vmcnt(12)\n"
  "s_barrier\n"
  "v_mfma_f32_16x16x32_bf16 v[116:119], a[116:119], a[12:15], v[116:119]\n"
  "ds_read_b128 a[128:131], v216 offset:0\n"
  "v_cvt_pk_bf16_f32 v56, v152, v153\n"
  "v_mfma_f32_16x16x32_bf16 v[180:183], a[120:123], a[8:11], 0\n"
  "ds_read_b128 a[132:135], v217 offset:0\n"
  "buffer_store_dword v250, v249, s[68:71], 0 offen sc1\n"
  "v_mfma_f32_16x16x32_bf16 v[180:183], a[124:127], a[12:15], v[180:183]\n"
  "ds_read_b128 a[136:139], v218 offset:0\n"
  "v_cvt_pk_bf16_f32 v57, v154, v155\n"
  "v_mfma_f32_16x16x32_bf16 v[88:91], a[96:99], a[16:19], 0\n"
  "ds_read_b128 a[140:143], v219 offset:0\n"
  "v_cvt_pk_bf16_f32 v31, v30, v31\n"
  "v_mfma_f32_16x16x32_bf16 v[88:91], a[100:103], a[20:23], v[88:91]\n"
  "ds_read_b128 a[144:147], v216 offset:4096\n"
  "v_cvt_pk_bf16_f32 v30, v28, v29\n"
  "v_mfma_f32_16x16x32_bf16 v[168:171], a[104:107], a[16:19], 0\n"
  "s_add_u32 m0, s63, 32768\n"
  "v_cvt_pk_bf16_f32 v28, v140, v141\n"
  "v_cvt_pk_bf16_f32 v29, v142, v143\n"
  "v_mfma_f32_16x16x32_bf16 v[168:171], a[108:111], a[20:23], v[168:171]\n"
  "s_waitcnt lgkmcnt(4)\n"
  "buffer_load_dwordx4 v228, s[24:27], s61 offen offset:0 lds\n"
  "v_mfma_f32_16x16x32_bf16 v[240:243], a[128:131], v[0:3], 0\n"
  "s_waitcnt lgkmcnt(3)\n"
  "v_cvt_pk_bf16_f32 v63, v62, v63\n"
  "v_cvt_pk_bf16_f32 v62, v60, v61\n"
  "v_mfma_f32_16x16x32_bf16 v[240:243], a[132:135], v[32:35], v[240:243]\n"
  "s_waitcnt lgkmcnt(2)\n"
  "ds_read_b64_tr_b16 a[68:69], v194 offset:12288\n"
  "v_cvt_pk_bf16_f32 v60, v156, v157\n"
  "v_mfma_f32_16x16x32_bf16 v[240:243], a[136:139], v[64:67], v[240:243]\n"
  "s_waitcnt lgkmcnt(2)\n"
  "ds_read_b64_tr_b16 a[70:71], v194 offset:14336\n"
  "v_cvt_pk_bf16_f32 v61, v158, v159\n"
  "v_mfma_f32_16x16x32_bf16 v[240:243], a[140:143], v[96:99], v[240:243]\n"
  "buffer_load_dwordx4 v229, s[24:27], s61 offen offset:1024 lds\n"
  "v_mfma_f32_16x16x32_bf16 v[244:247], a[128:131], v[4:7], 0\n"
  "ds_read_b64_tr_b16 a[76:77], v195 offset:12288\n"
  "v_accvgpr_write_b32 a192, v16\n"
  "v_mfma_f32_16x16x32_bf16 v[244:247], a[132:135], v[36:39], v[244:247]\n"
  "ds_read_b64_tr_b16 a[78:79], v195 offset:14336\n"
  "v_accvgpr_write_b32 a193, v17\n"
  "v_mfma_f32_16x16x32_bf16 v[244:247], a[136:139], v[68:71], v[244:247]\n"
  "ds_read_b64_tr_b16 a[84:85], v196 offset:12288\n"
  "v_accvgpr_write_b32 a194, v18\n"
  "v_mfma_f32_16x16x32_bf16 v[244:247], a[140:143], v[100:103], v[244:247]\n"
  "buffer_load_dwordx4 v230, s[24:27], s61 offen offset:2048 lds\n"
  "v_mfma_f32_16x16x32_bf16 v[248:251], a[128:131], v[8:11], 0\n"
  "ds_read_b64_tr_b16 a[86:87], v196 offset:14336\n"
  "v_accvgpr_write_b32 a195, v19\n"
  "v_mfma_f32_16x16x32_bf16 v[248:251], a[132:135], v[40:43], v[248:251]\n"
  "ds_read_b64_tr_b16 a[92:93], v197 offset:12288\n"
  "v_accvgpr_write_b32 a200, v24\n"
  "v_mfma_f32_16x16x32_bf16 v[248:251], a[136:139], v[72:75], v[248:251]\n"
  "ds_read_b64_tr_b16 a[94:95], v197 offset:14336\n"
  "v_accvgpr_write_b32 a201, v25\n"
  "v_mfma_f32_16x16x32_bf16 v[248:251], a[140:143], v[104:107], v[248:251]\n"
  "buffer_load_dwordx4 v231, s[24:27], s61 offen offset:3072 lds\n"
  "v_mfma_f32_16x16x32_bf16 v[252:255], a[128:131], v[12:15], 0\n"
  "v_accvgpr_write_b32 a202, v26\n"
  "v_accvgpr_write_b32 a203, v27\n"
  "v_mfma_f32_16x16x32_bf16 v[252:255], a[132:135], v[44:47], v[252:255]\n"
  "v_accvgpr_write_b32 a208, v48\n"
  "v_accvgpr_write_b32 a209, v49\n"
  "v_mfma_f32_16x16x32_bf16 v[252:255], a[136:139], v[76:79], v[252:255]\n"
  "s_add_u32 m0, s63, 49152\n"
  "v_accvgpr_write_b32 a210, v50\n"
  "v_accvgpr_write_b32 a211, v51\n"
  "v_mfma_f32_16x16x32_bf16 v[252:255], a[140:143], v[108:111], v[252:255]\n"
  "buffer_load_dwordx4 v232, s[28:31], s61 offen offset:0 lds\n"
  "v_mfma_f32_16x16x32_bf16 v[120:123], a[112:115], a[16:19], 0\n"
  "v_accvgpr_write_b32 a216, v56\n"
  "v_accvgpr_write_b32 a217, v57\n"
  "v_mfma_f32_16x16x32_bf16 v[120:123], a[116:119], a[20:23], v[120:123]\n"
  "v_accvgpr_write_b32 a218, v58\n"
  "v_accvgpr_write_b32 a219, v59\n"
  "v_max3_f32 v240, v240, v241, v242\n"
  "v_mfma_f32_16x16x32_bf16 v[184:187], a[120:123], a[16:19], 0\n"
  "v_max_f32 v240, v240, v243\n"
  "v_max3_f32 v244, v244, v245, v246\n"
  "v_max_f32 v244, v244, v247\n"
  "v_mfma_f32_16x16x32_bf16 v[184:187], a[124:127], a[20:23], v[184:187]\n"
  "buffer_load_dwordx4 v233, s[28:31], s61 offen offset:1024 lds\n"
  "v_max3_f32 v248, v248, v249, v250\n"
  "v_max_f32 v248, v248, v251\n"
  "v_max3_f32 v252, v252, v253, v254\n"
  "v_mfma_f32_16x16x32_bf16 v[92:95], a[96:99], a[24:27], 0\n"
  "v_max_f32 v252, v252, v255\n"
  "v_mov_b32 v241, v240\n"
  "v_mov_b32 v245, v244\n"
  "v_mfma_f32_16x16x32_bf16 v[92:95], a[100:103], a[28:31], v[92:95]\n"
  "v_mov_b32 v249, v248\n"
  "v_mov_b32 v253, v252\n"
  "v_permlane16_swap_b32 v240, v241\n"
  "v_mfma_f32_16x16x32_bf16 v[172:175], a[104:107], a[24:27], 0\n"
  "v_permlane16_swap_b32 v244, v245\n"
  "v_permlane16_swap_b32 v248, v249\n"
  "v_permlane16_swap_b32 v252, v253\n"
  "v_mfma_f32_16x16x32_bf16 v[172:175], a[108:111], a[28:31], v[172:175]\n"
  "buffer_load_dwordx4 v234, s[28:31], s61 offen offset:2048 lds\n"
  "v_max_f32 v240, v240, v241\n"
  "v_max_f32 v244, v244, v245\n"
  "v_max_f32 v248, v248, v249\n"
  "v_mfma_f32_16x16x32_bf16 v[124:127], a[112:115], a[24:27], 0\n"
  "v_max_f32 v252, v252, v253\n"
  "v_mov_b32 v241, v240\n"
  "v_mov_b32 v245, v244\n"
  "v_mfma_f32_16x16x32_bf16 v[124:127], a[116:119], a[28:31], v[124:127]\n"
  "v_mov_b32 v249, v248\n"
  "v_mov_b32 v253, v252\n"
  "v_permlane32_swap_b32 v240, v241\n"
  "v_mfma_f32_16x16x32_bf16 v[188:191], a[120:123], a[24:27], 0\n"
  "v_permlane32_swap_b32 v244, v245\n"
  "v_permlane32_swap_b32 v248, v249\n"
  "v_permlane32_swap_b32 v252, v253\n"
  "v_mfma_f32_16x16x32_bf16 v[188:191], a[124:127], a[28:31], v[188:191]\n"
  "buffer_load_dwordx4 v235, s[28:31], s61 offen offset:3072 lds\n"
  "v_max_f32 v240, v240, v241\n"
  "v_max_f32 v244, v244, v245\n"
  "v_max_f32 v248, v248, v249\n"
  "v_mfma_f32_16x16x32_bf16 v[80:83], a[64:67], a[160:163], v[80:83]\n"
  "s_waitcnt vmcnt(17)\n"
  "ds_read_b64_tr_b16 a[96:97], v194 offset:16384\n"
  "v_max_f32 v252, v252, v253\n"
  "v_sub_f32 v128, 0xc2200000, v240\n"
  "v_sub_f32 v129, 0xc2200000, v240\n"
  "s_waitcnt lgkmcnt(1)\n"
  "v_mfma_f32_16x16x32_bf16 v[80:83], a[68:71], a[164:167], v[80:83]\n"
  "ds_read_b64_tr_b16 a[98:99], v194 offset:18432\n"
  "v_sub_f32 v130, 0xc2200000, v240\n"
  "v_sub_f32 v131, 0xc2200000, v240\n"
  "v_sub_f32 v132, 0xc2200000, v244\n"
  "v_mfma_f32_16x16x32_bf16 v[160:163], a[72:75], a[160:163], v[160:163]\n"
  "ds_read_b64_tr_b16 a[104:105], v195 offset:16384\n"
  "v_sub_f32 v133, 0xc2200000, v244\n"
  "v_sub_f32 v134, 0xc2200000, v244\n"
  "v_sub_f32 v135, 0xc2200000, v244\n"
  "v_mfma_f32_16x16x32_bf16 v[160:163], a[76:79], a[164:167], v[160:163]\n"
  "ds_read_b64_tr_b16 a[106:107], v195 offset:18432\n"
  "v_sub_f32 v136, 0xc2200000, v248\n"
  "v_sub_f32 v137, 0xc2200000, v248\n"
  "v_sub_f32 v138, 0xc2200000, v248\n"
  "v_mfma_f32_16x16x32_bf16 v[112:115], a[80:83], a[160:163], v[112:115]\n"
  "ds_read_b64_tr_b16 a[112:113], v196 offset:16384\n"
  "v_sub_f32 v139, 0xc2200000, v248\n"
  "v_sub_f32 v140, 0xc2200000, v252\n"
  "v_sub_f32 v141, 0xc2200000, v252\n"
  "v_mfma_f32_16x16x32_bf16 v[112:115], a[84:87], a[164:167], v[112:115]\n"
  "ds_read_b64_tr_b16 a[114:115], v196 offset:18432\n"
  "v_sub_f32 v142, 0xc2200000, v252\n"
  "v_sub_f32 v143, 0xc2200000, v252\n"
  "v_mfma_f32_16x16x32_bf16 v[176:179], a[88:91], a[160:163], v[176:179]\n"
  "ds_read_b64_tr_b16 a[120:121], v197 offset:16384\n"
  "v_mfma_f32_16x16x32_bf16 v[176:179], a[92:95], a[164:167], v[176:179]\n"
  "ds_read_b64_tr_b16 a[122:123], v197 offset:18432\n"
  "v_mfma_f32_16x16x32_bf16 v[240:243], a[128:131], v[0:3], v[128:131]\n"
  "v_accvgpr_write_b32 a196, v20\n"
  "v_accvgpr_write_b32 a197, v21\n"
  "v_mfma_f32_16x16x32_bf16 v[240:243], a[132:135], v[32:35], v[240:243]\n"
  "ds_read_b128 a[148:151], v217 offset:4096\n"
  "v_accvgpr_write_b32 a198, v22\n"
  "v_mfma_f32_16x16x32_bf16 v[240:243], a[136:139], v[64:67], v[240:243]\n"
  "v_accvgpr_write_b32 a199, v23\n"
  "v_accvgpr_write_b32 a204, v28\n"
  "v_mfma_f32_16x16x32_bf16 v[240:243], a[140:143], v[96:99], v[240:243]\n"
  "v_accvgpr_write_b32 a205, v29\n"
  "v_accvgpr_write_b32 a206, v30\n"
  "v_mfma_f32_16x16x32_bf16 v[84:87], a[64:67], a[168:171], v[84:87]\n"
  "v_accvgpr_write_b32 a207, v31\n"
  "v_accvgpr_write_b32 a212, v52\n"
  "v_mfma_f32_16x16x32_bf16 v[84:87], a[68:71], a[172:175], v[84:87]\n"
  "v_accvgpr_write_b32 a213, v53\n"
  "v_accvgpr_write_b32 a214, v54\n"
  "v_mfma_f32_16x16x32_bf16 v[164:167], a[72:75], a[168:171], v[164:167]\n"
  "v_accvgpr_write_b32 a215, v55\n"
  "v_accvgpr_write_b32 a220, v60\n"
  "v_mfma_f32_16x16x32_bf16 v[164:167], a[76:79], a[172:175], v[164:167]\n"
  "v_accvgpr_write_b32 a221, v61\n"
  "v_accvgpr_write_b32 a222, v62\n"
  "v_mfma_f32_16x16x32_bf16 v[244:247], a[128:131], v[4:7], v[132:135]\n"
  "v_exp_f32 v240, v240\n"
  "v_mfma_f32_16x16x32_bf16 v[244:247], a[132:135], v[36:39], v[244:247]\n"
  "ds_read_b128 a[152:155], v218 offset:4096\n"
  "v_accvgpr_write_b32 a223, v63\n"
  "v_mfma_f32_16x16x32_bf16 v[244:247], a[136:139], v[68:71], v[244:247]\n"
  "v_exp_f32 v241, v241\n"
  "v_mfma_f32_16x16x32_bf16 v[244:247], a[140:143], v[100:103], v[244:247]\n"
  "v_cvt_pk_bf16_f32 v16, v240, v241\n"
  "v_and_b32 v156, 0xf0, v192\n"
  "v_mfma_f32_16x16x32_bf16 v[116:119], a[80:83], a[168:171], v[116:119]\n"
  "v_exp_f32 v242, v242\n"
  "v_mfma_f32_16x16x32_bf16 v[116:119], a[84:87], a[172:175], v[116:119]\n"
  "v_cmp_gt_u32 vcc, 32, v156\n"
  "v_mov_b32 v157, 0x3f803f80\n"
  "v_mfma_f32_16x16x32_bf16 v[180:183], a[88:91], a[168:171], v[180:183]\n"
  "v_exp_f32 v243, v243\n"
  "v_mfma_f32_16x16x32_bf16 v[180:183], a[92:95], a[172:175], v[180:183]\n"
  "v_cvt_pk_bf16_f32 v17, v242, v243\n"
  "v_mov_b32 v148, 0x44444444\n"
  "v_mfma_f32_16x16x32_bf16 v[248:251], a[128:131], v[8:11], v[136:139]\n"
  "v_exp_f32 v244, v244\n"
  "v_mfma_f32_16x16x32_bf16 v[248:251], a[132:135], v[40:43], v[248:251]\n"
  "ds_read_b128 a[156:159], v219 offset:4096\n"
  "v_cndmask_b32 v144, 0, v157, vcc\n"
  "v_mfma_f32_16x16x32_bf16 v[248:251], a[136:139], v[72:75], v[248:251]\n"
  "v_exp_f32 v245, v245\n"
  "v_mfma_f32_16x16x32_bf16 v[248:251], a[140:143], v[104:107], v[248:251]\n"
  "s_waitcnt vmcnt(13)\n"
  "ds_read_b64_tr_b16 a[100:101], v194 offset:20480\n"
  "v_cvt_pk_bf16_f32 v24, v244, v245\n"
  "v_mfma_f32_16x16x32_bf16 v[88:91], a[64:67], a[176:179], v[88:91]\n"
  "v_exp_f32 v246, v246\n"
  "v_mfma_f32_16x16x32_bf16 v[88:91], a[68:71], a[180:183], v[88:91]\n"
  "ds_read_b64_tr_b16 a[102:103], v194 offset:22528\n"
  "v_cndmask_b32 v145, 0, v157, vcc\n"
  "v_mfma_f32_16x16x32_bf16 v[168:171], a[72:75], a[176:179], v[168:171]\n"
  "v_exp_f32 v247, v247\n"
  "v_mfma_f32_16x16x32_bf16 v[168:171], a[76:79], a[180:183], v[168:171]\n"
  "ds_read_b64_tr_b16 a[108:109], v195 offset:20480\n"
  "v_cvt_pk_bf16_f32 v25, v246, v247\n"
  "v_mfma_f32_16x16x32_bf16 v[252:255], a[128:131], v[12:15], v[140:143]\n"
  "v_exp_f32 v248, v248\n"
  "v_mfma_f32_16x16x32_bf16 v[252:255], a[132:135], v[44:47], v[252:255]\n"
  "ds_read_b128 a[128:131], v216 offset:8192\n"
  "v_cndmask_b32 v146, 0, v157, vcc\n"
  "v_mfma_f32_16x16x32_bf16 v[252:255], a[136:139], v[76:79], v[252:255]\n"
  "v_exp_f32 v249, v249\n"
  "v_mfma_f32_16x16x32_bf16 v[252:255], a[140:143], v[108:111], v[252:255]\n"
  "ds_read_b64_tr_b16 a[110:111], v195 offset:22528\n"
  "v_cvt_pk_bf16_f32 v48, v248, v249\n"
  "v_mfma_f32_16x16x32_bf16 v[120:123], a[80:83], a[176:179], v[120:123]\n"
  "v_exp_f32 v250, v250\n"
  "v_mfma_f32_16x16x32_bf16 v[120:123], a[84:87], a[180:183], v[120:123]\n"
  "ds_read_b64_tr_b16 a[116:117], v196 offset:20480\n"
  "v_cndmask_b32 v147, 0, v157, vcc\n"
  "v_mfma_f32_16x16x32_bf16 v[184:187], a[88:91], a[176:179], v[184:187]\n"
  "v_exp_f32 v251, v251\n"
  "v_mfma_f32_16x16x32_bf16 v[184:187], a[92:95], a[180:183], v[184:187]\n"
  "ds_read_b64_tr_b16 a[118:119], v196 offset:22528\n"
  "v_cvt_pk_bf16_f32 v49, v250, v251\n"
  "v_mfma_f32_16x16x32_bf16 v[240:243], a[144:147], v[0:3], v[128:131]\n"
  "s_waitcnt lgkmcnt(4)\n"
  "v_exp_f32 v252, v252\n"
  "v_mfma_f32_16x16x32_bf16 v[240:243], a[148:151], v[32:35], v[240:243]\n"
  "ds_read_b128 a[132:135], v217 offset:8192\n"
  "v_cmp_eq_u32 vcc, 16, v156\n"
  "v_mfma_f32_16x16x32_bf16 v[240:243], a[152:155], v[64:67], v[240:243]\n"
  "v_exp_f32 v253, v253\n"
  "v_mfma_f32_16x16x32_bf16 v[240:243], a[156:159], v[96:99], v[240:243]\n"
  "ds_read_b64_tr_b16 a[124:125], v197 offset:20480\n"
  "v_cvt_pk_bf16_f32 v56, v252, v253\n"
  "v_mfma_f32_16x16x32_bf16 v[92:95], a[64:67], a[184:187], v[92:95]\n"
  "v_exp_f32 v254, v254\n"
  "v_mfma_f32_16x16x32_bf16 v[92:95], a[68:71], a[188:191], v[92:95]\n"
  "ds_read_b64_tr_b16 a[126:127], v197 offset:22528\n"
  "v_mov_b32 v157, 0xeeeeeeee\n"
  "v_mfma_f32_16x16x32_bf16 v[172:175], a[72:75], a[184:187], v[172:175]\n"
  "v_exp_f32 v255, v255\n"
  "v_mfma_f32_16x16x32_bf16 v[172:175], a[76:79], a[188:191], v[172:175]\n"
  "v_cvt_pk_bf16_f32 v57, v254, v255\n"
  "v_cndmask_b32 v148, v148, v157, vcc\n"
  "v_mfma_f32_16x16x32_bf16 v[244:247], a[144:147], v[4:7], v[132:135]\n"
  "v_exp_f32 v240, v240\n"
  "v_mfma_f32_16x16x32_bf16 v[244:247], a[148:151], v[36:39], v[244:247]\n"
  "ds_read_b128 a[136:139], v218 offset:8192\n"
  "v_mfma_f32_16x16x32_bf16 v[244:247], a[152:155], v[68:71], v[244:247]\n"
  "v_exp_f32 v241, v241\n"
  "v_mfma_f32_16x16x32_bf16 v[244:247], a[156:159], v[100:103], v[244:247]\n"
  "v_cvt_pk_bf16_f32 v18, v240, v241\n"
  "v_mfma_f32_16x16x32_bf16 v[124:127], a[80:83], a[184:187], v[124:127]\n"
  "v_exp_f32 v242, v242\n"
  "v_mfma_f32_16x16x32_bf16 v[124:127], a[84:87], a[188:191], v[124:127]\n"
  "v_mfma_f32_16x16x32_bf16 v[188:191], a[88:91], a[184:187], v[188:191]\n"
  "v_exp_f32 v243, v243\n"
  "v_mfma_f32_16x16x32_bf16 v[188:191], a[92:95], a[188:191], v[188:191]\n"
  "v_cvt_pk_bf16_f32 v19, v242, v243\n"
  "v_mfma_f32_16x16x32_bf16 v[248:251], a[144:147], v[8:11], v[136:139]\n"
  "v_exp_f32 v244, v244\n"
  "v_mfma_f32_16x16x32_bf16 v[248:251], a[148:151], v[40:43], v[248:251]\n"
  "ds_read_b128 a[140:143], v219 offset:8192\n"
  "v_mfma_f32_16x16x32_bf16 v[248:251], a[152:155], v[72:75], v[248:251]\n"
  "v_exp_f32 v245, v245\n"
  "v_mfma_f32_16x16x32_bf16 v[248:251], a[156:159], v[104:107], v[248:251]\n"
  "v_cvt_pk_bf16_f32 v26, v244, v245\n"
  "v_mfma_f32_16x16x32_bf16 v[80:83], a[96:99], a[32:35], v[80:83]\n"
  "v_exp_f32 v246, v246\n"
  "v_mfma_f32_16x16x32_bf16 v[80:83], a[100:103], a[36:39], v[80:83]\n"
  "v_mfma_f32_16x16x32_bf16 v[160:163], a[104:107], a[32:35], v[160:163]\n"
  "s_waitcnt lgkmcnt(1)\n"
  "v_exp_f32 v247, v247\n"
  "v_mfma_f32_16x16x32_bf16 v[160:163], a[108:111], a[36:39], v[160:163]\n"
  "v_cvt_pk_bf16_f32 v27, v246, v247\n"
  "v_mfma_f32_16x16x32_bf16 v[252:255], a[144:147], v[12:15], v[140:143]\n"
  "v_exp_f32 v248, v248\n"
  "v_mfma_f32_16x16x32_bf16 v[252:255], a[148:151], v[44:47], v[252:255]\n"
  "ds_read_b128 a[144:147], v216 offset:12288\n"
  "v_mfma_f32_16x16x32_bf16 v[252:255], a[152:155], v[76:79], v[252:255]\n"
  "v_exp_f32 v249, v249\n"
  "v_mfma_f32_16x16x32_bf16 v[252:255], a[156:159], v[108:111], v[252:255]\n"
  "v_cvt_pk_bf16_f32 v50, v248, v249\n"
  "v_mfma_f32_16x16x32_bf16 v[112:115], a[112:115], a[32:35], v[112:115]\n"
  "v_exp_f32 v250, v250\n"
  "v_mfma_f32_16x16x32_bf16 v[112:115], a[116:119], a[36:39], v[112:115]\n"
  "v_cvt_pk_bf16_f32 v80, v80, v81\n"
  "v_cvt_pk_bf16_f32 v81, v82, v83\n"
  "v_mfma_f32_16x16x32_bf16 v[176:179], a[120:123], a[32:35], v[176:179]\n"
  "v_exp_f32 v251, v251\n"
  "v_mfma_f32_16x16x32_bf16 v[176:179], a[124:127], a[36:39], v[176:179]\n"
  "v_cvt_pk_bf16_f32 v51, v250, v251\n"
  "v_cvt_pk_bf16_f32 v82, v160, v161\n"
  "v_mfma_f32_16x16x32_bf16 v[240:243], a[128:131], v[0:3], v[128:131]\n"
  "v_exp_f32 v252, v252\n"
  "v_mfma_f32_16x16x32_bf16 v[240:243], a[132:135], v[32:35], v[240:243]\n"
  "ds_read_b128 a[148:151], v217 offset:12288\n"
  "v_cvt_pk_bf16_f32 v83, v162, v163\n"
  "v_mfma_f32_16x16x32_bf16 v[240:243], a[136:139], v[64:67], v[240:243]\n"
  "s_waitcnt lgkmcnt(1)\n"
  "v_exp_f32 v253, v253\n"
  "v_mfma_f32_16x16x32_bf16 v[240:243], a[140:143], v[96:99], v[240:243]\n"
  "v_cvt_pk_bf16_f32 v58, v252, v253\n"
  "v_mfma_f32_16x16x32_bf16 v[84:87], a[96:99], a[40:43], v[84:87]\n"
  "v_exp_f32 v254, v254\n"
  "v_mfma_f32_16x16x32_bf16 v[84:87], a[100:103], a[44:47], v[84:87]\n"
  "v_cvt_pk_bf16_f32 v112, v112, v113\n"
  "v_cvt_pk_bf16_f32 v113, v114, v115\n"
  "v_mfma_f32_16x16x32_bf16 v[164:167], a[104:107], a[40:43], v[164:167]\n"
  "v_exp_f32 v255, v255\n"
  "v_mfma_f32_16x16x32_bf16 v[164:167], a[108:111], a[44:47], v[164:167]\n"
  "v_cvt_pk_bf16_f32 v59, v254, v255\n"
  "v_cvt_pk_bf16_f32 v114, v176, v177\n"
  "v_mfma_f32_16x16x32_bf16 v[244:247], a[128:131], v[4:7], v[132:135]\n"
  "s_add_u32 s61, s61, s9\n"
  "v_exp_f32 v240, v240\n"
  "v_mfma_f32_16x16x32_bf16 v[244:247], a[132:135], v[36:39], v[244:247]\n"
  "ds_read_b128 a[152:155], v218 offset:12288\n"
  "v_cvt_pk_bf16_f32 v115, v178, v179\n"
  "v_mfma_f32_16x16x32_bf16 v[244:247], a[136:139], v[68:71], v[244:247]\n"
  "s_and_b32 s61, s61, 0x7ffff\n"
  "v_exp_f32 v241, v241\n"
  "v_mfma_f32_16x16x32_bf16 v[244:247], a[140:143], v[100:103], v[244:247]\n"
  "v_cvt_pk_bf16_f32 v20, v240, v241\n"
  "v_mfma_f32_16x16x32_bf16 v[116:119], a[112:115], a[40:43], v[116:119]\n"
  "v_exp_f32 v242, v242\n"
  "v_mfma_f32_16x16x32_bf16 v[116:119], a[116:119], a[44:47], v[116:119]\n"
  "v_cvt_pk_bf16_f32 v84, v84, v85\n"
  "v_cvt_pk_bf16_f32 v85, v86, v87\n"
  "v_mfma_f32_16x16x32_bf16 v[180:183], a[120:123], a[40:43], v[180:183]\n"
  "v_exp_f32 v243, v243\n"
  "v_mfma_f32_16x16x32_bf16 v[180:183], a[124:127], a[44:47], v[180:183]\n"
  "v_cvt_pk_bf16_f32 v21, v242, v243\n"
  "v_cvt_pk_bf16_f32 v86, v164, v165\n"
  "v_mfma_f32_16x16x32_bf16 v[248:251], a[128:131], v[8:11], v[136:139]\n"
  "s_xor_b32 s52, s52, 0x10000\n"
  "v_exp_f32 v244, v244\n"
  "v_mfma_f32_16x16x32_bf16 v[248:251], a[132:135], v[40:43], v[248:251]\n"
  "ds_read_b128 a[156:159], v219 offset:12288\n"
  "v_cvt_pk_bf16_f32 v87, v166, v167\n"
  "v_mfma_f32_16x16x32_bf16 v[248:251], a[136:139], v[72:75], v[248:251]\n"
  "s_mov_b32 m0, s52\n"
  "v_exp_f32 v245, v245\n"
  "v_mfma_f32_16x16x32_bf16 v[248:251], a[140:143], v[104:107], v[248:251]\n"
  "v_cvt_pk_bf16_f32 v28, v244, v245\n"
  "v_mfma_f32_16x16x32_bf16 v[88:91], a[96:99], a[48:51], v[88:91]\n"
  "v_exp_f32 v246, v246\n"
  "v_mfma_f32_16x16x32_bf16 v[88:91], a[100:103], a[52:55], v[88:91]\n"
  "v_cvt_pk_bf16_f32 v116, v116, v117\n"
  "v_cvt_pk_bf16_f32 v117, v118, v119\n"
  "v_mfma_f32_16x16x32_bf16 v[168:171], a[104:107], a[48:51], v[168:171]\n"
  "v_exp_f32 v247, v247\n"
  "v_mfma_f32_16x16x32_bf16 v[168:171], a[108:111], a[52:55], v[168:171]\n"
  "v_cvt_pk_bf16_f32 v29, v246, v247\n"
  "v_cvt_pk_bf16_f32 v118, v180, v181\n"
  "s_waitcnt vmcnt(4)\n"
  "s_barrier\n"
  "v_mfma_f32_16x16x32_bf16 v[252:255], a[128:131], v[12:15], v[140:143]\n"
  "v_exp_f32 v248, v248\n"
  "v_mfma_f32_16x16x32_bf16 v[252:255], a[132:135], v[44:47], v[252:255]\n"
  "ds_read_b128 a[128:131], v216 offset:32768\n"
  "v_cvt_pk_bf16_f32 v119, v182, v183\n"
  "buffer_load_dwordx4 v229, s[24:27], s61 offen offset:1024 lds\n"
  "v_mfma_f32_16x16x32_bf16 v[252:255], a[136:139], v[76:79], v[252:255]\n"
  "v_exp_f32 v249, v249\n"
  "v_mfma_f32_16x16x32_bf16 v[252:255], a[140:143], v[108:111], v[252:255]\n"
  "ds_read_b64_tr_b16 a[160:161], v220 offset:16384\n"
  "v_cvt_pk_bf16_f32 v52, v248, v249\n"
  "v_mfma_f32_16x16x32_bf16 v[120:123], a[112:115], a[48:51], v[120:123]\n"
  "v_exp_f32 v250, v250\n"
  "v_mfma_f32_16x16x32_bf16 v[120:123], a[116:119], a[52:55], v[120:123]\n"
  "buffer_load_dwordx4 v228, s[24:27], s61 offen offset:0 lds\n"
  "v_cvt_pk_bf16_f32 v91, v90, v91\n"
  "v_cvt_pk_bf16_f32 v90, v88, v89\n"
  "v_mfma_f32_16x16x32_bf16 v[184:187], a[120:123], a[48:51], v[184:187]\n"
  "v_exp_f32 v251, v251\n"
  "v_mfma_f32_16x16x32_bf16 v[184:187], a[124:127], a[52:55], v[184:187]\n"
  "ds_read_b64_tr_b16 a[162:163], v220 offset:20480\n"
  "v_cvt_pk_bf16_f32 v53, v250, v251\n"
  "v_cvt_pk_bf16_f32 v88, v168, v169\n"
  "v_mfma_f32_16x16x32_bf16 v[240:243], a[144:147], v[0:3], v[128:131]\n"
  "s_waitcnt lgkmcnt(3)\n"
  "v_exp_f32 v252, v252\n"
  "v_mfma_f32_16x16x32_bf16 v[240:243], a[148:151], v[32:35], v[240:243]\n"
  "ds_read_b128 a[132:135], v217 offset:32768\n"
  "v_cvt_pk_bf16_f32 v89, v170, v171\n"
  "buffer_load_dwordx4 v231, s[24:27], s61 offen offset:3072 lds\n"
  "v_mfma_f32_16x16x32_bf16 v[240:243], a[152:155], v[64:67], v[240:243]\n"
  "v_exp_f32 v253, v253\n"
  "v_mfma_f32_16x16x32_bf16 v[240:243], a[156:159], v[96:99], v[240:243]\n"
  "ds_read_b64_tr_b16 a[164:165], v220 offset:24576\n"
  "v_cvt_pk_bf16_f32 v60, v252, v253\n"
  "v_mfma_f32_16x16x32_bf16 v[92:95], a[96:99], a[56:59], v[92:95]\n"
  "v_exp_f32 v254, v254\n"
  "v_mfma_f32_16x16x32_bf16 v[92:95], a[100:103], a[60:63], v[92:95]\n"
  "buffer_load_dwordx4 v230, s[24:27], s61 offen offset:2048 lds\n"
  "v_cvt_pk_bf16_f32 v120, v120, v121\n"
  "v_cvt_pk_bf16_f32 v121, v122, v123\n"
  "v_mfma_f32_16x16x32_bf16 v[172:175], a[104:107], a[56:59], v[172:175]\n"
  "v_exp_f32 v255, v255\n"
  "v_mfma_f32_16x16x32_bf16 v[172:175], a[108:111], a[60:63], v[172:175]\n"
  "ds_read_b64_tr_b16 a[166:167], v220 offset:28672\n"
  "v_cvt_pk_bf16_f32 v61, v254, v255\n"
  "v_cvt_pk_bf16_f32 v122, v184, v185\n"
  "v_mfma_f32_16x16x32_bf16 v[244:247], a[144:147], v[4:7], v[132:135]\n"
  "s_add_u32 m0, s52, 16384\n"
  "v_exp_f32 v240, v240\n"
  "v_mfma_f32_16x16x32_bf16 v[244:247], a[148:151], v[36:39], v[244:247]\n"
  "ds_read_b128 a[136:139], v218 offset:32768\n"
  "v_cvt_pk_bf16_f32 v123, v186, v187\n"
  "buffer_load_dwordx4 v233, s[28:31], s61 offen offset:1024 lds\n"
  "v_mfma_f32_16x16x32_bf16 v[244:247], a[152:155], v[68:71], v[244:247]\n"
  "v_exp_f32 v241, v241\n"
  "v_mfma_f32_16x16x32_bf16 v[244:247], a[156:159], v[100:103], v[244:247]\n"
  "ds_read_b64_tr_b16 a[168:169], v221 offset:16384\n"
  "v_cvt_pk_bf16_f32 v22, v240, v241\n"
  "v_mfma_f32_16x16x32_bf16 v[124:127], a[112:115], a[56:59], v[124:127]\n"
  "v_exp_f32 v242, v242\n"
  "v_mfma_f32_16x16x32_bf16 v[124:127], a[116:119], a[60:63], v[124:127]\n"
  "buffer_load_dwordx4 v232, s[28:31], s61 offen offset:0 lds\n"
  "v_cvt_pk_bf16_f32 v95, v94, v95\n"
  "v_cvt_pk_bf16_f32 v94, v92, v93\n"
  "v_mfma_f32_16x16x32_bf16 v[188:191], a[120:123], a[56:59], v[188:191]\n"
  "v_exp_f32 v243, v243\n"
  "v_mfma_f32_16x16x32_bf16 v[188:191], a[124:127], a[60:63], v[188:191]\n"
  "ds_read_b64_tr_b16 a[170:171], v221 offset:20480\n"
  "v_cvt_pk_bf16_f32 v23, v242, v243\n"
  "v_exp_f32 v244, v244\n"
  "v_mfma_f32_16x16x32_bf16 v[248:251], a[144:147], v[8:11], v[136:139]\n"
  "ds_read_b128 a[140:143], v219 offset:32768\n"
  "v_exp_f32 v245, v245\n"
  "v_exp_f32 v246, v246\n"
  "buffer_load_dwordx4 v235, s[28:31], s61 offen offset:3072 lds\n"
  "v_mfma_f32_16x16x32_bf16 v[248:251], a[148:151], v[40:43], v[248:251]\n"
  "ds_read_b64_tr_b16 a[172:173], v221 offset:24576\n"
  "v_cvt_pk_bf16_f32 v30, v244, v245\n"
  "v_exp_f32 v247, v247\n"
  "v_mfma_f32_16x16x32_bf16 v[248:251], a[152:155], v[72:75], v[248:251]\n"
  "buffer_load_dwordx4 v234, s[28:31], s61 offen offset:2048 lds\n"
  "v_cvt_pk_bf16_f32 v31, v246, v247\n"
  "v_cvt_pk_bf16_f32 v92, v172, v173\n"
  "v_mfma_f32_16x16x32_bf16 v[248:251], a[156:159], v[104:107], v[248:251]\n"
  "ds_read_b64_tr_b16 a[174:175], v221 offset:28672\n"
  "v_cvt_pk_bf16_f32 v93, v174, v175\n"
  "v_mfma_f32_16x16x32_bf16 v[252:255], a[144:147], v[12:15], v[140:143]\n"
  "ds_read_b128 a[144:147], v216 offset:36864\n"
  "v_cvt_pk_bf16_f32 v124, v124, v125\n"
  "v_cvt_pk_bf16_f32 v125, v126, v127\n"
  "v_mfma_f32_16x16x32_bf16 v[252:255], a[148:151], v[44:47], v[252:255]\n"
  "ds_read_b64_tr_b16 a[176:177], v222 offset:16384\n"
  "v_cvt_pk_bf16_f32 v126, v188, v189\n"
  "v_cvt_pk_bf16_f32 v127, v190, v191\n"
  "v_mfma_f32_16x16x32_bf16 v[252:255], a[152:155], v[76:79], v[252:255]\n"
  "v_exp_f32 v248, v248\n"
  "v_exp_f32 v249, v249\n"
  "v_mfma_f32_16x16x32_bf16 v[252:255], a[156:159], v[108:111], v[252:255]\n"
  "L_dry1_1:\n"
  "L_pad5:\n"
  "s_cbranch_execz L_dryr1\n"
  "ds_read_b64_tr_b16 a[178:179], v222 offset:20480\n"
  "v_exp_f32 v250, v250\n"
  "v_cvt_pk_bf16_f32 v54, v248, v249\n"
  "v_exp_f32 v251, v251\n"
  "s_nop 0\n"
  "v_cvt_pk_bf16_f32 v55, v250, v251\n"
  "L_p1:\n"
  "s_waitcnt lgkmcnt(7)\n"
  "v_mfma_f32_16x16x32_bf16 v[240:243], a[128:131], v[0:3], v[128:131]\n"
  "s_add_u32 s61, s61, s9\n"
  "v_exp_f32 v252, v252\n"
  "v_mfma_f32_16x16x32_bf16 v[240:243], a[132:135], v[32:35], v[240:243]\n"
  "L_dry1_2:\n"
  "L_pad6:\n"
  "s_cbranch_execz L_dryr1\n"
  "ds_read_b128 a[148:151], v217 offset:36864\n"
  "v_subrev_u32 v194, s51, v194\n"
  "v_mfma_f32_16x16x32_bf16 v[240:243], a[136:139], v[64:67], v[240:243]\n"
  "s_waitcnt lgkmcnt(5)\n"
  "s_and_b32 s61, s61, 0x7ffff\n"
  "v_exp_f32 v253, v253\n"
  "v_mfma_f32_16x16x32_bf16 v[240:243], a[140:143], v[96:99], v[240:243]\n"
  "ds_read_b64_tr_b16 a[180:181], v222 offset:24576\n"
  "v_cvt_pk_bf16_f32 v62, v252, v253\n"
  "v_mfma_f32_16x16x32_bf16 a[0:3], a[160:163], v[16:19], 0\n"
  "s_add_u32 m0, s52, 32768\n"
  "v_exp_f32 v254, v254\n"
  "v_mfma_f32_16x16x32_bf16 a[0:3], a[164:167], v[20:23], a[0:3]\n"
  "buffer_load_dwordx4 v228, s[24:27], s61 offen offset:0 lds\n"
  "v_mfma_f32_16x16x32_bf16 a[4:7], a[160:163], v[24:27], 0\n"
  "v_exp_f32 v255, v255\n"
  "v_mfma_f32_16x16x32_bf16 a[4:7], a[164:167], v[28:31], a[4:7]\n"
  "ds_read_b64_tr_b16 a[182:183], v222 offset:28672\n"
  "v_cvt_pk_bf16_f32 v63, v254, v255\n"
  "v_mfma_f32_16x16x32_bf16 v[244:247], a[128:131], v[4:7], v[132:135]\n"
  "v_exp_f32 v240, v240\n"
  "v_mfma_f32_16x16x32_bf16 v[244:247], a[132:135], v[36:39], v[244:247]\n"
  "ds_read_b128 a[152:155], v218 offset:36864\n"
  "v_subrev_u32 v195, s51, v195\n"
  "v_mfma_f32_16x16x32_bf16 v[244:247], a[136:139], v[68:71], v[244:247]\n"
  "v_exp_f32 v241, v241\n"
  "v_mfma_f32_16x16x32_bf16 v[244:247], a[140:143], v[100:103], v[244:247]\n"
  "ds_read_b64_tr_b16 a[184:185], v223 offset:16384\n"
  "v_cvt_pk_bf16_f32 v160, v240, v241\n"
  "v_mfma_f32_16x16x32_bf16 a[8:11], a[160:163], v[48:51], 0\n"
  "v_exp_f32 v242, v242\n"
  "v_mfma_f32_16x16x32_bf16 a[8:11], a[164:167], v[52:55], a[8:11]\n"
  "buffer_load_dwordx4 v229, s[24:27], s61 offen offset:1024 lds\n"
  "v_mfma_f32_16x16x32_bf16 a[12:15], a[160:163], v[56:59], 0\n"
  "v_exp_f32 v243, v243\n"
  "v_mfma_f32_16x16x32_bf16 a[12:15], a[164:167], v[60:63], a[12:15]\n"
  "ds_read_b64_tr_b16 a[186:187], v223 offset:20480\n"
  "v_cvt_pk_bf16_f32 v161, v242, v243\n"
  "v_mfma_f32_16x16x32_bf16 v[248:251], a[128:131], v[8:11], v[136:139]\n"
  "v_exp_f32 v244, v244\n"
  "v_mfma_f32_16x16x32_bf16 v[248:251], a[132:135], v[40:43], v[248:251]\n"
  "ds_read_b128 a[156:159], v219 offset:36864\n"
  "v_subrev_u32 v196, s51, v196\n"
  "v_mfma_f32_16x16x32_bf16 v[248:251], a[136:139], v[72:75], v[248:251]\n"
  "v_exp_f32 v245, v245\n"
  "v_mfma_f32_16x16x32_bf16 v[248:251], a[140:143], v[104:107], v[248:251]\n"
  "ds_read_b64_tr_b16 a[188:189], v223 offset:24576\n"
  "v_cvt_pk_bf16_f32 v168, v244, v245\n"
  "v_mfma_f32_16x16x32_bf16 a[16:19], a[168:171], v[16:19], 0\n"
  "s_waitcnt lgkmcnt(3)\n"
  "v_exp_f32 v246, v246\n"
  "v_mfma_f32_16x16x32_bf16 a[16:19], a[172:175], v[20:23], a[16:19]\n"
  "buffer_load_dwordx4 v230, s[24:27], s61 offen offset:2048 lds\n"
  "v_mfma_f32_16x16x32_bf16 a[20:23], a[168:171], v[24:27], 0\n"
  "v_exp_f32 v247, v247\n"
  "v_mfma_f32_16x16x32_bf16 a[20:23], a[172:175], v[28:31], a[20:23]\n"
  "ds_read_b64_tr_b16 a[190:191], v223 offset:28672\n"
  "v_cvt_pk_bf16_f32 v169, v246, v247\n"
  "v_smfmac_f32_16x16x64_bf16 a[240:243], v[144:147], v[16:23], v148\n"
  "v_mfma_f32_16x16x32_bf16 v[252:255], a[128:131], v[12:15], v[140:143]\n"
  "v_exp_f32 v248, v248\n"
  "v_mfma_f32_16x16x32_bf16 v[252:255], a[132:135], v[44:47], v[252:255]\n"
  "ds_read_b128 a[128:131], v216 offset:40960\n"
  "v_subrev_u32 v197, s51, v197\n"
  "v_mfma_f32_16x16x32_bf16 v[252:255], a[136:139], v[76:79], v[252:255]\n"
  "v_exp_f32 v249, v249\n"
  "v_mfma_f32_16x16x32_bf16 v[252:255], a[140:143], v[108:111], v[252:255]\n"
  "ds_read_b64_tr_b16 a[160:161], v224 offset:16384\n"
  "v_cvt_pk_bf16_f32 v176, v248, v249\n"
  "v_mfma_f32_16x16x32_bf16 a[24:27], a[168:171], v[48:51], 0\n"
  "v_exp_f32 v250, v250\n"
  "v_mfma_f32_16x16x32_bf16 a[24:27], a[172:175], v[52:55], a[24:27]\n"
  "buffer_load_dwordx4 v231, s[24:27], s61 offen offset:3072 lds\n"
  "v_mfma_f32_16x16x32_bf16 a[28:31], a[168:171], v[56:59], 0\n"
  "s_waitcnt lgkmcnt(2)\n"
  "v_exp_f32 v251, v251\n"
  "v_mfma_f32_16x16x32_bf16 a[28:31], a[172:175], v[60:63], a[28:31]\n"
  "ds_read_b64_tr_b16 a[162:163], v224 offset:20480\n"
  "v_cvt_pk_bf16_f32 v177, v250, v251\n"
  "v_mfma_f32_16x16x32_bf16 v[240:243], a[144:147], v[0:3], v[128:131]\n"
  "v_exp_f32 v252, v252\n"
  "v_mfma_f32_16x16x32_bf16 v[240:243], a[148:151], v[32:35], v[240:243]\n"
  "ds_read_b128 a[132:135], v217 offset:40960\n"
  "v_mov_b32 v156, 0x3f80\n"
  "v_mfma_f32_16x16x32_bf16 v[240:243], a[152:155], v[64:67], v[240:243]\n"
  "v_exp_f32 v253, v253\n"
  "v_mfma_f32_16x16x32_bf16 v[240:243], a[156:159], v[96:99], v[240:243]\n"
  "ds_read_b64_tr_b16 a[164:165], v224 offset:24576\n"
  "v_cvt_pk_bf16_f32 v184, v252, v253\n"
  "v_mfma_f32_16x16x32_bf16 a[32:35], a[176:179], v[16:19], 0\n"
  "v_exp_f32 v254, v254\n"
  "v_mfma_f32_16x16x32_bf16 a[32:35], a[180:183], v[20:23], a[32:35]\n"
  "v_mov_b32 v157, 1.0\n"
  "v_mov_b32 v152, 0\n"
  "v_mfma_f32_16x16x32_bf16 a[36:39], a[176:179], v[24:27], 0\n"
  "v_exp_f32 v255, v255\n"
  "v_mfma_f32_16x16x32_bf16 a[36:39], a[180:183], v[28:31], a[36:39]\n"
  "ds_read_b64_tr_b16 a[166:167], v224 offset:28672\n"
  "v_cvt_pk_bf16_f32 v185, v254, v255\n"
  "v_mfma_f32_16x16x32_bf16 v[244:247], a[144:147], v[4:7], v[132:135]\n"
  "v_exp_f32 v240, v240\n"
  "v_mfma_f32_16x16x32_bf16 v[244:247], a[148:151], v[36:39], v[244:247]\n"
  "ds_read_b128 a[136:139], v218 offset:40960\n"
  "v_mov_b32 v153, 0\n"
  "v_mfma_f32_16x16x32_bf16 v[244:247], a[152:155], v[68:71], v[244:247]\n"
  "v_exp_f32 v241, v241\n"
  "v_mfma_f32_16x16x32_bf16 v[244:247], a[156:159], v[100:103], v[244:247]\n"
  "ds_read_b64_tr_b16 a[168:169], v225 offset:16384\n"
  "v_cvt_pk_bf16_f32 v162, v240, v241\n"
  "v_mfma_f32_16x16x32_bf16 a[40:43], a[176:179], v[48:51], 0\n"
  "v_exp_f32 v242, v242\n"
  "v_mfma_f32_16x16x32_bf16 a[40:43], a[180:183], v[52:55], a[40:43]\n"
  "v_cmp_eq_u32 vcc, 0, v213\n"
  "s_nop 1\n"
  "v_cndmask_b32 v150, 0, v156, vcc\n"
  "v_mfma_f32_16x16x32_bf16 a[44:47], a[176:179], v[56:59], 0\n"
  "v_exp_f32 v243, v243\n"
  "v_mfma_f32_16x16x32_bf16 a[44:47], a[180:183], v[60:63], a[44:47]\n"
  "ds_read_b64_tr_b16 a[170:171], v225 offset:20480\n"
  "v_cvt_pk_bf16_f32 v163, v242, v243\n"
  "v_mfma_f32_16x16x32_bf16 v[248:251], a[144:147], v[8:11], v[136:139]\n"
  "v_exp_f32 v244, v244\n"
  "v_mfma_f32_16x16x32_bf16 v[248:251], a[148:151], v[40:43], v[248:251]\n"
  "ds_read_b128 a[140:143], v219 offset:40960\n"
  "v_cmp_eq_u32 vcc, 1, v213\n"
  "v_mfma_f32_16x16x32_bf16 v[248:251], a[152:155], v[72:75], v[248:251]\n"
  "v_exp_f32 v245, v245\n"
  "v_mfma_f32_16x16x32_bf16 v[248:251], a[156:159], v[104:107], v[248:251]\n"
  "ds_read_b64_tr_b16 a[172:173], v225 offset:24576\n"
  "v_cvt_pk_bf16_f32 v170, v244, v245\n"
  "v_mfma_f32_16x16x32_bf16 a[48:51], a[184:187], v[16:19], 0\n"
  "v_exp_f32 v246, v246\n"
  "v_mfma_f32_16x16x32_bf16 a[48:51], a[188:191], v[20:23], a[48:51]\n"
  "v_cndmask_b32 v150, v150, v157, vcc\n"
  "v_mov_b32 v154, v150\n"
  "v_mfma_f32_16x16x32_bf16 a[52:55], a[184:187], v[24:27], 0\n"
  "v_exp_f32 v247, v247\n"
  "v_mfma_f32_16x16x32_bf16 a[52:55], a[188:191], v[28:31], a[52:55]\n"
  "ds_read_b64_tr_b16 a[174:175], v225 offset:28672\n"
  "v_cvt_pk_bf16_f32 v171, v246, v247\n"
  "v_smfmac_f32_16x16x64_bf16 a[244:247], v[144:147], v[24:31], v148\n"
  "v_mfma_f32_16x16x32_bf16 v[252:255], a[144:147], v[12:15], v[140:143]\n"
  "v_exp_f32 v248, v248\n"
  "v_mfma_f32_16x16x32_bf16 v[252:255], a[148:151], v[44:47], v[252:255]\n"
  "ds_read_b128 a[144:147], v216 offset:45056\n"
  "v_cmp_eq_u32 vcc, 2, v213\n"
  "v_mfma_f32_16x16x32_bf16 v[252:255], a[152:155], v[76:79], v[252:255]\n"
  "v_exp_f32 v249, v249\n"
  "v_mfma_f32_16x16x32_bf16 v[252:255], a[156:159], v[108:111], v[252:255]\n"
  "ds_read_b64_tr_b16 a[176:177], v226 offset:16384\n"
  "v_cvt_pk_bf16_f32 v178, v248, v249\n"
  "v_mfma_f32_16x16x32_bf16 a[56:59], a[184:187], v[48:51], 0\n"
  "v_exp_f32 v250, v250\n"
  "v_mfma_f32_16x16x32_bf16 a[56:59], a[188:191], v[52:55], a[56:59]\n"
  "v_xor_b32 v216, 0x10000, v216\n"
  "v_cndmask_b32 v151, 0, v156, vcc\n"
  "v_cmp_eq_u32 vcc, 3, v213\n"
  "v_mfma_f32_16x16x32_bf16 a[60:63], a[184:187], v[56:59], 0\n"
  "s_waitcnt lgkmcnt(2)\n"
  "v_exp_f32 v251, v251\n"
  "v_mfma_f32_16x16x32_bf16 a[60:63], a[188:191], v[60:63], a[60:63]\n"
  "ds_read_b64_tr_b16 a[178:179], v226 offset:20480\n"
  "v_cvt_pk_bf16_f32 v179, v250, v251\n"
  "v_mfma_f32_16x16x32_bf16 v[240:243], a[128:131], v[0:3], v[128:131]\n"
  "v_exp_f32 v252, v252\n"
  "v_mfma_f32_16x16x32_bf16 v[240:243], a[132:135], v[32:35], v[240:243]\n"
  "ds_read_b128 a[148:151], v217 offset:45056\n"
  "v_cndmask_b32 v151, v151, v157, vcc\n"
  "v_mfma_f32_16x16x32_bf16 v[240:243], a[136:139], v[64:67], v[240:243]\n"
  "v_exp_f32 v253, v253\n"
  "v_mfma_f32_16x16x32_bf16 v[240:243], a[140:143], v[96:99], v[240:243]\n"
  "ds_read_b64_tr_b16 a[180:181], v226 offset:24576\n"
  "v_cvt_pk_bf16_f32 v186, v252, v253\n"
  "v_mfma_f32_16x16x32_bf16 a[64:67], a[160:163], v[16:19], 0\n"
  "v_exp_f32 v254, v254\n"
  "v_mfma_f32_16x16x32_bf16 a[64:67], a[164:167], v[20:23], a[64:67]\n"
  "v_xor_b32 v217, 0x10000, v217\n"
  "v_mov_b32 v155, v151\n"
  "v_mov_b32 v156, v150\n"
  "v_mfma_f32_16x16x32_bf16 a[68:71], a[160:163], v[24:27], 0\n"
  "v_exp_f32 v255, v255\n"
  "v_mfma_f32_16x16x32_bf16 a[68:71], a[164:167], v[28:31], a[68:71]\n"
  "ds_read_b64_tr_b16 a[182:183], v226 offset:28672\n"
  "v_cvt_pk_bf16_f32 v187, v254, v255\n"
  "v_mfma_f32_16x16x32_bf16 v[244:247], a[128:131], v[4:7], v[132:135]\n"
  "v_exp_f32 v240, v240\n"
  "v_mfma_f32_16x16x32_bf16 v[244:247], a[132:135], v[36:39], v[244:247]\n"
  "ds_read_b128 a[152:155], v218 offset:45056\n"
  "v_mov_b32 v157, v151\n"
  "v_mfma_f32_16x16x32_bf16 v[244:247], a[136:139], v[68:71], v[244:247]\n"
  "v_exp_f32 v241, v241\n"
  "v_mfma_f32_16x16x32_bf16 v[244:247], a[140:143], v[100:103], v[244:247]\n"
  "ds_read_b64_tr_b16 a[184:185], v227 offset:16384\n"
  "v_cvt_pk_bf16_f32 v164, v240, v241\n"
  "v_mfma_f32_16x16x32_bf16 a[72:75], a[160:163], v[48:51], 0\n"
  "v_exp_f32 v242, v242\n"
  "v_mfma_f32_16x16x32_bf16 a[72:75], a[164:167], v[52:55], a[72:75]\n"
  "v_xor_b32 v218, 0x10000, v218\n"
  "v_mfma_f32_16x16x32_bf16 a[76:79], a[160:163], v[56:59], 0\n"
  "v_exp_f32 v243, v243\n"
  "v_mfma_f32_16x16x32_bf16 a[76:79], a[164:167], v[60:63], a[76:79]\n"
  "ds_read_b64_tr_b16 a[186:187], v227 offset:20480\n"
  "v_cvt_pk_bf16_f32 v165, v242, v243\n"
  "v_mfma_f32_16x16x32_bf16 v[248:251], a[128:131], v[8:11], v[136:139]\n"
  "v_exp_f32 v244, v244\n"
  "v_mfma_f32_16x16x32_bf16 v[248:251], a[132:135], v[40:43], v[248:251]\n"
  "ds_read_b128 a[156:159], v219 offset:45056\n"
  "v_add_u32 v202, 65536, v202\n"
  "v_mfma_f32_16x16x32_bf16 v[248:251], a[136:139], v[72:75], v[248:251]\n"
  "s_add_u32 m0, s52, 49152\n"
  "v_exp_f32 v245, v245\n"
  "v_mfma_f32_16x16x32_bf16 v[248:251], a[140:143], v[104:107], v[248:251]\n"
  "ds_read_b64_tr_b16 a[188:189], v227 offset:24576\n"
  "v_cvt_pk_bf16_f32 v172, v244, v245\n"
  "v_mfma_f32_16x16x32_bf16 a[80:83], a[168:171], v[16:19], 0\n"
  "v_exp_f32 v246, v246\n"
  "v_mfma_f32_16x16x32_bf16 a[80:83], a[172:175], v[20:23], a[80:83]\n"
  "v_xor_b32 v219, 0x10000, v219\n"
  "v_mfma_f32_16x16x32_bf16 a[84:87], a[168:171], v[24:27], 0\n"
  "s_waitcnt vmcnt(8)\n"
  "v_exp_f32 v247, v247\n"
  "v_mfma_f32_16x16x32_bf16 a[84:87], a[172:175], v[28:31], a[84:87]\n"
  "ds_read_b64_tr_b16 a[190:191], v227 offset:28672\n"
  "v_cvt_pk_bf16_f32 v173, v246, v247\n"
  "v_smfmac_f32_16x16x64_bf16 a[248:251], v[144:147], v[48:55], v148\n"
  "s_barrier\n"
  "buffer_load_dwordx4 v232, s[28:31], s61 offen offset:0 lds\n"
  "v_mfma_f32_16x16x32_bf16 v[252:255], a[128:131], v[12:15], v[140:143]\n"
  "v_exp_f32 v248, v248\n"
  "v_mfma_f32_16x16x32_bf16 v[252:255], a[132:135], v[44:47], v[252:255]\n"
  "ds_read_b128 a[128:131], v216 offset:0\n"
  "v_add_u32 v203, 65536, v203\n"
  "v_mfma_f32_16x16x32_bf16 v[252:255], a[136:139], v[76:79], v[252:255]\n"
  "v_exp_f32 v249, v249\n"
  "v_mfma_f32_16x16x32_bf16 v[252:255], a[140:143], v[108:111], v[252:255]\n"
  "ds_read_b64_tr_b16 a[160:161], v220 offset:49152\n"
  "v_cvt_pk_bf16_f32 v180, v248, v249\n"
  "v_mfma_f32_16x16x32_bf16 a[88:91], a[168:171], v[48:51], 0\n"
  "v_exp_f32 v250, v250\n"
  "v_mfma_f32_16x16x32_bf16 a[88:91], a[172:175], v[52:55], a[88:91]\n"
  "buffer_load_dwordx4 v233, s[28:31], s61 offen offset:1024 lds\n"
  "v_mfma_f32_16x16x32_bf16 a[92:95], a[168:171], v[56:59], 0\n"
  "s_waitcnt lgkmcnt(2)\n"
  "v_exp_f32 v251, v251\n"
  "v_mfma_f32_16x16x32_bf16 a[92:95], a[172:175], v[60:63], a[92:95]\n"
  "ds_read_b64_tr_b16 a[162:163], v220 offset:53248\n"
  "v_cvt_pk_bf16_f32 v181, v250, v251\n"
  "v_mfma_f32_16x16x32_bf16 v[240:243], a[144:147], v[0:3], v[128:131]\n"
  "s_xor_b32 s52, s52, 0x10000\n"
  "v_exp_f32 v252, v252\n"
  "v_mfma_f32_16x16x32_bf16 v[240:243], a[148:151], v[32:35], v[240:243]\n"
  "ds_read_b128 a[132:135], v217 offset:0\n"
  "v_mfma_f32_16x16x32_bf16 v[240:243], a[152:155], v[64:67], v[240:243]\n"
  "v_exp_f32 v253, v253\n"
  "v_mfma_f32_16x16x32_bf16 v[240:243], a[156:159], v[96:99], v[240:243]\n"
  "ds_read_b64_tr_b16 a[164:165], v220 offset:57344\n"
  "v_cvt_pk_bf16_f32 v188, v252, v253\n"
  "v_mfma_f32_16x16x32_bf16 a[96:99], a[176:179], v[16:19], 0\n"
  "v_exp_f32 v254, v254\n"
  "v_mfma_f32_16x16x32_bf16 a[96:99], a[180:183], v[20:23], a[96:99]\n"
  "buffer_load_dwordx4 v234, s[28:31], s61 offen offset:2048 lds\n"
  "v_mfma_f32_16x16x32_bf16 a[100:103], a[176:179], v[24:27], 0\n"
  "v_exp_f32 v255, v255\n"
  "v_mfma_f32_16x16x32_bf16 a[100:103], a[180:183], v[28:31], a[100:103]\n"
  "ds_read_b64_tr_b16 a[166:167], v220 offset:61440\n"
  "v_cvt_pk_bf16_f32 v189, v254, v255\n"
  "v_mfma_f32_16x16x32_bf16 v[244:247], a[144:147], v[4:7], v[132:135]\n"
  "v_exp_f32 v240, v240\n"
  "v_mfma_f32_16x16x32_bf16 v[244:247], a[148:151], v[36:39], v[244:247]\n"
  "ds_read_b128 a[136:139], v218 offset:0\n"
  "v_xor_b32 v220, 0x10000, v220\n"
  "v_mfma_f32_16x16x32_bf16 v[244:247], a[152:155], v[68:71], v[244:247]\n"
  "v_exp_f32 v241, v241\n"
  "v_mfma_f32_16x16x32_bf16 v[244:247], a[156:159], v[100:103], v[244:247]\n"
  "ds_read_b64_tr_b16 a[168:169], v221 offset:49152\n"
  "v_cvt_pk_bf16_f32 v166, v240, v241\n"
  "v_mfma_f32_16x16x32_bf16 a[104:107], a[176:179], v[48:51], 0\n"
  "v_exp_f32 v242, v242\n"
  "v_mfma_f32_16x16x32_bf16 a[104:107], a[180:183], v[52:55], a[104:107]\n"
  "buffer_load_dwordx4 v235, s[28:31], s61 offen offset:3072 lds\n"
  "v_mfma_f32_16x16x32_bf16 a[108:111], a[176:179], v[56:59], 0\n"
  "v_exp_f32 v243, v243\n"
  "v_mfma_f32_16x16x32_bf16 a[108:111], a[180:183], v[60:63], a[108:111]\n"
  "ds_read_b64_tr_b16 a[170:171], v221 offset:53248\n"
  "v_cvt_pk_bf16_f32 v167, v242, v243\n"
  "v_mfma_f32_16x16x32_bf16 v[248:251], a[144:147], v[8:11], v[136:139]\n"
  "v_exp_f32 v244, v244\n"
  "v_mfma_f32_16x16x32_bf16 v[248:251], a[148:151], v[40:43], v[248:251]\n"
  "ds_read_b128 a[140:143], v219 offset:0\n"
  "v_mfma_f32_16x16x32_bf16 v[248:251], a[152:155], v[72:75], v[248:251]\n"
  "v_exp_f32 v245, v245\n"
  "v_mfma_f32_16x16x32_bf16 v[248:251], a[156:159], v[104:107], v[248:251]\n"
  "ds_read_b64_tr_b16 a[172:173], v221 offset:57344\n"
  "v_cvt_pk_bf16_f32 v174, v244, v245\n"
  "v_mfma_f32_16x16x32_bf16 a[112:115], a[184:187], v[16:19], 0\n"
  "v_exp_f32 v246, v246\n"
  "v_mfma_f32_16x16x32_bf16 a[112:115], a[188:191], v[20:23], a[112:115]\n"
  "s_mov_b64 exec, 0xffff\n"
  "buffer_load_dword v209, v237, s[32:35], 0 offen sc1\n"
  "s_mov_b64 exec, -1\n"
  "v_mfma_f32_16x16x32_bf16 a[116:119], a[184:187], v[24:27], 0\n"
  "v_exp_f32 v247, v247\n"
  "v_mfma_f32_16x16x32_bf16 a[116:119], a[188:191], v[28:31], a[116:119]\n"
  "ds_read_b64_tr_b16 a[174:175], v221 offset:61440\n"
  "v_cvt_pk_bf16_f32 v175, v246, v247\n"
  "v_mfma_f32_16x16x32_bf16 v[252:255], a[144:147], v[12:15], v[140:143]\n"
  "v_exp_f32 v248, v248\n"
  "v_mfma_f32_16x16x32_bf16 v[252:255], a[148:151], v[44:47], v[252:255]\n"
  "ds_read_b128 a[144:147], v216 offset:4096\n"
  "v_xor_b32 v221, 0x10000, v221\n"
  "v_mfma_f32_16x16x32_bf16 v[252:255], a[152:155], v[76:79], v[252:255]\n"
  "v_exp_f32 v249, v249\n"
  "v_mfma_f32_16x16x32_bf16 v[252:255], a[156:159], v[108:111], v[252:255]\n"
  "ds_read_b64_tr_b16 a[176:177], v222 offset:49152\n"
  "v_cvt_pk_bf16_f32 v182, v248, v249\n"
  "v_mfma_f32_16x16x32_bf16 a[120:123], a[184:187], v[48:51], 0\n"
  "v_exp_f32 v250, v250\n"
  "v_mfma_f32_16x16x32_bf16 a[120:123], a[188:191], v[52:55], a[120:123]\n"
  "v_mfma_f32_16x16x32_bf16 a[124:127], a[184:187], v[56:59], 0\n"
  "s_waitcnt lgkmcnt(2)\n"
  "v_exp_f32 v251, v251\n"
  "v_mfma_f32_16x16x32_bf16 a[124:127], a[188:191], v[60:63], a[124:127]\n"
  "ds_read_b64_tr_b16 a[178:179], v222 offset:53248\n"
  "v_cvt_pk_bf16_f32 v183, v250, v251\n"
  "v_smfmac_f32_16x16x64_bf16 a[252:255], v[144:147], v[56:63], v148\n"
  "s_branch L_att\n"
  ".p2align 12\n"
  "L_pad:\n"
  "L_dry1_3:\n"
  "L_pad7:\n"
  "s_cbranch_execz L_dryr1\n"
  "s_nop 0\n"
  "L_att:\n"
  "v_mfma_f32_16x16x32_bf16 v[240:243], a[128:131], v[0:3], v[128:131]\n"
  "s_add_u32 s61, s61, s9\n"
  "v_exp_f32 v252, v252\n"
  "v_mfma_f32_16x16x32_bf16 v[240:243], a[132:135], v[32:35], v[240:243]\n"
  "ds_read_b128 a[148:151], v217 offset:4096\n"
  "v_mfma_f32_16x16x32_bf16 v[240:243], a[136:139], v[64:67], v[240:243]\n"
  "s_and_b32 s61, s61, 0x7ffff\n"
  "v_exp_f32 v253, v253\n"
  "v_mfma_f32_16x16x32_bf16 v[240:243], a[140:143], v[96:99], v[240:243]\n"
  "ds_read_b64_tr_b16 a[180:181], v222 offset:57344\n"
  "v_cvt_pk_bf16_f32 v190, v252, v253\n"
  "v_mfma_f32_16x16x32_bf16 a[0:3], a[160:163], v[160:163], a[0:3]\n"
  "s_add_u32 m0, s52, 0\n"
  "v_exp_f32 v254, v254\n"
  "v_mfma_f32_16x16x32_bf16 a[0:3], a[164:167], v[164:167], a[0:3]\n"
  "buffer_load_dwordx4 v228, s[24:27], s61 offen offset:0 lds\n"
  "v_mfma_f32_16x16x32_bf16 a[4:7], a[160:163], v[168:171], a[4:7]\n"
  "v_exp_f32 v255, v255\n"
  "v_mfma_f32_16x16x32_bf16 a[4:7], a[164:167], v[172:175], a[4:7]\n"
  "ds_read_b64_tr_b16 a[182:183], v222 offset:61440\n"
  "v_cvt_pk_bf16_f32 v191, v254, v255\n"
  "v_mfma_f32_16x16x32_bf16 v[244:247], a[128:131], v[4:7], v[132:135]\n"
  "v_exp_f32 v240, v240\n"
  "v_mfma_f32_16x16x32_bf16 v[244:247], a[132:135], v[36:39], v[244:247]\n"
  "ds_read_b128 a[152:155], v218 offset:4096\n"
  "v_xor_b32 v222, 0x10000, v222\n"
  "v_mfma_f32_16x16x32_bf16 v[244:247], a[136:139], v[68:71], v[244:247]\n"
  "v_exp_f32 v241, v241\n"
  "v_mfma_f32_16x16x32_bf16 v[244:247], a[140:143], v[100:103], v[244:247]\n"
  "ds_read_b64_tr_b16 a[184:185], v223 offset:49152\n"
  "v_cvt_pk_bf16_f32 v16, v240, v241\n"
  "v_mfma_f32_16x16x32_bf16 a[8:11], a[160:163], v[176:179], a[8:11]\n"
  "v_exp_f32 v242, v242\n"
  "v_mfma_f32_16x16x32_bf16 a[8:11], a[164:167], v[180:183], a[8:11]\n"
  "buffer_load_dwordx4 v229, s[24:27], s61 offen offset:1024 lds\n"
  "v_mfma_f32_16x16x32_bf16 a[12:15], a[160:163], v[184:187], a[12:15]\n"
  "v_exp_f32 v243, v243\n"
  "v_mfma_f32_16x16x32_bf16 a[12:15], a[164:167], v[188:191], a[12:15]\n"
  "ds_read_b64_tr_b16 a[186:187], v223 offset:53248\n"
  "v_cvt_pk_bf16_f32 v17, v242, v243\n"
  "v_mfma_f32_16x16x32_bf16 v[248:251], a[128:131], v[8:11], v[136:139]\n"
  "v_exp_f32 v244, v244\n"
  "v_mfma_f32_16x16x32_bf16 v[248:251], a[132:135], v[40:43], v[248:251]\n"
  "ds_read_b128 a[156:159], v219 offset:4096\n"
  "v_mfma_f32_16x16x32_bf16 v[248:251], a[136:139], v[72:75], v[248:251]\n"
  "v_exp_f32 v245, v245\n"
  "v_mfma_f32_16x16x32_bf16 v[248:251], a[140:143], v[104:107], v[248:251]\n"
  "ds_read_b64_tr_b16 a[188:189], v223 offset:57344\n"
  "v_cvt_pk_bf16_f32 v24, v244, v245\n"
  "v_mfma_f32_16x16x32_bf16 a[16:19], a[168:171], v[160:163], a[16:19]\n"
  "v_exp_f32 v246, v246\n"
  "v_mfma_f32_16x16x32_bf16 a[16:19], a[172:175], v[164:167], a[16:19]\n"
  "buffer_load_dwordx4 v230, s[24:27], s61 offen offset:2048 lds\n"
  "v_mfma_f32_16x16x32_bf16 a[20:23], a[168:171], v[168:171], a[20:23]\n"
  "v_exp_f32 v247, v247\n"
  "v_mfma_f32_16x16x32_bf16 a[20:23], a[172:175], v[172:175], a[20:23]\n"
  "ds_read_b64_tr_b16 a[190:191], v223 offset:61440\n"
  "v_cvt_pk_bf16_f32 v25, v246, v247\n"
  "v_smfmac_f32_16x16x64_bf16 a[240:243], v[144:147], v[160:167], v148\n"
  "v_mfma_f32_16x16x32_bf16 v[252:255], a[128:131], v[12:15], v[140:143]\n"
  "v_exp_f32 v248, v248\n"
  "v_mfma_f32_16x16x32_bf16 v[252:255], a[132:135], v[44:47], v[252:255]\n"
  "ds_read_b128 a[128:131], v216 offset:8192\n"
  "v_xor_b32 v223, 0x10000, v223\n"
  "v_mfma_f32_16x16x32_bf16 v[252:255], a[136:139], v[76:79], v[252:255]\n"
  "v_exp_f32 v249, v249\n"
  "v_mfma_f32_16x16x32_bf16 v[252:255], a[140:143], v[108:111], v[252:255]\n"
  "ds_read_b64_tr_b16 a[160:161], v224 offset:49152\n"
  "v_cvt_pk_bf16_f32 v48, v248, v249\n"
  "v_mfma_f32_16x16x32_bf16 a[24:27], a[168:171], v[176:179], a[24:27]\n"
  "v_exp_f32 v250, v250\n"
  "v_mfma_f32_16x16x32_bf16 a[24:27], a[172:175], v[180:183], a[24:27]\n"
  "buffer_load_dwordx4 v231, s[24:27], s61 offen offset:3072 lds\n"
  "v_mfma_f32_16x16x32_bf16 a[28:31], a[168:171], v[184:187], a[28:31]\n"
  "s_waitcnt lgkmcnt(2)\n"
  "v_exp_f32 v251, v251\n"
  "v_mfma_f32_16x16x32_bf16 a[28:31], a[172:175], v[188:191], a[28:31]\n"
  "ds_read_b64_tr_b16 a[162:163], v224 offset:53248\n"
  "v_cvt_pk_bf16_f32 v49, v250, v251\n"
  "v_mfma_f32_16x16x32_bf16 v[240:243], a[144:147], v[0:3], v[128:131]\n"
  "v_exp_f32 v252, v252\n"
  "v_mfma_f32_16x16x32_bf16 v[240:243], a[148:151], v[32:35], v[240:243]\n"
  "ds_read_b128 a[132:135], v217 offset:8192\n"
  "v_mfma_f32_16x16x32_bf16 v[240:243], a[152:155], v[64:67], v[240:243]\n"
  "v_exp_f32 v253, v253\n"
  "v_mfma_f32_16x16x32_bf16 v[240:243], a[156:159], v[96:99], v[240:243]\n"
  "ds_read_b64_tr_b16 a[164:165], v224 offset:57344\n"
  "v_cvt_pk_bf16_f32 v56, v252, v253\n"
  "v_mfma_f32_16x16x32_bf16 a[32:35], a[176:179], v[160:163], a[32:35]\n"
  "v_exp_f32 v254, v254\n"
  "v_mfma_f32_16x16x32_bf16 a[32:35], a[180:183], v[164:167], a[32:35]\n"
  "v_mfma_f32_16x16x32_bf16 a[36:39], a[176:179], v[168:171], a[36:39]\n"
  "v_exp_f32 v255, v255\n"
  "v_mfma_f32_16x16x32_bf16 a[36:39], a[180:183], v[172:175], a[36:39]\n"
  "ds_read_b64_tr_b16 a[166:167], v224 offset:61440\n"
  "v_cvt_pk_bf16_f32 v57, v254, v255\n"
  "v_mfma_f32_16x16x32_bf16 v[244:247], a[144:147], v[4:7], v[132:135]\n"
  "v_exp_f32 v240, v240\n"
  "v_mfma_f32_16x16x32_bf16 v[244:247], a[148:151], v[36:39], v[244:247]\n"
  "ds_read_b128 a[136:139], v218 offset:8192\n"
  "v_xor_b32 v224, 0x10000, v224\n"
  "v_mfma_f32_16x16x32_bf16 v[244:247], a[152:155], v[68:71], v[244:247]\n"
  "v_exp_f32 v241, v241\n"
  "v_mfma_f32_16x16x32_bf16 v[244:247], a[156:159], v[100:103], v[244:247]\n"
  "ds_read_b64_tr_b16 a[168:169], v225 offset:49152\n"
  "v_cvt_pk_bf16_f32 v18, v240, v241\n"
  "v_mfma_f32_16x16x32_bf16 a[40:43], a[176:179], v[176:179], a[40:43]\n"
  "v_exp_f32 v242, v242\n"
  "v_mfma_f32_16x16x32_bf16 a[40:43], a[180:183], v[180:183], a[40:43]\n"
  "v_mfma_f32_16x16x32_bf16 a[44:47], a[176:179], v[184:187], a[44:47]\n"
  "v_exp_f32 v243, v243\n"
  "v_mfma_f32_16x16x32_bf16 a[44:47], a[180:183], v[188:191], a[44:47]\n"
  "ds_read_b64_tr_b16 a[170:171], v225 offset:53248\n"
  "v_cvt_pk_bf16_f32 v19, v242, v243\n"
  "v_mfma_f32_16x16x32_bf16 v[248:251], a[144:147], v[8:11], v[136:139]\n"
  "v_exp_f32 v244, v244\n"
  "v_mfma_f32_16x16x32_bf16 v[248:251], a[148:151], v[40:43], v[248:251]\n"
  "ds_read_b128 a[140:143], v219 offset:8192\n"
  "v_mfma_f32_16x16x32_bf16 v[248:251], a[152:155], v[72:75], v[248:251]\n"
  "v_exp_f32 v245, v245\n"
  "v_mfma_f32_16x16x32_bf16 v[248:251], a[156:159], v[104:107], v[248:251]\n"
  "ds_read_b64_tr_b16 a[172:173], v225 offset:57344\n"
  "v_cvt_pk_bf16_f32 v26, v244, v245\n"
  "v_mfma_f32_16x16x32_bf16 a[48:51], a[184:187], v[160:163], a[48:51]\n"
  "v_exp_f32 v246, v246\n"
  "v_mfma_f32_16x16x32_bf16 a[48:51], a[188:191], v[164:167], a[48:51]\n"
  "v_mfma_f32_16x16x32_bf16 a[52:55], a[184:187], v[168:171], a[52:55]\n"
  "v_exp_f32 v247, v247\n"
  "v_mfma_f32_16x16x32_bf16 a[52:55], a[188:191], v[172:175], a[52:55]\n"
  "ds_read_b64_tr_b16 a[174:175], v225 offset:61440\n"
  "v_cvt_pk_bf16_f32 v27, v246, v247\n"
  "v_smfmac_f32_16x16x64_bf16 a[244:247], v[144:147], v[168:175], v148\n"
  "v_mfma_f32_16x16x32_bf16 v[252:255], a[144:147], v[12:15], v[140:143]\n"
  "v_exp_f32 v248, v248\n"
  "v_mfma_f32_16x16x32_bf16 v[252:255], a[148:151], v[44:47], v[252:255]\n"
  "ds_read_b128 a[144:147], v216 offset:12288\n"
  "v_xor_b32 v225, 0x10000, v225\n"
  "v_mfma_f32_16x16x32_bf16 v[252:255], a[152:155], v[76:79], v[252:255]\n"
  "v_exp_f32 v249, v249\n"
  "v_mfma_f32_16x16x32_bf16 v[252:255], a[156:159], v[108:111], v[252:255]\n"
  "ds_read_b64_tr_b16 a[176:177], v226 offset:49152\n"
  "v_cvt_pk_bf16_f32 v50, v248, v249\n"
  "v_mfma_f32_16x16x32_bf16 a[56:59], a[184:187], v[176:179], a[56:59]\n"
  "v_exp_f32 v250, v250\n"
  "v_mfma_f32_16x16x32_bf16 a[56:59], a[188:191], v[180:183], a[56:59]\n"
  "ds_read_b128 a[156:159], v219 offset:12288\n"
  "v_mfma_f32_16x16x32_bf16 a[60:63], a[184:187], v[184:187], a[60:63]\n"
  "s_waitcnt lgkmcnt(3)\n"
  "v_exp_f32 v251, v251\n"
  "v_mfma_f32_16x16x32_bf16 a[60:63], a[188:191], v[188:191], a[60:63]\n"
  "ds_read_b64_tr_b16 a[178:179], v226 offset:53248\n"
  "v_cvt_pk_bf16_f32 v51, v250, v251\n"
  "v_mfma_f32_16x16x32_bf16 v[240:243], a[128:131], v[0:3], v[128:131]\n"
  "v_exp_f32 v252, v252\n"
  "v_mfma_f32_16x16x32_bf16 v[240:243], a[132:135], v[32:35], v[240:243]\n"
  "ds_read_b128 a[148:151], v217 offset:12288\n"
  "v_mfma_f32_16x16x32_bf16 v[240:243], a[136:139], v[64:67], v[240:243]\n"
  "v_exp_f32 v253, v253\n"
  "v_mfma_f32_16x16x32_bf16 v[240:243], a[140:143], v[96:99], v[240:243]\n"
  "ds_read_b64_tr_b16 a[180:181], v226 offset:57344\n"
  "v_cvt_pk_bf16_f32 v58, v252, v253\n"
  "v_mfma_f32_16x16x32_bf16 a[64:67], a[160:163], v[160:163], a[64:67]\n"
  "v_exp_f32 v254, v254\n"
  "v_mfma_f32_16x16x32_bf16 a[64:67], a[164:167], v[164:167], a[64:67]\n"
  "ds_read_b64_tr_b16 a[188:189], v227 offset:57344\n"
  "v_mfma_f32_16x16x32_bf16 a[68:71], a[160:163], v[168:171], a[68:71]\n"
  "v_exp_f32 v255, v255\n"
  "v_mfma_f32_16x16x32_bf16 a[68:71], a[164:167], v[172:175], a[68:71]\n"
  "ds_read_b64_tr_b16 a[182:183], v226 offset:61440\n"
  "v_cvt_pk_bf16_f32 v59, v254, v255\n"
  "v_mfma_f32_16x16x32_bf16 v[244:247], a[128:131], v[4:7], v[132:135]\n"
  "v_exp_f32 v240, v240\n"
  "v_mfma_f32_16x16x32_bf16 v[244:247], a[132:135], v[36:39], v[244:247]\n"
  "ds_read_b128 a[152:155], v218 offset:12288\n"
  "v_xor_b32 v226, 0x10000, v226\n"
  "v_mfma_f32_16x16x32_bf16 v[244:247], a[136:139], v[68:71], v[244:247]\n"
  "v_exp_f32 v241, v241\n"
  "v_mfma_f32_16x16x32_bf16 v[244:247], a[140:143], v[100:103], v[244:247]\n"
  "ds_read_b64_tr_b16 a[184:185], v227 offset:49152\n"
  "v_cvt_pk_bf16_f32 v20, v240, v241\n"
  "v_mfma_f32_16x16x32_bf16 a[72:75], a[160:163], v[176:179], a[72:75]\n"
  "v_exp_f32 v242, v242\n"
  "v_mfma_f32_16x16x32_bf16 a[72:75], a[164:167], v[180:183], a[72:75]\n"
  "ds_read_b64_tr_b16 a[190:191], v227 offset:61440\n"
  "v_mfma_f32_16x16x32_bf16 a[76:79], a[160:163], v[184:187], a[76:79]\n"
  "v_exp_f32 v243, v243\n"
  "v_mfma_f32_16x16x32_bf16 a[76:79], a[164:167], v[188:191], a[76:79]\n"
  "ds_read_b64_tr_b16 a[186:187], v227 offset:53248\n"
  "v_cvt_pk_bf16_f32 v21, v242, v243\n"
  "v_mfma_f32_16x16x32_bf16 v[248:251], a[128:131], v[8:11], v[136:139]\n"
  "v_exp_f32 v244, v244\n"
  "v_mfma_f32_16x16x32_bf16 v[248:251], a[132:135], v[40:43], v[248:251]\n"
  "v_mfma_f32_16x16x32_bf16 v[248:251], a[136:139], v[72:75], v[248:251]\n"
  "s_add_u32 m0, s52, 16384\n"
  "v_exp_f32 v245, v245\n"
  "v_mfma_f32_16x16x32_bf16 v[248:251], a[140:143], v[104:107], v[248:251]\n"
  "v_cvt_pk_bf16_f32 v28, v244, v245\n"
  "v_mfma_f32_16x16x32_bf16 a[80:83], a[168:171], v[160:163], a[80:83]\n"
  "v_exp_f32 v246, v246\n"
  "v_mfma_f32_16x16x32_bf16 a[80:83], a[172:175], v[164:167], a[80:83]\n"
  "v_mfma_f32_16x16x32_bf16 a[84:87], a[168:171], v[168:171], a[84:87]\n"
  "s_waitcnt vmcnt(0) lgkmcnt(0)\n"
  "v_exp_f32 v247, v247\n"
  "v_mfma_f32_16x16x32_bf16 a[84:87], a[172:175], v[172:175], a[84:87]\n"
  "v_cvt_pk_bf16_f32 v29, v246, v247\n"
  "v_smfmac_f32_16x16x64_bf16 a[248:251], v[144:147], v[176:183], v148\n"
  "s_barrier\n"
  "v_mfma_f32_16x16x32_bf16 v[252:255], a[128:131], v[12:15], v[140:143]\n"
  "v_exp_f32 v248, v248\n"
  "v_mfma_f32_16x16x32_bf16 v[252:255], a[132:135], v[44:47], v[252:255]\n"
  "ds_read_b128 a[128:131], v216 offset:32768\n"
  "v_xor_b32 v227, 0x10000, v227\n"
  "v_mfma_f32_16x16x32_bf16 v[252:255], a[136:139], v[76:79], v[252:255]\n"
  "v_exp_f32 v249, v249\n"
  "v_mfma_f32_16x16x32_bf16 v[252:255], a[140:143], v[108:111], v[252:255]\n"
  "ds_read_b64_tr_b16 a[160:161], v220 offset:16384\n"
  "v_cvt_pk_bf16_f32 v52, v248, v249\n"
  "v_mfma_f32_16x16x32_bf16 a[88:91], a[168:171], v[176:179], a[88:91]\n"
  "v_exp_f32 v250, v250\n"
  "v_mfma_f32_16x16x32_bf16 a[88:91], a[172:175], v[180:183], a[88:91]\n"
  "buffer_load_dwordx4 v232, s[28:31], s61 offen offset:0 lds\n"
  "v_mfma_f32_16x16x32_bf16 a[92:95], a[168:171], v[184:187], a[92:95]\n"
  "v_exp_f32 v251, v251\n"
  "v_mfma_f32_16x16x32_bf16 a[92:95], a[172:175], v[188:191], a[92:95]\n"
  "ds_read_b64_tr_b16 a[162:163], v220 offset:20480\n"
  "v_cvt_pk_bf16_f32 v53, v250, v251\n"
  "v_mfma_f32_16x16x32_bf16 v[240:243], a[144:147], v[0:3], v[128:131]\n"
  "v_exp_f32 v252, v252\n"
  "v_mfma_f32_16x16x32_bf16 v[240:243], a[148:151], v[32:35], v[240:243]\n"
  "ds_read_b128 a[132:135], v217 offset:32768\n"
  "v_mfma_f32_16x16x32_bf16 v[240:243], a[152:155], v[64:67], v[240:243]\n"
  "v_exp_f32 v253, v253\n"
  "v_mfma_f32_16x16x32_bf16 v[240:243], a[156:159], v[96:99], v[240:243]\n"
  "ds_read_b64_tr_b16 a[164:165], v220 offset:24576\n"
  "v_cvt_pk_bf16_f32 v60, v252, v253\n"
  "v_mfma_f32_16x16x32_bf16 a[96:99], a[176:179], v[160:163], a[96:99]\n"
  "v_exp_f32 v254, v254\n"
  "v_mfma_f32_16x16x32_bf16 a[96:99], a[180:183], v[164:167], a[96:99]\n"
  "buffer_load_dwordx4 v233, s[28:31], s61 offen offset:1024 lds\n"
  "v_mfma_f32_16x16x32_bf16 a[100:103], a[176:179], v[168:171], a[100:103]\n"
  "v_exp_f32 v255, v255\n"
  "v_mfma_f32_16x16x32_bf16 a[100:103], a[180:183], v[172:175], a[100:103]\n"
  "ds_read_b64_tr_b16 a[166:167], v220 offset:28672\n"
  "v_cvt_pk_bf16_f32 v61, v254, v255\n"
  "v_mfma_f32_16x16x32_bf16 v[244:247], a[144:147], v[4:7], v[132:135]\n"
  "v_exp_f32 v240, v240\n"
  "v_mfma_f32_16x16x32_bf16 v[244:247], a[148:151], v[36:39], v[244:247]\n"
  "ds_read_b128 a[136:139], v218 offset:32768\n"
  "v_mfma_f32_16x16x32_bf16 v[244:247], a[152:155], v[68:71], v[244:247]\n"
  "v_exp_f32 v241, v241\n"
  "v_mfma_f32_16x16x32_bf16 v[244:247], a[156:159], v[100:103], v[244:247]\n"
  "ds_read_b64_tr_b16 a[168:169], v221 offset:16384\n"
  "v_cvt_pk_bf16_f32 v22, v240, v241\n"
  "v_mfma_f32_16x16x32_bf16 a[104:107], a[176:179], v[176:179], a[104:107]\n"
  "v_exp_f32 v242, v242\n"
  "v_mfma_f32_16x16x32_bf16 a[104:107], a[180:183], v[180:183], a[104:107]\n"
  "buffer_load_dwordx4 v234, s[28:31], s61 offen offset:2048 lds\n"
  "v_mfma_f32_16x16x32_bf16 a[108:111], a[176:179], v[184:187], a[108:111]\n"
  "v_exp_f32 v243, v243\n"
  "v_mfma_f32_16x16x32_bf16 a[108:111], a[180:183], v[188:191], a[108:111]\n"
  "ds_read_b64_tr_b16 a[170:171], v221 offset:20480\n"
  "v_cvt_pk_bf16_f32 v23, v242, v243\n"
  "v_mfma_f32_16x16x32_bf16 v[248:251], a[144:147], v[8:11], v[136:139]\n"
  "v_exp_f32 v244, v244\n"
  "v_mfma_f32_16x16x32_bf16 v[248:251], a[148:151], v[40:43], v[248:251]\n"
  "ds_read_b128 a[140:143], v219 offset:32768\n"
  "v_mfma_f32_16x16x32_bf16 v[248:251], a[152:155], v[72:75], v[248:251]\n"
  "v_exp_f32 v245, v245\n"
  "v_mfma_f32_16x16x32_bf16 v[248:251], a[156:159], v[104:107], v[248:251]\n"
  "ds_read_b64_tr_b16 a[172:173], v221 offset:24576\n"
  "v_cvt_pk_bf16_f32 v30, v244, v245\n"
  "v_mfma_f32_16x16x32_bf16 a[112:115], a[184:187], v[160:163], a[112:115]\n"
  "v_exp_f32 v246, v246\n"
  "v_mfma_f32_16x16x32_bf16 a[112:115], a[188:191], v[164:167], a[112:115]\n"
  "buffer_load_dwordx4 v235, s[28:31], s61 offen offset:3072 lds\n"
  "v_mfma_f32_16x16x32_bf16 a[116:119], a[184:187], v[168:171], a[116:119]\n"
  "v_exp_f32 v247, v247\n"
  "v_mfma_f32_16x16x32_bf16 a[116:119], a[188:191], v[172:175], a[116:119]\n"
  "ds_read_b64_tr_b16 a[174:175], v221 offset:28672\n"
  "v_cvt_pk_bf16_f32 v31, v246, v247\n"
  "v_mfma_f32_16x16x32_bf16 v[252:255], a[144:147], v[12:15], v[140:143]\n"
  "v_exp_f32 v248, v248\n"
  "v_mfma_f32_16x16x32_bf16 v[252:255], a[148:151], v[44:47], v[252:255]\n"
  "ds_read_b128 a[144:147], v216 offset:36864\n"
  "v_mfma_f32_16x16x32_bf16 v[252:255], a[152:155], v[76:79], v[252:255]\n"
  "v_exp_f32 v249, v249\n"
  "v_mfma_f32_16x16x32_bf16 v[252:255], a[156:159], v[108:111], v[252:255]\n"
  "ds_read_b64_tr_b16 a[176:177], v222 offset:16384\n"
  "v_cvt_pk_bf16_f32 v54, v248, v249\n"
  "v_mfma_f32_16x16x32_bf16 a[120:123], a[184:187], v[176:179], a[120:123]\n"
  "v_exp_f32 v250, v250\n"
  "v_mfma_f32_16x16x32_bf16 a[120:123], a[188:191], v[180:183], a[120:123]\n"
  "v_mfma_f32_16x16x32_bf16 a[124:127], a[184:187], v[184:187], a[124:127]\n"
  "s_waitcnt lgkmcnt(2)\n"
  "v_exp_f32 v251, v251\n"
  "v_mfma_f32_16x16x32_bf16 a[124:127], a[188:191], v[188:191], a[124:127]\n"
  "ds_read_b64_tr_b16 a[178:179], v222 offset:20480\n"
  "v_cvt_pk_bf16_f32 v55, v250, v251\n"
  "v_smfmac_f32_16x16x64_bf16 a[252:255], v[144:147], v[184:191], v148\n"
  "v_mfma_f32_16x16x32_bf16 v[240:243], a[128:131], v[0:3], v[128:131]\n"
  "s_add_u32 s61, s61, s9\n"
  "v_exp_f32 v252, v252\n"
  "v_mfma_f32_16x16x32_bf16 v[240:243], a[132:135], v[32:35], v[240:243]\n"
  "ds_read_b128 a[148:151], v217 offset:36864\n"
  "v_mfma_f32_16x16x32_bf16 v[240:243], a[136:139], v[64:67], v[240:243]\n"
  "s_and_b32 s61, s61, 0x7ffff\n"
  "v_exp_f32 v253, v253\n"
  "v_mfma_f32_16x16x32_bf16 v[240:243], a[140:143], v[96:99], v[240:243]\n"
  "ds_read_b64_tr_b16 a[180:181], v222 offset:24576\n"
  "v_cvt_pk_bf16_f32 v62, v252, v253\n"
  "v_mfma_f32_16x16x32_bf16 a[0:3], a[160:163], v[16:19], a[0:3]\n"
  "s_add_u32 m0, s52, 32768\n"
  "v_exp_f32 v254, v254\n"
  "v_mfma_f32_16x16x32_bf16 a[0:3], a[164:167], v[20:23], a[0:3]\n"
  "buffer_load_dwordx4 v228, s[24:27], s61 offen offset:0 lds\n"
  "v_mfma_f32_16x16x32_bf16 a[4:7], a[160:163], v[24:27], a[4:7]\n"
  "v_exp_f32 v255, v255\n"
  "v_mfma_f32_16x16x32_bf16 a[4:7], a[164:167], v[28:31], a[4:7]\n"
  "ds_read_b64_tr_b16 a[182:183], v222 offset:28672\n"
  "v_cvt_pk_bf16_f32 v63, v254, v255\n"
  "v_mfma_f32_16x16x32_bf16 v[244:247], a[128:131], v[4:7], v[132:135]\n"
  "v_exp_f32 v240, v240\n"
  "v_mfma_f32_16x16x32_bf16 v[244:247], a[132:135], v[36:39], v[244:247]\n"
  "ds_read_b128 a[152:155], v218 offset:36864\n"
  "v_mfma_f32_16x16x32_bf16 v[244:247], a[136:139], v[68:71], v[244:247]\n"
  "v_exp_f32 v241, v241\n"
  "v_mfma_f32_16x16x32_bf16 v[244:247], a[140:143], v[100:103], v[244:247]\n"
  "ds_read_b64_tr_b16 a[184:185], v223 offset:16384\n"
  "v_cvt_pk_bf16_f32 v160, v240, v241\n"
  "v_mfma_f32_16x16x32_bf16 a[8:11], a[160:163], v[48:51], a[8:11]\n"
  "v_exp_f32 v242, v242\n"
  "v_mfma_f32_16x16x32_bf16 a[8:11], a[164:167], v[52:55], a[8:11]\n"
  "buffer_load_dwordx4 v229, s[24:27], s61 offen offset:1024 lds\n"
  "v_mfma_f32_16x16x32_bf16 a[12:15], a[160:163], v[56:59], a[12:15]\n"
  "v_exp_f32 v243, v243\n"
  "v_mfma_f32_16x16x32_bf16 a[12:15], a[164:167], v[60:63], a[12:15]\n"
  "ds_read_b64_tr_b16 a[186:187], v223 offset:20480\n"
  "v_cvt_pk_bf16_f32 v161, v242, v243\n"
  "v_mfma_f32_16x16x32_bf16 v[248:251], a[128:131], v[8:11], v[136:139]\n"
  "v_exp_f32 v244, v244\n"
  "v_mfma_f32_16x16x32_bf16 v[248:251], a[132:135], v[40:43], v[248:251]\n"
  "ds_read_b128 a[156:159], v219 offset:36864\n"
  "v_mfma_f32_16x16x32_bf16 v[248:251], a[136:139], v[72:75], v[248:251]\n"
  "v_exp_f32 v245, v245\n"
  "v_mfma_f32_16x16x32_bf16 v[248:251], a[140:143], v[104:107], v[248:251]\n"
  "ds_read_b64_tr_b16 a[188:189], v223 offset:24576\n"
  "v_cvt_pk_bf16_f32 v168, v244, v245\n"
  "v_mfma_f32_16x16x32_bf16 a[16:19], a[168:171], v[16:19], a[16:19]\n"
  "v_exp_f32 v246, v246\n"
  "v_mfma_f32_16x16x32_bf16 a[16:19], a[172:175], v[20:23], a[16:19]\n"
  "buffer_load_dwordx4 v230, s[24:27], s61 offen offset:2048 lds\n"
  "v_mfma_f32_16x16x32_bf16 a[20:23], a[168:171], v[24:27], a[20:23]\n"
  "v_exp_f32 v247, v247\n"
  "v_mfma_f32_16x16x32_bf16 a[20:23], a[172:175], v[28:31], a[20:23]\n"
  "ds_read_b64_tr_b16 a[190:191], v223 offset:28672\n"
  "v_cvt_pk_bf16_f32 v169, v246, v247\n"
  "v_smfmac_f32_16x16x64_bf16 a[240:243], v[144:147], v[16:23], v148\n"
  "v_mfma_f32_16x16x32_bf16 v[252:255], a[128:131], v[12:15], v[140:143]\n"
  "v_exp_f32 v248, v248\n"
  "v_mfma_f32_16x16x32_bf16 v[252:255], a[132:135], v[44:47], v[252:255]\n"
  "ds_read_b128 a[128:131], v216 offset:40960\n"
  "v_mfma_f32_16x16x32_bf16 v[252:255], a[136:139], v[76:79], v[252:255]\n"
  "v_exp_f32 v249, v249\n"
  "v_mfma_f32_16x16x32_bf16 v[252:255], a[140:143], v[108:111], v[252:255]\n"
  "ds_read_b64_tr_b16 a[160:161], v224 offset:16384\n"
  "v_cvt_pk_bf16_f32 v176, v248, v249\n"
  "v_mfma_f32_16x16x32_bf16 a[24:27], a[168:171], v[48:51], a[24:27]\n"
  "v_exp_f32 v250, v250\n"
  "v_mfma_f32_16x16x32_bf16 a[24:27], a[172:175], v[52:55], a[24:27]\n"
  "buffer_load_dwordx4 v231, s[24:27], s61 offen offset:3072 lds\n"
  "v_mfma_f32_16x16x32_bf16 a[28:31], a[168:171], v[56:59], a[28:31]\n"
  "s_waitcnt lgkmcnt(2)\n"
  "v_exp_f32 v251, v251\n"
  "v_mfma_f32_16x16x32_bf16 a[28:31], a[172:175], v[60:63], a[28:31]\n"
  "ds_read_b64_tr_b16 a[162:163], v224 offset:20480\n"
  "v_cvt_pk_bf16_f32 v177, v250, v251\n"
  "v_mfma_f32_16x16x32_bf16 v[240:243], a[144:147], v[0:3], v[128:131]\n"
  "v_exp_f32 v252, v252\n"
  "v_mfma_f32_16x16x32_bf16 v[240:243], a[148:151], v[32:35], v[240:243]\n"
  "ds_read_b128 a[132:135], v217 offset:40960\n"
  "v_mfma_f32_16x16x32_bf16 v[240:243], a[152:155], v[64:67], v[240:243]\n"
  "s_add_u32 m0, s52, 49152\n"
  "v_exp_f32 v253, v253\n"
  "v_mfma_f32_16x16x32_bf16 v[240:243], a[156:159], v[96:99], v[240:243]\n"
  "ds_read_b64_tr_b16 a[164:165], v224 offset:24576\n"
  "v_cvt_pk_bf16_f32 v184, v252, v253\n"
  "v_mfma_f32_16x16x32_bf16 a[32:35], a[176:179], v[16:19], a[32:35]\n"
  "v_exp_f32 v254, v254\n"
  "v_mfma_f32_16x16x32_bf16 a[32:35], a[180:183], v[20:23], a[32:35]\n"
  "buffer_load_dwordx4 v232, s[28:31], s61 offen offset:0 lds\n"
  "v_mfma_f32_16x16x32_bf16 a[36:39], a[176:179], v[24:27], a[36:39]\n"
  "v_exp_f32 v255, v255\n"
  "v_mfma_f32_16x16x32_bf16 a[36:39], a[180:183], v[28:31], a[36:39]\n"
  "ds_read_b64_tr_b16 a[166:167], v224 offset:28672\n"
  "v_cvt_pk_bf16_f32 v185, v254, v255\n"
  "v_mfma_f32_16x16x32_bf16 v[244:247], a[144:147], v[4:7], v[132:135]\n"
  "v_exp_f32 v240, v240\n"
  "v_mfma_f32_16x16x32_bf16 v[244:247], a[148:151], v[36:39], v[244:247]\n"
  "ds_read_b128 a[136:139], v218 offset:40960\n"
  "v_mfma_f32_16x16x32_bf16 v[244:247], a[152:155], v[68:71], v[244:247]\n"
  "v_exp_f32 v241, v241\n"
  "v_mfma_f32_16x16x32_bf16 v[244:247], a[156:159], v[100:103], v[244:247]\n"
  "ds_read_b64_tr_b16 a[168:169], v225 offset:16384\n"
  "v_cvt_pk_bf16_f32 v162, v240, v241\n"
  "v_mfma_f32_16x16x32_bf16 a[40:43], a[176:179], v[48:51], a[40:43]\n"
  "v_exp_f32 v242, v242\n"
  "v_mfma_f32_16x16x32_bf16 a[40:43], a[180:183], v[52:55], a[40:43]\n"
  "buffer_load_dwordx4 v233, s[28:31], s61 offen offset:1024 lds\n"
  "v_mfma_f32_16x16x32_bf16 a[44:47], a[176:179], v[56:59], a[44:47]\n"
  "v_exp_f32 v243, v243\n"
  "v_mfma_f32_16x16x32_bf16 a[44:47], a[180:183], v[60:63], a[44:47]\n"
  "ds_read_b64_tr_b16 a[170:171], v225 offset:20480\n"
  "v_cvt_pk_bf16_f32 v163, v242, v243\n"
  "v_mfma_f32_16x16x32_bf16 v[248:251], a[144:147], v[8:11], v[136:139]\n"
  "v_exp_f32 v244, v244\n"
  "v_mfma_f32_16x16x32_bf16 v[248:251], a[148:151], v[40:43], v[248:251]\n"
  "ds_read_b128 a[140:143], v219 offset:40960\n"
  "v_mfma_f32_16x16x32_bf16 v[248:251], a[152:155], v[72:75], v[248:251]\n"
  "v_exp_f32 v245, v245\n"
  "v_mfma_f32_16x16x32_bf16 v[248:251], a[156:159], v[104:107], v[248:251]\n"
  "ds_read_b64_tr_b16 a[172:173], v225 offset:24576\n"
  "v_cvt_pk_bf16_f32 v170, v244, v245\n"
  "v_mfma_f32_16x16x32_bf16 a[48:51], a[184:187], v[16:19], a[48:51]\n"
  "v_exp_f32 v246, v246\n"
  "v_mfma_f32_16x16x32_bf16 a[48:51], a[188:191], v[20:23], a[48:51]\n"
  "buffer_load_dwordx4 v234, s[28:31], s61 offen offset:2048 lds\n"
  "v_mfma_f32_16x16x32_bf16 a[52:55], a[184:187], v[24:27], a[52:55]\n"
  "v_exp_f32 v247, v247\n"
  "v_mfma_f32_16x16x32_bf16 a[52:55], a[188:191], v[28:31], a[52:55]\n"
  "ds_read_b64_tr_b16 a[174:175], v225 offset:28672\n"
  "v_cvt_pk_bf16_f32 v171, v246, v247\n"
  "v_smfmac_f32_16x16x64_bf16 a[244:247], v[144:147], v[24:31], v148\n"
  "v_mfma_f32_16x16x32_bf16 v[252:255], a[144:147], v[12:15], v[140:143]\n"
  "v_exp_f32 v248, v248\n"
  "v_mfma_f32_16x16x32_bf16 v[252:255], a[148:151], v[44:47], v[252:255]\n"
  "ds_read_b128 a[144:147], v216 offset:45056\n"
  "v_xor_b32 v216, 0x10000, v216\n"
  "v_mfma_f32_16x16x32_bf16 v[252:255], a[152:155], v[76:79], v[252:255]\n"
  "v_exp_f32 v249, v249\n"
  "v_mfma_f32_16x16x32_bf16 v[252:255], a[156:159], v[108:111], v[252:255]\n"
  "ds_read_b64_tr_b16 a[176:177], v226 offset:16384\n"
  "v_cvt_pk_bf16_f32 v178, v248, v249\n"
  "v_mfma_f32_16x16x32_bf16 a[56:59], a[184:187], v[48:51], a[56:59]\n"
  "v_exp_f32 v250, v250\n"
  "v_mfma_f32_16x16x32_bf16 a[56:59], a[188:191], v[52:55], a[56:59]\n"
  "buffer_load_dwordx4 v235, s[28:31], s61 offen offset:3072 lds\n"
  "v_mfma_f32_16x16x32_bf16 a[60:63], a[184:187], v[56:59], a[60:63]\n"
  "s_waitcnt lgkmcnt(2)\n"
  "v_exp_f32 v251, v251\n"
  "v_mfma_f32_16x16x32_bf16 a[60:63], a[188:191], v[60:63], a[60:63]\n"
  "ds_read_b64_tr_b16 a[178:179], v226 offset:20480\n"
  "v_cvt_pk_bf16_f32 v179, v250, v251\n"
  "v_mfma_f32_16x16x32_bf16 v[240:243], a[128:131], v[0:3], v[128:131]\n"
  "v_exp_f32 v252, v252\n"
  "v_mfma_f32_16x16x32_bf16 v[240:243], a[132:135], v[32:35], v[240:243]\n"
  "ds_read_b128 a[148:151], v217 offset:45056\n"
  "v_xor_b32 v217, 0x10000, v217\n"
  "v_mfma_f32_16x16x32_bf16 v[240:243], a[136:139], v[64:67], v[240:243]\n"
  "v_exp_f32 v253, v253\n"
  "v_mfma_f32_16x16x32_bf16 v[240:243], a[140:143], v[96:99], v[240:243]\n"
  "ds_read_b64_tr_b16 a[180:181], v226 offset:24576\n"
  "v_cvt_pk_bf16_f32 v186, v252, v253\n"
  "v_mfma_f32_16x16x32_bf16 a[64:67], a[160:163], v[16:19], a[64:67]\n"
  "v_exp_f32 v254, v254\n"
  "v_mfma_f32_16x16x32_bf16 a[64:67], a[164:167], v[20:23], a[64:67]\n"
  "v_mfma_f32_16x16x32_bf16 a[68:71], a[160:163], v[24:27], a[68:71]\n"
  "v_exp_f32 v255, v255\n"
  "v_mfma_f32_16x16x32_bf16 a[68:71], a[164:167], v[28:31], a[68:71]\n"
  "ds_read_b64_tr_b16 a[182:183], v226 offset:28672\n"
  "v_cvt_pk_bf16_f32 v187, v254, v255\n"
  "v_mfma_f32_16x16x32_bf16 v[244:247], a[128:131], v[4:7], v[132:135]\n"
  "v_exp_f32 v240, v240\n"
  "v_mfma_f32_16x16x32_bf16 v[244:247], a[132:135], v[36:39], v[244:247]\n"
  "ds_read_b128 a[152:155], v218 offset:45056\n"
  "v_xor_b32 v218, 0x10000, v218\n"
  "v_mfma_f32_16x16x32_bf16 v[244:247], a[136:139], v[68:71], v[244:247]\n"
  "v_exp_f32 v241, v241\n"
  "v_mfma_f32_16x16x32_bf16 v[244:247], a[140:143], v[100:103], v[244:247]\n"
  "ds_read_b64_tr_b16 a[184:185], v227 offset:16384\n"
  "v_cvt_pk_bf16_f32 v164, v240, v241\n"
  "v_mfma_f32_16x16x32_bf16 a[72:75], a[160:163], v[48:51], a[72:75]\n"
  "v_exp_f32 v242, v242\n"
  "v_mfma_f32_16x16x32_bf16 a[72:75], a[164:167], v[52:55], a[72:75]\n"
  "v_mfma_f32_16x16x32_bf16 a[76:79], a[160:163], v[56:59], a[76:79]\n"
  "v_exp_f32 v243, v243\n"
  "v_mfma_f32_16x16x32_bf16 a[76:79], a[164:167], v[60:63], a[76:79]\n"
  "ds_read_b64_tr_b16 a[186:187], v227 offset:20480\n"
  "v_cvt_pk_bf16_f32 v165, v242, v243\n"
  "v_mfma_f32_16x16x32_bf16 v[248:251], a[128:131], v[8:11], v[136:139]\n"
  "v_exp_f32 v244, v244\n"
  "v_mfma_f32_16x16x32_bf16 v[248:251], a[132:135], v[40:43], v[248:251]\n"
  "ds_read_b128 a[156:159], v219 offset:45056\n"
  "v_xor_b32 v219, 0x10000, v219\n"
  "v_mfma_f32_16x16x32_bf16 v[248:251], a[136:139], v[72:75], v[248:251]\n"
  "v_exp_f32 v245, v245\n"
  "v_mfma_f32_16x16x32_bf16 v[248:251], a[140:143], v[104:107], v[248:251]\n"
  "ds_read_b64_tr_b16 a[188:189], v227 offset:24576\n"
  "v_cvt_pk_bf16_f32 v172, v244, v245\n"
  "v_mfma_f32_16x16x32_bf16 a[80:83], a[168:171], v[16:19], a[80:83]\n"
  "v_exp_f32 v246, v246\n"
  "v_mfma_f32_16x16x32_bf16 a[80:83], a[172:175], v[20:23], a[80:83]\n"
  "v_mfma_f32_16x16x32_bf16 a[84:87], a[168:171], v[24:27], a[84:87]\n"
  "v_exp_f32 v247, v247\n"
  "v_mfma_f32_16x16x32_bf16 a[84:87], a[172:175], v[28:31], a[84:87]\n"
  "ds_read_b64_tr_b16 a[190:191], v227 offset:28672\n"
  "v_cvt_pk_bf16_f32 v173, v246, v247\n"
  "v_smfmac_f32_16x16x64_bf16 a[248:251], v[144:147], v[48:55], v148\n"
  "v_mfma_f32_16x16x32_bf16 v[252:255], a[128:131], v[12:15], v[140:143]\n"
  "v_exp_f32 v248, v248\n"
  "v_mfma_f32_16x16x32_bf16 v[252:255], a[132:135], v[44:47], v[252:255]\n"
  "ds_read_b128 a[128:131], v216 offset:0\n"
  "v_mfma_f32_16x16x32_bf16 v[252:255], a[136:139], v[76:79], v[252:255]\n"
  "v_exp_f32 v249, v249\n"
  "v_mfma_f32_16x16x32_bf16 v[252:255], a[140:143], v[108:111], v[252:255]\n"
  "ds_read_b64_tr_b16 a[160:161], v220 offset:49152\n"
  "v_cvt_pk_bf16_f32 v180, v248, v249\n"
  "v_mfma_f32_16x16x32_bf16 a[88:91], a[168:171], v[48:51], a[88:91]\n"
  "v_exp_f32 v250, v250\n"
  "v_mfma_f32_16x16x32_bf16 a[88:91], a[172:175], v[52:55], a[88:91]\n"
  "v_mfma_f32_16x16x32_bf16 a[92:95], a[168:171], v[56:59], a[92:95]\n"
  "s_waitcnt lgkmcnt(2)\n"
  "v_exp_f32 v251, v251\n"
  "v_mfma_f32_16x16x32_bf16 a[92:95], a[172:175], v[60:63], a[92:95]\n"
  "ds_read_b64_tr_b16 a[162:163], v220 offset:53248\n"
  "v_cvt_pk_bf16_f32 v181, v250, v251\n"
  "v_mfma_f32_16x16x32_bf16 v[240:243], a[144:147], v[0:3], v[128:131]\n"
  "s_xor_b32 s52, s52, 0x10000\n"
  "v_exp_f32 v252, v252\n"
  "v_mfma_f32_16x16x32_bf16 v[240:243], a[148:151], v[32:35], v[240:243]\n"
  "ds_read_b128 a[132:135], v217 offset:0\n"
  "v_mfma_f32_16x16x32_bf16 v[240:243], a[152:155], v[64:67], v[240:243]\n"
  "v_exp_f32 v253, v253\n"
  "v_mfma_f32_16x16x32_bf16 v[240:243], a[156:159], v[96:99], v[240:243]\n"
  "ds_read_b64_tr_b16 a[164:165], v220 offset:57344\n"
  "v_cvt_pk_bf16_f32 v188, v252, v253\n"
  "v_mfma_f32_16x16x32_bf16 a[96:99], a[176:179], v[16:19], a[96:99]\n"
  "v_exp_f32 v254, v254\n"
  "v_mfma_f32_16x16x32_bf16 a[96:99], a[180:183], v[20:23], a[96:99]\n"
  "s_setpc_b64 s[10:11]\n"
  ".p2align 6\n"
  "L_h0:\n"
  "v_mfma_f32_16x16x32_bf16 a[100:103], a[176:179], v[24:27], a[100:103]\n"
  "v_exp_f32 v255, v255\n"
  "v_mfma_f32_16x16x32_bf16 a[100:103], a[180:183], v[28:31], a[100:103]\n"
  "ds_read_b64_tr_b16 a[166:167], v220 offset:61440\n"
  "v_cvt_pk_bf16_f32 v189, v254, v255\n"
  "v_mfma_f32_16x16x32_bf16 v[244:247], a[144:147], v[4:7], v[132:135]\n"
  "v_exp_f32 v240, v240\n"
  "v_mfma_f32_16x16x32_bf16 v[244:247], a[148:151], v[36:39], v[244:247]\n"
  "ds_read_b128 a[136:139], v218 offset:0\n"
  "v_xor_b32 v220, 0x10000, v220\n"
  "v_mfma_f32_16x16x32_bf16 v[244:247], a[152:155], v[68:71], v[244:247]\n"
  "v_exp_f32 v241, v241\n"
  "v_mfma_f32_16x16x32_bf16 v[244:247], a[156:159], v[100:103], v[244:247]\n"
  "ds_read_b64_tr_b16 a[168:169], v221 offset:49152\n"
  "v_cvt_pk_bf16_f32 v166, v240, v241\n"
  "v_mfma_f32_16x16x32_bf16 a[104:107], a[176:179], v[48:51], a[104:107]\n"
  "s_xor_b32 s57, s12, 7168\n"
  "v_exp_f32 v242, v242\n"
  "v_mfma_f32_16x16x32_bf16 a[104:107], a[180:183], v[52:55], a[104:107]\n"
  "buffer_store_dwordx4 a[192:195], v192, s[32:35], s57 offen sc1\n"
  "v_mfma_f32_16x16x32_bf16 a[108:111], a[176:179], v[56:59], a[108:111]\n"
  "v_exp_f32 v243, v243\n"
  "v_mfma_f32_16x16x32_bf16 a[108:111], a[180:183], v[60:63], a[108:111]\n"
  "ds_read_b64_tr_b16 a[170:171], v221 offset:53248\n"
  "v_cvt_pk_bf16_f32 v167, v242, v243\n"
  "v_mfma_f32_16x16x32_bf16 v[248:251], a[144:147], v[8:11], v[136:139]\n"
  "v_exp_f32 v244, v244\n"
  "v_mfma_f32_16x16x32_bf16 v[248:251], a[148:151], v[40:43], v[248:251]\n"
  "ds_read_b128 a[140:143], v219 offset:0\n"
  "v_mfma_f32_16x16x32_bf16 v[248:251], a[152:155], v[72:75], v[248:251]\n"
  "v_exp_f32 v245, v245\n"
  "v_mfma_f32_16x16x32_bf16 v[248:251], a[156:159], v[104:107], v[248:251]\n"
  "ds_read_b64_tr_b16 a[172:173], v221 offset:57344\n"
  "v_cvt_pk_bf16_f32 v174, v244, v245\n"
  "v_mfma_f32_16x16x32_bf16 a[112:115], a[184:187], v[16:19], a[112:115]\n"
  "s_xor_b32 s58, s12, 39936\n"
  "v_exp_f32 v246, v246\n"
  "v_mfma_f32_16x16x32_bf16 a[112:115], a[188:191], v[20:23], a[112:115]\n"
  "L_dry2_0:\n"
  "L_pad8:\n"
  "s_cbranch_execz L_dryr2\n"
  "buffer_store_dwordx4 a[196:199], v192, s[32:35], s58 offen sc1\n"
  "v_mfma_f32_16x16x32_bf16 a[116:119], a[184:187], v[24:27], a[116:119]\n"
  "v_exp_f32 v247, v247\n"
  "v_mfma_f32_16x16x32_bf16 a[116:119], a[188:191], v[28:31], a[116:119]\n"
  "ds_read_b64_tr_b16 a[174:175], v221 offset:61440\n"
  "v_cvt_pk_bf16_f32 v175, v246, v247\n"
  "v_mfma_f32_16x16x32_bf16 v[252:255], a[144:147], v[12:15], v[140:143]\n"
  "s_add_u32 s10, s10, L_h1-L_h0\n"
  "v_exp_f32 v248, v248\n"
  "v_mfma_f32_16x16x32_bf16 v[252:255], a[148:151], v[44:47], v[252:255]\n"
  "ds_read_b128 a[144:147], v216 offset:4096\n"
  "v_xor_b32 v221, 0x10000, v221\n"
  "v_mfma_f32_16x16x32_bf16 v[252:255], a[152:155], v[76:79], v[252:255]\n"
  "s_addc_u32 s11, s11, 0\n"
  "v_exp_f32 v249, v249\n"
  "v_mfma_f32_16x16x32_bf16 v[252:255], a[156:159], v[108:111], v[252:255]\n"
  "ds_read_b64_tr_b16 a[176:177], v222 offset:49152\n"
  "v_cvt_pk_bf16_f32 v182, v248, v249\n"
  "v_mfma_f32_16x16x32_bf16 a[120:123], a[184:187], v[48:51], a[120:123]\n"
  "s_xor_b32 s59, s12, 71680\n"
  "v_exp_f32 v250, v250\n"
  "v_mfma_f32_16x16x32_bf16 a[120:123], a[188:191], v[52:55], a[120:123]\n"
  "buffer_store_dwordx4 a[200:203], v192, s[32:35], s59 offen sc1\n"
  "v_mfma_f32_16x16x32_bf16 a[124:127], a[184:187], v[56:59], a[124:127]\n"
  "s_waitcnt lgkmcnt(2)\n"
  "v_exp_f32 v251, v251\n"
  "v_mfma_f32_16x16x32_bf16 a[124:127], a[188:191], v[60:63], a[124:127]\n"
  "ds_read_b64_tr_b16 a[178:179], v222 offset:53248\n"
  "v_cvt_pk_bf16_f32 v183, v250, v251\n"
  "v_smfmac_f32_16x16x64_bf16 a[252:255], v[144:147], v[56:63], v148\n"
  "s_branch L_att\n"
  ".p2align 6\n"
  "L_h1:\n"
  "v_mfma_f32_16x16x32_bf16 a[100:103], a[176:179], v[24:27], a[100:103]\n"
  "v_exp_f32 v255, v255\n"
  "v_mfma_f32_16x16x32_bf16 a[100:103], a[180:183], v[28:31], a[100:103]\n"
  "ds_read_b64_tr_b16 a[166:167], v220 offset:61440\n"
  "v_cvt_pk_bf16_f32 v189, v254, v255\n"
  "v_mfma_f32_16x16x32_bf16 v[244:247], a[144:147], v[4:7], v[132:135]\n"
  "v_exp_f32 v240, v240\n"
  "v_mfma_f32_16x16x32_bf16 v[244:247], a[148:151], v[36:39], v[244:247]\n"
  "ds_read_b128 a[136:139], v218 offset:0\n"
  "v_xor_b32 v220, 0x10000, v220\n"
  "v_mfma_f32_16x16x32_bf16 v[244:247], a[152:155], v[68:71], v[244:247]\n"
  "v_exp_f32 v241, v241\n"
  "v_mfma_f32_16x16x32_bf16 v[244:247], a[156:159], v[100:103], v[244:247]\n"
  "ds_read_b64_tr_b16 a[168:169], v221 offset:49152\n"
  "v_cvt_pk_bf16_f32 v166, v240, v241\n"
  "v_mfma_f32_16x16x32_bf16 a[104:107], a[176:179], v[48:51], a[104:107]\n"
  "s_xor_b32 s57, s12, 104448\n"
  "v_exp_f32 v242, v242\n"
  "v_mfma_f32_16x16x32_bf16 a[104:107], a[180:183], v[52:55], a[104:107]\n"
  "buffer_store_dwordx4 a[204:207], v192, s[32:35], s57 offen sc1\n"
  "v_mfma_f32_16x16x32_bf16 a[108:111], a[176:179], v[56:59], a[108:111]\n"
  "v_exp_f32 v243, v243\n"
  "v_mfma_f32_16x16x32_bf16 a[108:111], a[180:183], v[60:63], a[108:111]\n"
  "ds_read_b64_tr_b16 a[170:171], v221 offset:53248\n"
  "v_cvt_pk_bf16_f32 v167, v242, v243\n"
  "v_mfma_f32_16x16x32_bf16 v[248:251], a[144:147], v[8:11], v[136:139]\n"
  "v_exp_f32 v244, v244\n"
  "v_mfma_f32_16x16x32_bf16 v[248:251], a[148:151], v[40:43], v[248:251]\n"
  "ds_read_b128 a[140:143], v219 offset:0\n"
  "v_mfma_f32_16x16x32_bf16 v[248:251], a[152:155], v[72:75], v[248:251]\n"
  "v_exp_f32 v245, v245\n"
  "v_mfma_f32_16x16x32_bf16 v[248:251], a[156:159], v[104:107], v[248:251]\n"
  "ds_read_b64_tr_b16 a[172:173], v221 offset:57344\n"
  "v_cvt_pk_bf16_f32 v174, v244, v245\n"
  "v_mfma_f32_16x16x32_bf16 a[112:115], a[184:187], v[16:19], a[112:115]\n"
  "s_xor_b32 s58, s12, 136192\n"
  "v_exp_f32 v246, v246\n"
  "v_mfma_f32_16x16x32_bf16 a[112:115], a[188:191], v[20:23], a[112:115]\n"
  "buffer_store_dwordx4 a[208:211], v192, s[32:35], s58 offen sc1\n"
  "v_mfma_f32_16x16x32_bf16 a[116:119], a[184:187], v[24:27], a[116:119]\n"
  "v_exp_f32 v247, v247\n"
  "v_mfma_f32_16x16x32_bf16 a[116:119], a[188:191], v[28:31], a[116:119]\n"
  "ds_read_b64_tr_b16 a[174:175], v221 offset:61440\n"
  "v_cvt_pk_bf16_f32 v175, v246, v247\n"
  "v_mfma_f32_16x16x32_bf16 v[252:255], a[144:147], v[12:15], v[140:143]\n"
  "s_add_u32 s10, s10, L_h2-L_h1\n"
  "v_exp_f32 v248, v248\n"
  "v_mfma_f32_16x16x32_bf16 v[252:255], a[148:151], v[44:47], v[252:255]\n"
  "ds_read_b128 a[144:147], v216 offset:4096\n"
  "v_xor_b32 v221, 0x10000, v221\n"
  "v_mfma_f32_16x16x32_bf16 v[252:255], a[152:155], v[76:79], v[252:255]\n"
  "s_addc_u32 s11, s11, 0\n"
  "v_exp_f32 v249, v249\n"
  "v_mfma_f32_16x16x32_bf16 v[252:255], a[156:159], v[108:111], v[252:255]\n"
  "ds_read_b64_tr_b16 a[176:177], v222 offset:49152\n"
  "v_cvt_pk_bf16_f32 v182, v248, v249\n"
  "v_mfma_f32_16x16x32_bf16 a[120:123], a[184:187], v[48:51], a[120:123]\n"
  "s_xor_b32 s59, s12, 168960\n"
  "v_exp_f32 v250, v250\n"
  "v_mfma_f32_16x16x32_bf16 a[120:123], a[188:191], v[52:55], a[120:123]\n"
  "buffer_store_dwordx4 a[212:215], v192, s[32:35], s59 offen sc1\n"
  "v_mfma_f32_16x16x32_bf16 a[124:127], a[184:187], v[56:59], a[124:127]\n"
  "s_waitcnt lgkmcnt(2)\n"
  "v_exp_f32 v251, v251\n"
  "v_mfma_f32_16x16x32_bf16 a[124:127], a[188:191], v[60:63], a[124:127]\n"
  "ds_read_b64_tr_b16 a[178:179], v222 offset:53248\n"
  "v_cvt_pk_bf16_f32 v183, v250, v251\n"
  "v_smfmac_f32_16x16x64_bf16 a[252:255], v[144:147], v[56:63], v148\n"
  "s_branch L_att\n"
  ".p2align 6\n"
  "L_h2:\n"
  "v_mfma_f32_16x16x32_bf16 a[100:103], a[176:179], v[24:27], a[100:103]\n"
  "v_exp_f32 v255, v255\n"
  "v_mfma_f32_16x16x32_bf16 a[100:103], a[180:183], v[28:31], a[100:103]\n"
  "ds_read_b64_tr_b16 a[166:167], v220 offset:61440\n"
  "v_cvt_pk_bf16_f32 v189, v254, v255\n"
  "v_mfma_f32_16x16x32_bf16 v[244:247], a[144:147], v[4:7], v[132:135]\n"
  "v_exp_f32 v240, v240\n"
  "v_mfma_f32_16x16x32_bf16 v[244:247], a[148:151], v[36:39], v[244:247]\n"
  "ds_read_b128 a[136:139], v218 offset:0\n"
  "v_xor_b32 v220, 0x10000, v220\n"
  "v_mfma_f32_16x16x32_bf16 v[244:247], a[152:155], v[68:71], v[244:247]\n"
  "v_exp_f32 v241, v241\n"
  "v_mfma_f32_16x16x32_bf16 v[244:247], a[156:159], v[100:103], v[244:247]\n"
  "ds_read_b64_tr_b16 a[168:169], v221 offset:49152\n"
  "v_cvt_pk_bf16_f32 v166, v240, v241\n"
  "v_mfma_f32_16x16x32_bf16 a[104:107], a[176:179], v[48:51], a[104:107]\n"
  "s_xor_b32 s57, s12, 200704\n"
  "v_exp_f32 v242, v242\n"
  "v_mfma_f32_16x16x32_bf16 a[104:107], a[180:183], v[52:55], a[104:107]\n"
  "buffer_store_dwordx4 a[216:219], v192, s[32:35], s57 offen sc1\n"
  "v_mfma_f32_16x16x32_bf16 a[108:111], a[176:179], v[56:59], a[108:111]\n"
  "v_exp_f32 v243, v243\n"
  "v_mfma_f32_16x16x32_bf16 a[108:111], a[180:183], v[60:63], a[108:111]\n"
  "ds_read_b64_tr_b16 a[170:171], v221 offset:53248\n"
  "v_cvt_pk_bf16_f32 v167, v242, v243\n"
  "v_mfma_f32_16x16x32_bf16 v[248:251], a[144:147], v[8:11], v[136:139]\n"
  "v_exp_f32 v244, v244\n"
  "v_mfma_f32_16x16x32_bf16 v[248:251], a[148:151], v[40:43], v[248:251]\n"
  "ds_read_b128 a[140:143], v219 offset:0\n"
  "v_mfma_f32_16x16x32_bf16 v[248:251], a[152:155], v[72:75], v[248:251]\n"
  "v_exp_f32 v245, v245\n"
  "v_mfma_f32_16x16x32_bf16 v[248:251], a[156:159], v[104:107], v[248:251]\n"
  "ds_read_b64_tr_b16 a[172:173], v221 offset:57344\n"
  "v_cvt_pk_bf16_f32 v174, v244, v245\n"
  "v_mfma_f32_16x16x32_bf16 a[112:115], a[184:187], v[16:19], a[112:115]\n"
  "s_xor_b32 s58, s12, 233472\n"
  "v_exp_f32 v246, v246\n"
  "v_mfma_f32_16x16x32_bf16 a[112:115], a[188:191], v[20:23], a[112:115]\n"
  "buffer_store_dwordx4 a[220:223], v192, s[32:35], s58 offen sc1\n"
  "v_mfma_f32_16x16x32_bf16 a[116:119], a[184:187], v[24:27], a[116:119]\n"
  "v_exp_f32 v247, v247\n"
  "v_mfma_f32_16x16x32_bf16 a[116:119], a[188:191], v[28:31], a[116:119]\n"
  "ds_read_b64_tr_b16 a[174:175], v221 offset:61440\n"
  "v_cvt_pk_bf16_f32 v175, v246, v247\n"
  "v_mfma_f32_16x16x32_bf16 v[252:255], a[144:147], v[12:15], v[140:143]\n"
  "s_add_u32 s10, s10, L_h3-L_h2\n"
  "v_exp_f32 v248, v248\n"
  "v_mfma_f32_16x16x32_bf16 v[252:255], a[148:151], v[44:47], v[252:255]\n"
  "ds_read_b128 a[144:147], v216 offset:4096\n"
  "v_xor_b32 v221, 0x10000, v221\n"
  "v_mfma_f32_16x16x32_bf16 v[252:255], a[152:155], v[76:79], v[252:255]\n"
  "s_addc_u32 s11, s11, 0\n"
  "v_exp_f32 v249, v249\n"
  "v_mfma_f32_16x16x32_bf16 v[252:255], a[156:159], v[108:111], v[252:255]\n"
  "ds_read_b64_tr_b16 a[176:177], v222 offset:49152\n"
  "v_cvt_pk_bf16_f32 v182, v248, v249\n"
  "v_mfma_f32_16x16x32_bf16 a[120:123], a[184:187], v[48:51], a[120:123]\n"
  "s_xor_b32 s59, s12, 265216\n"
  "v_exp_f32 v250, v250\n"
  "v_mfma_f32_16x16x32_bf16 a[120:123], a[188:191], v[52:55], a[120:123]\n"
  "buffer_store_dwordx4 v[80:83], v192, s[32:35], s59 offen sc1\n"
  "v_mfma_f32_16x16x32_bf16 a[124:127], a[184:187], v[56:59], a[124:127]\n"
  "s_waitcnt lgkmcnt(2)\n"
  "v_exp_f32 v251, v251\n"
  "v_mfma_f32_16x16x32_bf16 a[124:127], a[188:191], v[60:63], a[124:127]\n"
  "ds_read_b64_tr_b16 a[178:179], v222 offset:53248\n"
  "v_cvt_pk_bf16_f32 v183, v250, v251\n"
  "v_smfmac_f32_16x16x64_bf16 a[252:255], v[144:147], v[56:63], v148\n"
  "s_branch L_att\n"
  ".p2align 6\n"
  "L_h3:\n"
  "v_mfma_f32_16x16x32_bf16 a[100:103], a[176:179], v[24:27], a[100:103]\n"
  "v_exp_f32 v255, v255\n"
  "v_mfma_f32_16x16x32_bf16 a[100:103], a[180:183], v[28:31], a[100:103]\n"
  "ds_read_b64_tr_b16 a[166:167], v220 offset:61440\n"
  "v_cvt_pk_bf16_f32 v189, v254, v255\n"
  "v_mfma_f32_16x16x32_bf16 v[244:247], a[144:147], v[4:7], v[132:135]\n"
  "v_exp_f32 v240, v240\n"
  "v_mfma_f32_16x16x32_bf16 v[244:247], a[148:151], v[36:39], v[244:247]\n"
  "ds_read_b128 a[136:139], v218 offset:0\n"
  "v_xor_b32 v220, 0x10000, v220\n"
  "v_mfma_f32_16x16x32_bf16 v[244:247], a[152:155], v[68:71], v[244:247]\n"
  "v_exp_f32 v241, v241\n"
  "v_mfma_f32_16x16x32_bf16 v[244:247], a[156:159], v[100:103], v[244:247]\n"
  "ds_read_b64_tr_b16 a[168:169], v221 offset:49152\n"
  "v_cvt_pk_bf16_f32 v166, v240, v241\n"
  "v_mfma_f32_16x16x32_bf16 a[104:107], a[176:179], v[48:51], a[104:107]\n"
  "s_xor_b32 s57, s12, 297984\n"
  "v_exp_f32 v242, v242\n"
  "v_mfma_f32_16x16x32_bf16 a[104:107], a[180:183], v[52:55], a[104:107]\n"
  "buffer_store_dwordx4 v[84:87], v192, s[32:35], s57 offen sc1\n"
  "v_mfma_f32_16x16x32_bf16 a[108:111], a[176:179], v[56:59], a[108:111]\n"
  "v_exp_f32 v243, v243\n"
  "v_mfma_f32_16x16x32_bf16 a[108:111], a[180:183], v[60:63], a[108:111]\n"
  "ds_read_b64_tr_b16 a[170:171], v221 offset:53248\n"
  "v_cvt_pk_bf16_f32 v167, v242, v243\n"
  "v_mfma_f32_16x16x32_bf16 v[248:251], a[144:147], v[8:11], v[136:139]\n"
  "v_exp_f32 v244, v244\n"
  "v_mfma_f32_16x16x32_bf16 v[248:251], a[148:151], v[40:43], v[248:251]\n"
  "ds_read_b128 a[140:143], v219 offset:0\n"
  "v_mfma_f32_16x16x32_bf16 v[248:251], a[152:155], v[72:75], v[248:251]\n"
  "v_exp_f32 v245, v245\n"
  "v_mfma_f32_16x16x32_bf16 v[248:251], a[156:159], v[104:107], v[248:251]\n"
  "ds_read_b64_tr_b16 a[172:173], v221 offset:57344\n"
  "v_cvt_pk_bf16_f32 v174, v244, v245\n"
  "v_mfma_f32_16x16x32_bf16 a[112:115], a[184:187], v[16:19], a[112:115]\n"
  "s_xor_b32 s58, s12, 329728\n"
  "v_exp_f32 v246, v246\n"
  "v_mfma_f32_16x16x32_bf16 a[112:115], a[188:191], v[20:23], a[112:115]\n"
  "buffer_store_dwordx4 v[88:91], v192, s[32:35], s58 offen sc1\n"
  "v_mfma_f32_16x16x32_bf16 a[116:119], a[184:187], v[24:27], a[116:119]\n"
  "v_exp_f32 v247, v247\n"
  "v_mfma_f32_16x16x32_bf16 a[116:119], a[188:191], v[28:31], a[116:119]\n"
  "ds_read_b64_tr_b16 a[174:175], v221 offset:61440\n"
  "v_cvt_pk_bf16_f32 v175, v246, v247\n"
  "v_mfma_f32_16x16x32_bf16 v[252:255], a[144:147], v[12:15], v[140:143]\n"
  "s_add_u32 s10, s10, L_h4-L_h3\n"
  "v_exp_f32 v248, v248\n"
  "v_mfma_f32_16x16x32_bf16 v[252:255], a[148:151], v[44:47], v[252:255]\n"
  "ds_read_b128 a[144:147], v216 offset:4096\n"
  "v_xor_b32 v221, 0x10000, v221\n"
  "v_mfma_f32_16x16x32_bf16 v[252:255], a[152:155], v[76:79], v[252:255]\n"
  "s_addc_u32 s11, s11, 0\n"
  "v_exp_f32 v249, v249\n"
  "v_mfma_f32_16x16x32_bf16 v[252:255], a[156:159], v[108:111], v[252:255]\n"
  "ds_read_b64_tr_b16 a[176:177], v222 offset:49152\n"
  "v_cvt_pk_bf16_f32 v182, v248, v249\n"
  "v_mfma_f32_16x16x32_bf16 a[120:123], a[184:187], v[48:51], a[120:123]\n"
  "s_xor_b32 s59, s12, 362496\n"
  "v_exp_f32 v250, v250\n"
  "v_mfma_f32_16x16x32_bf16 a[120:123], a[188:191], v[52:55], a[120:123]\n"
  "buffer_store_dwordx4 v[92:95], v192, s[32:35], s59 offen sc1\n"
  "v_mfma_f32_16x16x32_bf16 a[124:127], a[184:187], v[56:59], a[124:127]\n"
  "s_waitcnt lgkmcnt(2)\n"
  "v_exp_f32 v251, v251\n"
  "v_mfma_f32_16x16x32_bf16 a[124:127], a[188:191], v[60:63], a[124:127]\n"
  "ds_read_b64_tr_b16 a[178:179], v222 offset:53248\n"
  "v_cvt_pk_bf16_f32 v183, v250, v251\n"
  "v_smfmac_f32_16x16x64_bf16 a[252:255], v[144:147], v[56:63], v148\n"
  "s_branch L_att\n"
  ".p2align 6\n"
  "L_h4:\n"
  "v_mfma_f32_16x16x32_bf16 a[100:103], a[176:179], v[24:27], a[100:103]\n"
  "v_exp_f32 v255, v255\n"
  "v_mfma_f32_16x16x32_bf16 a[100:103], a[180:183], v[28:31], a[100:103]\n"
  "ds_read_b64_tr_b16 a[166:167], v220 offset:61440\n"
  "v_cvt_pk_bf16_f32 v189, v254, v255\n"
  "v_mfma_f32_16x16x32_bf16 v[244:247], a[144:147], v[4:7], v[132:135]\n"
  "v_exp_f32 v240, v240\n"
  "v_mfma_f32_16x16x32_bf16 v[244:247], a[148:151], v[36:39], v[244:247]\n"
  "ds_read_b128 a[136:139], v218 offset:0\n"
  "v_xor_b32 v220, 0x10000, v220\n"
  "v_mfma_f32_16x16x32_bf16 v[244:247], a[152:155], v[68:71], v[244:247]\n"
  "v_exp_f32 v241, v241\n"
  "v_mfma_f32_16x16x32_bf16 v[244:247], a[156:159], v[100:103], v[244:247]\n"
  "ds_read_b64_tr_b16 a[168:169], v221 offset:49152\n"
  "v_cvt_pk_bf16_f32 v166, v240, v241\n"
  "v_mfma_f32_16x16x32_bf16 a[104:107], a[176:179], v[48:51], a[104:107]\n"
  "s_xor_b32 s57, s12, 394240\n"
  "v_exp_f32 v242, v242\n"
  "v_mfma_f32_16x16x32_bf16 a[104:107], a[180:183], v[52:55], a[104:107]\n"
  "buffer_store_dwordx4 v[112:115], v192, s[32:35], s57 offen sc1\n"
  "v_mfma_f32_16x16x32_bf16 a[108:111], a[176:179], v[56:59], a[108:111]\n"
  "v_exp_f32 v243, v243\n"
  "v_mfma_f32_16x16x32_bf16 a[108:111], a[180:183], v[60:63], a[108:111]\n"
  "ds_read_b64_tr_b16 a[170:171], v221 offset:53248\n"
  "v_cvt_pk_bf16_f32 v167, v242, v243\n"
  "v_mfma_f32_16x16x32_bf16 v[248:251], a[144:147], v[8:11], v[136:139]\n"
  "v_exp_f32 v244, v244\n"
  "v_mfma_f32_16x16x32_bf16 v[248:251], a[148:151], v[40:43], v[248:251]\n"
  "ds_read_b128 a[140:143], v219 offset:0\n"
  "v_mfma_f32_16x16x32_bf16 v[248:251], a[152:155], v[72:75], v[248:251]\n"
  "v_exp_f32 v245, v245\n"
  "v_mfma_f32_16x16x32_bf16 v[248:251], a[156:159], v[104:107], v[248:251]\n"
  "ds_read_b64_tr_b16 a[172:173], v221 offset:57344\n"
  "v_cvt_pk_bf16_f32 v174, v244, v245\n"
  "v_mfma_f32_16x16x32_bf16 a[112:115], a[184:187], v[16:19], a[112:115]\n"
  "s_xor_b32 s58, s12, 427008\n"
  "v_exp_f32 v246, v246\n"
  "v_mfma_f32_16x16x32_bf16 a[112:115], a[188:191], v[20:23], a[112:115]\n"
  "buffer_store_dwordx4 v[116:119], v192, s[32:35], s58 offen sc1\n"
  "v_mfma_f32_16x16x32_bf16 a[116:119], a[184:187], v[24:27], a[116:119]\n"
  "v_exp_f32 v247, v247\n"
  "v_mfma_f32_16x16x32_bf16 a[116:119], a[188:191], v[28:31], a[116:119]\n"
  "ds_read_b64_tr_b16 a[174:175], v221 offset:61440\n"
  "v_cvt_pk_bf16_f32 v175, v246, v247\n"
  "v_mfma_f32_16x16x32_bf16 v[252:255], a[144:147], v[12:15], v[140:143]\n"
  "s_add_u32 s10, s10, L_h5-L_h4\n"
  "v_exp_f32 v248, v248\n"
  "v_mfma_f32_16x16x32_bf16 v[252:255], a[148:151], v[44:47], v[252:255]\n"
  "ds_read_b128 a[144:147], v216 offset:4096\n"
  "v_xor_b32 v221, 0x10000, v221\n"
  "v_mfma_f32_16x16x32_bf16 v[252:255], a[152:155], v[76:79], v[252:255]\n"
  "s_addc_u32 s11, s11, 0\n"
  "v_exp_f32 v249, v249\n"
  "v_mfma_f32_16x16x32_bf16 v[252:255], a[156:159], v[108:111], v[252:255]\n"
  "ds_read_b64_tr_b16 a[176:177], v222 offset:49152\n"
  "v_cvt_pk_bf16_f32 v182, v248, v249\n"
  "v_mfma_f32_16x16x32_bf16 a[120:123], a[184:187], v[48:51], a[120:123]\n"
  "v_exp_f32 v250, v250\n"
  "v_mfma_f32_16x16x32_bf16 a[120:123], a[188:191], v[52:55], a[120:123]\n"
  "buffer_load_dword v193, v238, s[68:71], 0 offen sc1\n"
  "v_mfma_f32_16x16x32_bf16 a[124:127], a[184:187], v[56:59], a[124:127]\n"
  "s_waitcnt lgkmcnt(2)\n"
  "v_exp_f32 v251, v251\n"
  "v_mfma_f32_16x16x32_bf16 a[124:127], a[188:191], v[60:63], a[124:127]\n"
  "ds_read_b64_tr_b16 a[178:179], v222 offset:53248\n"
  "v_cvt_pk_bf16_f32 v183, v250, v251\n"
  "v_smfmac_f32_16x16x64_bf16 a[252:255], v[144:147], v[56:63], v148\n"
  "s_branch L_att\n"
  ".p2align 6\n"
  "L_h5:\n"
  "v_mfma_f32_16x16x32_bf16 a[100:103], a[176:179], v[24:27], a[100:103]\n"
  "v_exp_f32 v255, v255\n"
  "v_mfma_f32_16x16x32_bf16 a[100:103], a[180:183], v[28:31], a[100:103]\n"
  "ds_read_b64_tr_b16 a[166:167], v220 offset:61440\n"
  "v_cvt_pk_bf16_f32 v189, v254, v255\n"
  "v_mfma_f32_16x16x32_bf16 v[244:247], a[144:147], v[4:7], v[132:135]\n"
  "v_exp_f32 v240, v240\n"
  "v_mfma_f32_16x16x32_bf16 v[244:247], a[148:151], v[36:39], v[244:247]\n"
  "ds_read_b128 a[136:139], v218 offset:0\n"
  "v_xor_b32 v220, 0x10000, v220\n"
  "v_mfma_f32_16x16x32_bf16 v[244:247], a[152:155], v[68:71], v[244:247]\n"
  "v_exp_f32 v241, v241\n"
  "v_mfma_f32_16x16x32_bf16 v[244:247], a[156:159], v[100:103], v[244:247]\n"
  "ds_read_b64_tr_b16 a[168:169], v221 offset:49152\n"
  "v_cvt_pk_bf16_f32 v166, v240, v241\n"
  "v_mfma_f32_16x16x32_bf16 a[104:107], a[176:179], v[48:51], a[104:107]\n"
  "s_lshl_b32 s57, s67, 14\n"
  "v_exp_f32 v242, v242\n"
  "v_mfma_f32_16x16x32_bf16 a[104:107], a[180:183], v[52:55], a[104:107]\n"
  "s_bfm_b64 exec, 32, 0\n"
  "buffer_load_dword v209, v208, s[16:19], s57 offen\n"
  "s_mov_b64 exec, -1\n"
  "v_mfma_f32_16x16x32_bf16 a[108:111], a[176:179], v[56:59], a[108:111]\n"
  "v_exp_f32 v243, v243\n"
  "v_mfma_f32_16x16x32_bf16 a[108:111], a[180:183], v[60:63], a[108:111]\n"
  "ds_read_b64_tr_b16 a[170:171], v221 offset:53248\n"
  "v_cvt_pk_bf16_f32 v167, v242, v243\n"
  "v_mfma_f32_16x16x32_bf16 v[248:251], a[144:147], v[8:11], v[136:139]\n"
  "v_exp_f32 v244, v244\n"
  "v_mfma_f32_16x16x32_bf16 v[248:251], a[148:151], v[40:43], v[248:251]\n"
  "ds_read_b128 a[140:143], v219 offset:0\n"
  "v_mfma_f32_16x16x32_bf16 v[248:251], a[152:155], v[72:75], v[248:251]\n"
  "v_exp_f32 v245, v245\n"
  "v_mfma_f32_16x16x32_bf16 v[248:251], a[156:159], v[104:107], v[248:251]\n"
  "ds_read_b64_tr_b16 a[172:173], v221 offset:57344\n"
  "v_cvt_pk_bf16_f32 v174, v244, v245\n"
  "v_mfma_f32_16x16x32_bf16 a[112:115], a[184:187], v[16:19], a[112:115]\n"
  "v_exp_f32 v246, v246\n"
  "v_mfma_f32_16x16x32_bf16 a[112:115], a[188:191], v[20:23], a[112:115]\n"
  "buffer_store_dword v193, v239, s[68:71], 0 offen offset:0 sc1\n"
  "v_mfma_f32_16x16x32_bf16 a[116:119], a[184:187], v[24:27], a[116:119]\n"
  "v_exp_f32 v247, v247\n"
  "v_mfma_f32_16x16x32_bf16 a[116:119], a[188:191], v[28:31], a[116:119]\n"
  "ds_read_b64_tr_b16 a[174:175], v221 offset:61440\n"
  "v_cvt_pk_bf16_f32 v175, v246, v247\n"
  "v_mfma_f32_16x16x32_bf16 v[252:255], a[144:147], v[12:15], v[140:143]\n"
  "s_add_u32 s10, s10, L_h6-L_h5\n"
  "v_exp_f32 v248, v248\n"
  "v_mfma_f32_16x16x32_bf16 v[252:255], a[148:151], v[44:47], v[252:255]\n"
  "ds_read_b128 a[144:147], v216 offset:4096\n"
  "v_xor_b32 v221, 0x10000, v221\n"
  "v_mfma_f32_16x16x32_bf16 v[252:255], a[152:155], v[76:79], v[252:255]\n"
  "s_addc_u32 s11, s11, 0\n"
  "v_exp_f32 v249, v249\n"
  "v_mfma_f32_16x16x32_bf16 v[252:255], a[156:159], v[108:111], v[252:255]\n"
  "ds_read_b64_tr_b16 a[176:177], v222 offset:49152\n"
  "v_cvt_pk_bf16_f32 v182, v248, v249\n"
  "v_mfma_f32_16x16x32_bf16 a[120:123], a[184:187], v[48:51], a[120:123]\n"
  "v_exp_f32 v250, v250\n"
  "v_mfma_f32_16x16x32_bf16 a[120:123], a[188:191], v[52:55], a[120:123]\n"
  "v_mfma_f32_16x16x32_bf16 a[124:127], a[184:187], v[56:59], a[124:127]\n"
  "s_waitcnt lgkmcnt(2)\n"
  "v_exp_f32 v251, v251\n"
  "v_mfma_f32_16x16x32_bf16 a[124:127], a[188:191], v[60:63], a[124:127]\n"
  "ds_read_b64_tr_b16 a[178:179], v222 offset:53248\n"
  "v_cvt_pk_bf16_f32 v183, v250, v251\n"
  "v_smfmac_f32_16x16x64_bf16 a[252:255], v[144:147], v[56:63], v148\n"
  "s_branch L_att\n"
  ".p2align 6\n"
  "L_h6:\n"
  "v_mfma_f32_16x16x32_bf16 a[100:103], a[176:179], v[24:27], a[100:103]\n"
  "v_exp_f32 v255, v255\n"
  "v_mfma_f32_16x16x32_bf16 a[100:103], a[180:183], v[28:31], a[100:103]\n"
  "ds_read_b64_tr_b16 a[166:167], v220 offset:61440\n"
  "v_cvt_pk_bf16_f32 v189, v254, v255\n"
  "v_mfma_f32_16x16x32_bf16 v[244:247], a[144:147], v[4:7], v[132:135]\n"
  "v_exp_f32 v240, v240\n"
  "v_mfma_f32_16x16x32_bf16 v[244:247], a[148:151], v[36:39], v[244:247]\n"
  "ds_read_b128 a[136:139], v218 offset:0\n"
  "v_xor_b32 v220, 0x10000, v220\n"
  "v_mfma_f32_16x16x32_bf16 v[244:247], a[152:155], v[68:71], v[244:247]\n"
  "v_exp_f32 v241, v241\n"
  "v_mfma_f32_16x16x32_bf16 v[244:247], a[156:159], v[100:103], v[244:247]\n"
  "ds_read_b64_tr_b16 a[168:169], v221 offset:49152\n"
  "v_cvt_pk_bf16_f32 v166, v240, v241\n"
  "v_mfma_f32_16x16x32_bf16 a[104:107], a[176:179], v[48:51], a[104:107]\n"
  "v_exp_f32 v242, v242\n"
  "v_mfma_f32_16x16x32_bf16 a[104:107], a[180:183], v[52:55], a[104:107]\n"
  "s_add_u32 s32, s32, 0x1000000\n"
  "s_addc_u32 s33, s33, 0\n"
  "s_mov_b64 exec, 0xffff\n"
  "buffer_load_dword v209, v237, s[32:35], 0 offen sc1\n"
  "global_load_dword v209, v206, s[40:41]\n"
  "s_mov_b64 exec, -1\n"
  "v_mfma_f32_16x16x32_bf16 a[108:111], a[176:179], v[56:59], a[108:111]\n"
  "v_exp_f32 v243, v243\n"
  "v_mfma_f32_16x16x32_bf16 a[108:111], a[180:183], v[60:63], a[108:111]\n"
  "ds_read_b64_tr_b16 a[170:171], v221 offset:53248\n"
  "v_cvt_pk_bf16_f32 v167, v242, v243\n"
  "v_mfma_f32_16x16x32_bf16 v[248:251], a[144:147], v[8:11], v[136:139]\n"
  "v_exp_f32 v244, v244\n"
  "v_mfma_f32_16x16x32_bf16 v[248:251], a[148:151], v[40:43], v[248:251]\n"
  "ds_read_b128 a[140:143], v219 offset:0\n"
  "v_mfma_f32_16x16x32_bf16 v[248:251], a[152:155], v[72:75], v[248:251]\n"
  "v_exp_f32 v245, v245\n"
  "v_mfma_f32_16x16x32_bf16 v[248:251], a[156:159], v[104:107], v[248:251]\n"
  "ds_read_b64_tr_b16 a[172:173], v221 offset:57344\n"
  "v_cvt_pk_bf16_f32 v174, v244, v245\n"
  "v_mfma_f32_16x16x32_bf16 a[112:115], a[184:187], v[16:19], a[112:115]\n"
  "s_add_u32 m0, s63, 131072\n"
  "s_add_u32 s53, s13, 131072\n"
  "v_exp_f32 v246, v246\n"
  "v_mfma_f32_16x16x32_bf16 a[112:115], a[188:191], v[20:23], a[112:115]\n"
  "buffer_load_dwordx4 v198, s[16:19], s53 offen offset:0 lds\n"
  "v_mfma_f32_16x16x32_bf16 a[116:119], a[184:187], v[24:27], a[116:119]\n"
  "v_exp_f32 v247, v247\n"
  "v_mfma_f32_16x16x32_bf16 a[116:119], a[188:191], v[28:31], a[116:119]\n"
  "ds_read_b64_tr_b16 a[174:175], v221 offset:61440\n"
  "v_cvt_pk_bf16_f32 v175, v246, v247\n"
  "v_mfma_f32_16x16x32_bf16 v[252:255], a[144:147], v[12:15], v[140:143]\n"
  "s_add_u32 s10, s10, L_h7-L_h6\n"
  "v_exp_f32 v248, v248\n"
  "v_mfma_f32_16x16x32_bf16 v[252:255], a[148:151], v[44:47], v[252:255]\n"
  "ds_read_b128 a[144:147], v216 offset:4096\n"
  "v_xor_b32 v221, 0x10000, v221\n"
  "v_mfma_f32_16x16x32_bf16 v[252:255], a[152:155], v[76:79], v[252:255]\n"
  "s_addc_u32 s11, s11, 0\n"
  "v_exp_f32 v249, v249\n"
  "v_mfma_f32_16x16x32_bf16 v[252:255], a[156:159], v[108:111], v[252:255]\n"
  "ds_read_b64_tr_b16 a[176:177], v222 offset:49152\n"
  "v_cvt_pk_bf16_f32 v182, v248, v249\n"
  "v_mfma_f32_16x16x32_bf16 a[120:123], a[184:187], v[48:51], a[120:123]\n"
  "v_exp_f32 v250, v250\n"
  "v_mfma_f32_16x16x32_bf16 a[120:123], a[188:191], v[52:55], a[120:123]\n"
  "buffer_load_dwordx4 v199, s[16:19], s53 offen offset:1024 lds\n"
  "v_mfma_f32_16x16x32_bf16 a[124:127], a[184:187], v[56:59], a[124:127]\n"
  "s_waitcnt lgkmcnt(2)\n"
  "v_exp_f32 v251, v251\n"
  "v_mfma_f32_16x16x32_bf16 a[124:127], a[188:191], v[60:63], a[124:127]\n"
  "ds_read_b64_tr_b16 a[178:179], v222 offset:53248\n"
  "v_cvt_pk_bf16_f32 v183, v250, v251\n"
  "v_smfmac_f32_16x16x64_bf16 a[252:255], v[144:147], v[56:63], v148\n"
  "s_branch L_att\n"
  ".p2align 6\n"
  "L_h7:\n"
  "v_mfma_f32_16x16x32_bf16 a[100:103], a[176:179], v[24:27], a[100:103]\n"
  "v_exp_f32 v255, v255\n"
  "v_mfma_f32_16x16x32_bf16 a[100:103], a[180:183], v[28:31], a[100:103]\n"
  "ds_read_b64_tr_b16 a[166:167], v220 offset:61440\n"
  "v_cvt_pk_bf16_f32 v189, v254, v255\n"
  "v_mfma_f32_16x16x32_bf16 v[244:247], a[144:147], v[4:7], v[132:135]\n"
  "v_exp_f32 v240, v240\n"
  "v_mfma_f32_16x16x32_bf16 v[244:247], a[148:151], v[36:39], v[244:247]\n"
  "ds_read_b128 a[136:139], v218 offset:0\n"
  "v_xor_b32 v220, 0x10000, v220\n"
  "v_mfma_f32_16x16x32_bf16 v[244:247], a[152:155], v[68:71], v[244:247]\n"
  "v_exp_f32 v241, v241\n"
  "v_mfma_f32_16x16x32_bf16 v[244:247], a[156:159], v[100:103], v[244:247]\n"
  "ds_read_b64_tr_b16 a[168:169], v221 offset:49152\n"
  "v_cvt_pk_bf16_f32 v166, v240, v241\n"
  "v_mfma_f32_16x16x32_bf16 a[104:107], a[176:179], v[48:51], a[104:107]\n"
  "v_exp_f32 v242, v242\n"
  "v_mfma_f32_16x16x32_bf16 a[104:107], a[180:183], v[52:55], a[104:107]\n"
  "buffer_load_dword v193, v238, s[68:71], 0 offen sc1\n"
  "v_mfma_f32_16x16x32_bf16 a[108:111], a[176:179], v[56:59], a[108:111]\n"
  "v_exp_f32 v243, v243\n"
  "v_mfma_f32_16x16x32_bf16 a[108:111], a[180:183], v[60:63], a[108:111]\n"
  "ds_read_b64_tr_b16 a[170:171], v221 offset:53248\n"
  "v_cvt_pk_bf16_f32 v167, v242, v243\n"
  "v_mfma_f32_16x16x32_bf16 v[248:251], a[144:147], v[8:11], v[136:139]\n"
  "v_exp_f32 v244, v244\n"
  "v_mfma_f32_16x16x32_bf16 v[248:251], a[148:151], v[40:43], v[248:251]\n"
  "ds_read_b128 a[140:143], v219 offset:0\n"
  "v_mfma_f32_16x16x32_bf16 v[248:251], a[152:155], v[72:75], v[248:251]\n"
  "v_exp_f32 v245, v245\n"
  "v_mfma_f32_16x16x32_bf16 v[248:251], a[156:159], v[104:107], v[248:251]\n"
  "ds_read_b64_tr_b16 a[172:173], v221 offset:57344\n"
  "v_cvt_pk_bf16_f32 v174, v244, v245\n"
  "v_mfma_f32_16x16x32_bf16 a[112:115], a[184:187], v[16:19], a[112:115]\n"
  "s_add_u32 m0, s63, 131072\n"
  "s_add_u32 s53, s13, 131072\n"
  "v_exp_f32 v246, v246\n"
  "v_mfma_f32_16x16x32_bf16 a[112:115], a[188:191], v[20:23], a[112:115]\n"
  "buffer_load_dwordx4 v200, s[16:19], s53 offen offset:2048 lds\n"
  "v_mfma_f32_16x16x32_bf16 a[116:119], a[184:187], v[24:27], a[116:119]\n"
  "v_exp_f32 v247, v247\n"
  "v_mfma_f32_16x16x32_bf16 a[116:119], a[188:191], v[28:31], a[116:119]\n"
  "ds_read_b64_tr_b16 a[174:175], v221 offset:61440\n"
  "v_cvt_pk_bf16_f32 v175, v246, v247\n"
  "v_mfma_f32_16x16x32_bf16 v[252:255], a[144:147], v[12:15], v[140:143]\n"
  "s_add_u32 s10, s10, L_h8-L_h7\n"
  "v_exp_f32 v248, v248\n"
  "v_mfma_f32_16x16x32_bf16 v[252:255], a[148:151], v[44:47], v[252:255]\n"
  "ds_read_b128 a[144:147], v216 offset:4096\n"
  "v_xor_b32 v221, 0x10000, v221\n"
  "v_mfma_f32_16x16x32_bf16 v[252:255], a[152:155], v[76:79], v[252:255]\n"
  "s_addc_u32 s11, s11, 0\n"
  "v_exp_f32 v249, v249\n"
  "v_mfma_f32_16x16x32_bf16 v[252:255], a[156:159], v[108:111], v[252:255]\n"
  "ds_read_b64_tr_b16 a[176:177], v222 offset:49152\n"
  "v_cvt_pk_bf16_f32 v182, v248, v249\n"
  "v_mfma_f32_16x16x32_bf16 a[120:123], a[184:187], v[48:51], a[120:123]\n"
  "v_exp_f32 v250, v250\n"
  "v_mfma_f32_16x16x32_bf16 a[120:123], a[188:191], v[52:55], a[120:123]\n"
  "buffer_load_dwordx4 v201, s[16:19], s53 offen offset:3072 lds\n"
  "v_mfma_f32_16x16x32_bf16 a[124:127], a[184:187], v[56:59], a[124:127]\n"
  "s_waitcnt lgkmcnt(2)\n"
  "v_exp_f32 v251, v251\n"
  "v_mfma_f32_16x16x32_bf16 a[124:127], a[188:191], v[60:63], a[124:127]\n"
  "ds_read_b64_tr_b16 a[178:179], v222 offset:53248\n"
  "v_cvt_pk_bf16_f32 v183, v250, v251\n"
  "v_smfmac_f32_16x16x64_bf16 a[252:255], v[144:147], v[56:63], v148\n"
  "s_branch L_att\n"
  ".p2align 6\n"
  "L_h8:\n"
  "v_mfma_f32_16x16x32_bf16 a[100:103], a[176:179], v[24:27], a[100:103]\n"
  "v_exp_f32 v255, v255\n"
  "v_mfma_f32_16x16x32_bf16 a[100:103], a[180:183], v[28:31], a[100:103]\n"
  "ds_read_b64_tr_b16 a[166:167], v220 offset:61440\n"
  "v_cvt_pk_bf16_f32 v189, v254, v255\n"
  "v_mfma_f32_16x16x32_bf16 v[244:247], a[144:147], v[4:7], v[132:135]\n"
  "v_exp_f32 v240, v240\n"
  "v_mfma_f32_16x16x32_bf16 v[244:247], a[148:151], v[36:39], v[244:247]\n"
  "ds_read_b128 a[136:139], v218 offset:0\n"
  "v_xor_b32 v220, 0x10000, v220\n"
  "v_mfma_f32_16x16x32_bf16 v[244:247], a[152:155], v[68:71], v[244:247]\n"
  "v_exp_f32 v241, v241\n"
  "v_mfma_f32_16x16x32_bf16 v[244:247], a[156:159], v[100:103], v[244:247]\n"
  "ds_read_b64_tr_b16 a[168:169], v221 offset:49152\n"
  "v_cvt_pk_bf16_f32 v166, v240, v241\n"
  "v_mfma_f32_16x16x32_bf16 a[104:107], a[176:179], v[48:51], a[104:107]\n"
  "v_exp_f32 v242, v242\n"
  "v_mfma_f32_16x16x32_bf16 a[104:107], a[180:183], v[52:55], a[104:107]\n"
  "v_cmp_eq_u32 vcc, s65, v193\n"
  "s_nop 3\n"
  "s_bfe_u32 s57, vcc_lo, 0x80008\n"
  "s_cmp_eq_u32 s57, 0xff\n"
  "s_cbranch_scc0 L_poll1\n"
  "L_poll1r:\n"
  "buffer_load_dword v193, v238, s[68:71], 0 offen sc1\n"
  "v_mfma_f32_16x16x32_bf16 a[108:111], a[176:179], v[56:59], a[108:111]\n"
  "v_exp_f32 v243, v243\n"
  "v_mfma_f32_16x16x32_bf16 a[108:111], a[180:183], v[60:63], a[108:111]\n"
  "ds_read_b64_tr_b16 a[170:171], v221 offset:53248\n"
  "v_cvt_pk_bf16_f32 v167, v242, v243\n"
  "v_mfma_f32_16x16x32_bf16 v[248:251], a[144:147], v[8:11], v[136:139]\n"
  "v_exp_f32 v244, v244\n"
  "v_mfma_f32_16x16x32_bf16 v[248:251], a[148:151], v[40:43], v[248:251]\n"
  "ds_read_b128 a[140:143], v219 offset:0\n"
  "v_mfma_f32_16x16x32_bf16 v[248:251], a[152:155], v[72:75], v[248:251]\n"
  "v_exp_f32 v245, v245\n"
  "v_mfma_f32_16x16x32_bf16 v[248:251], a[156:159], v[104:107], v[248:251]\n"
  "ds_read_b64_tr_b16 a[172:173], v221 offset:57344\n"
  "v_cvt_pk_bf16_f32 v174, v244, v245\n"
  "v_mfma_f32_16x16x32_bf16 a[112:115], a[184:187], v[16:19], a[112:115]\n"
  "s_mov_b32 s58, 1024\n"
  "v_exp_f32 v246, v246\n"
  "v_mfma_f32_16x16x32_bf16 a[112:115], a[188:191], v[20:23], a[112:115]\n"
  "L_dry2_1:\n"
  "L_pad9:\n"
  "s_cbranch_execz L_dryr2\n"
  "buffer_load_dwordx4 v[80:83], v192, s[72:75], s58 offen sc1\n"
  "v_mfma_f32_16x16x32_bf16 a[116:119], a[184:187], v[24:27], a[116:119]\n"
  "v_exp_f32 v247, v247\n"
  "v_mfma_f32_16x16x32_bf16 a[116:119], a[188:191], v[28:31], a[116:119]\n"
  "ds_read_b64_tr_b16 a[174:175], v221 offset:61440\n"
  "v_cvt_pk_bf16_f32 v175, v246, v247\n"
  "v_mfma_f32_16x16x32_bf16 v[252:255], a[144:147], v[12:15], v[140:143]\n"
  "s_add_u32 s10, s10, L_h9-L_h8\n"
  "v_exp_f32 v248, v248\n"
  "v_mfma_f32_16x16x32_bf16 v[252:255], a[148:151], v[44:47], v[252:255]\n"
  "ds_read_b128 a[144:147], v216 offset:4096\n"
  "v_xor_b32 v221, 0x10000, v221\n"
  "v_mfma_f32_16x16x32_bf16 v[252:255], a[152:155], v[76:79], v[252:255]\n"
  "s_addc_u32 s11, s11, 0\n"
  "v_exp_f32 v249, v249\n"
  "v_mfma_f32_16x16x32_bf16 v[252:255], a[156:159], v[108:111], v[252:255]\n"
  "ds_read_b64_tr_b16 a[176:177], v222 offset:49152\n"
  "v_cvt_pk_bf16_f32 v182, v248, v249\n"
  "v_mfma_f32_16x16x32_bf16 a[120:123], a[184:187], v[48:51], a[120:123]\n"
  "s_mov_b32 s59, 33792\n"
  "v_exp_f32 v250, v250\n"
  "v_mfma_f32_16x16x32_bf16 a[120:123], a[188:191], v[52:55], a[120:123]\n"
  "buffer_load_dwordx4 v[84:87], v192, s[72:75], s59 offen sc1\n"
  "v_mfma_f32_16x16x32_bf16 a[124:127], a[184:187], v[56:59], a[124:127]\n"
  "s_waitcnt lgkmcnt(2)\n"
  "v_exp_f32 v251, v251\n"
  "v_mfma_f32_16x16x32_bf16 a[124:127], a[188:191], v[60:63], a[124:127]\n"
  "ds_read_b64_tr_b16 a[178:179], v222 offset:53248\n"
  "v_cvt_pk_bf16_f32 v183, v250, v251\n"
  "v_smfmac_f32_16x16x64_bf16 a[252:255], v[144:147], v[56:63], v148\n"
  "s_branch L_att\n"
  ".p2align 6\n"
  "L_h9:\n"
  "v_mfma_f32_16x16x32_bf16 a[100:103], a[176:179], v[24:27], a[100:103]\n"
  "v_exp_f32 v255, v255\n"
  "v_mfma_f32_16x16x32_bf16 a[100:103], a[180:183], v[28:31], a[100:103]\n"
  "ds_read_b64_tr_b16 a[166:167], v220 offset:61440\n"
  "v_cvt_pk_bf16_f32 v189, v254, v255\n"
  "v_mfma_f32_16x16x32_bf16 v[244:247], a[144:147], v[4:7], v[132:135]\n"
  "v_exp_f32 v240, v240\n"
  "v_mfma_f32_16x16x32_bf16 v[244:247], a[148:151], v[36:39], v[244:247]\n"
  "ds_read_b128 a[136:139], v218 offset:0\n"
  "v_xor_b32 v220, 0x10000, v220\n"
  "v_mfma_f32_16x16x32_bf16 v[244:247], a[152:155], v[68:71], v[244:247]\n"
  "v_exp_f32 v241, v241\n"
  "v_mfma_f32_16x16x32_bf16 v[244:247], a[156:159], v[100:103], v[244:247]\n"
  "ds_read_b64_tr_b16 a[168:169], v221 offset:49152\n"
  "v_cvt_pk_bf16_f32 v166, v240, v241\n"
  "v_mfma_f32_16x16x32_bf16 a[104:107], a[176:179], v[48:51], a[104:107]\n"
  "s_mov_b32 s57, 2048\n"
  "v_exp_f32 v242, v242\n"
  "v_mfma_f32_16x16x32_bf16 a[104:107], a[180:183], v[52:55], a[104:107]\n"
  "buffer_load_dwordx4 a[212:215], v192, s[72:75], s57 offen sc1\n"
  "v_mfma_f32_16x16x32_bf16 a[108:111], a[176:179], v[56:59], a[108:111]\n"
  "v_exp_f32 v243, v243\n"
  "v_mfma_f32_16x16x32_bf16 a[108:111], a[180:183], v[60:63], a[108:111]\n"
  "ds_read_b64_tr_b16 a[170:171], v221 offset:53248\n"
  "v_cvt_pk_bf16_f32 v167, v242, v243\n"
  "v_mfma_f32_16x16x32_bf16 v[248:251], a[144:147], v[8:11], v[136:139]\n"
  "v_exp_f32 v244, v244\n"
  "v_mfma_f32_16x16x32_bf16 v[248:251], a[148:151], v[40:43], v[248:251]\n"
  "ds_read_b128 a[140:143], v219 offset:0\n"
  "v_mfma_f32_16x16x32_bf16 v[248:251], a[152:155], v[72:75], v[248:251]\n"
  "v_exp_f32 v245, v245\n"
  "v_mfma_f32_16x16x32_bf16 v[248:251], a[156:159], v[104:107], v[248:251]\n"
  "ds_read_b64_tr_b16 a[172:173], v221 offset:57344\n"
  "v_cvt_pk_bf16_f32 v174, v244, v245\n"
  "v_mfma_f32_16x16x32_bf16 a[112:115], a[184:187], v[16:19], a[112:115]\n"
  "s_mov_b32 s58, 34816\n"
  "v_exp_f32 v246, v246\n"
  "v_mfma_f32_16x16x32_bf16 a[112:115], a[188:191], v[20:23], a[112:115]\n"
  "buffer_load_dwordx4 a[236:239], v192, s[72:75], s58 offen sc1\n"
  "v_mfma_f32_16x16x32_bf16 a[116:119], a[184:187], v[24:27], a[116:119]\n"
  "v_exp_f32 v247, v247\n"
  "v_mfma_f32_16x16x32_bf16 a[116:119], a[188:191], v[28:31], a[116:119]\n"
  "ds_read_b64_tr_b16 a[174:175], v221 offset:61440\n"
  "v_cvt_pk_bf16_f32 v175, v246, v247\n"
  "v_mfma_f32_16x16x32_bf16 v[252:255], a[144:147], v[12:15], v[140:143]\n"
  "s_add_u32 s10, s10, L_h10-L_h9\n"
  "v_exp_f32 v248, v248\n"
  "v_mfma_f32_16x16x32_bf16 v[252:255], a[148:151], v[44:47], v[252:255]\n"
  "ds_read_b128 a[144:147], v216 offset:4096\n"
  "v_xor_b32 v221, 0x10000, v221\n"
  "v_mfma_f32_16x16x32_bf16 v[252:255], a[152:155], v[76:79], v[252:255]\n"
  "s_addc_u32 s11, s11, 0\n"
  "v_exp_f32 v249, v249\n"
  "v_mfma_f32_16x16x32_bf16 v[252:255], a[156:159], v[108:111], v[252:255]\n"
  "ds_read_b64_tr_b16 a[176:177], v222 offset:49152\n"
  "v_cvt_pk_bf16_f32 v182, v248, v249\n"
  "v_mfma_f32_16x16x32_bf16 a[120:123], a[184:187], v[48:51], a[120:123]\n"
  "s_mov_b32 s59, 3072\n"
  "v_exp_f32 v250, v250\n"
  "v_mfma_f32_16x16x32_bf16 a[120:123], a[188:191], v[52:55], a[120:123]\n"
  "buffer_load_dwordx4 a[208:211], v192, s[72:75], s59 offen sc1\n"
  "v_mfma_f32_16x16x32_bf16 a[124:127], a[184:187], v[56:59], a[124:127]\n"
  "s_waitcnt lgkmcnt(2)\n"
  "v_exp_f32 v251, v251\n"
  "v_mfma_f32_16x16x32_bf16 a[124:127], a[188:191], v[60:63], a[124:127]\n"
  "ds_read_b64_tr_b16 a[178:179], v222 offset:53248\n"
  "v_cvt_pk_bf16_f32 v183, v250, v251\n"
  "v_smfmac_f32_16x16x64_bf16 a[252:255], v[144:147], v[56:63], v148\n"
  "s_branch L_att\n"
  ".p2align 6\n"
  "L_h10:\n"
  "v_mfma_f32_16x16x32_bf16 a[100:103], a[176:179], v[24:27], a[100:103]\n"
  "v_exp_f32 v255, v255\n"
  "v_mfma_f32_16x16x32_bf16 a[100:103], a[180:183], v[28:31], a[100:103]\n"
  "ds_read_b64_tr_b16 a[166:167], v220 offset:61440\n"
  "v_cvt_pk_bf16_f32 v189, v254, v255\n"
  "v_mfma_f32_16x16x32_bf16 v[244:247], a[144:147], v[4:7], v[132:135]\n"
  "v_exp_f32 v240, v240\n"
  "v_mfma_f32_16x16x32_bf16 v[244:247], a[148:151], v[36:39], v[244:247]\n"
  "ds_read_b128 a[136:139], v218 offset:0\n"
  "v_xor_b32 v220, 0x10000, v220\n"
  "v_mfma_f32_16x16x32_bf16 v[244:247], a[152:155], v[68:71], v[244:247]\n"
  "v_exp_f32 v241, v241\n"
  "v_mfma_f32_16x16x32_bf16 v[244:247], a[156:159], v[100:103], v[244:247]\n"
  "ds_read_b64_tr_b16 a[168:169], v221 offset:49152\n"
  "v_cvt_pk_bf16_f32 v166, v240, v241\n"
  "v_mfma_f32_16x16x32_bf16 a[104:107], a[176:179], v[48:51], a[104:107]\n"
  "s_mov_b32 s57, 35840\n"
  "v_exp_f32 v242, v242\n"
  "v_mfma_f32_16x16x32_bf16 a[104:107], a[180:183], v[52:55], a[104:107]\n"
  "buffer_load_dwordx4 a[232:235], v192, s[72:75], s57 offen sc1\n"
  "v_mfma_f32_16x16x32_bf16 a[108:111], a[176:179], v[56:59], a[108:111]\n"
  "v_exp_f32 v243, v243\n"
  "v_mfma_f32_16x16x32_bf16 a[108:111], a[180:183], v[60:63], a[108:111]\n"
  "ds_read_b64_tr_b16 a[170:171], v221 offset:53248\n"
  "v_cvt_pk_bf16_f32 v167, v242, v243\n"
  "v_mfma_f32_16x16x32_bf16 v[248:251], a[144:147], v[8:11], v[136:139]\n"
  "v_exp_f32 v244, v244\n"
  "v_mfma_f32_16x16x32_bf16 v[248:251], a[148:151], v[40:43], v[248:251]\n"
  "ds_read_b128 a[140:143], v219 offset:0\n"
  "v_mfma_f32_16x16x32_bf16 v[248:251], a[152:155], v[72:75], v[248:251]\n"
  "v_exp_f32 v245, v245\n"
  "v_mfma_f32_16x16x32_bf16 v[248:251], a[156:159], v[104:107], v[248:251]\n"
  "ds_read_b64_tr_b16 a[172:173], v221 offset:57344\n"
  "v_cvt_pk_bf16_f32 v174, v244, v245\n"
  "v_mfma_f32_16x16x32_bf16 a[112:115], a[184:187], v[16:19], a[112:115]\n"
  "s_mov_b32 s58, 4096\n"
  "v_exp_f32 v246, v246\n"
  "v_mfma_f32_16x16x32_bf16 a[112:115], a[188:191], v[20:23], a[112:115]\n"
  "buffer_load_dwordx4 a[204:207], v192, s[72:75], s58 offen sc1\n"
  "v_mfma_f32_16x16x32_bf16 a[116:119], a[184:187], v[24:27], a[116:119]\n"
  "v_exp_f32 v247, v247\n"
  "v_mfma_f32_16x16x32_bf16 a[116:119], a[188:191], v[28:31], a[116:119]\n"
  "ds_read_b64_tr_b16 a[174:175], v221 offset:61440\n"
  "v_cvt_pk_bf16_f32 v175, v246, v247\n"
  "v_mfma_f32_16x16x32_bf16 v[252:255], a[144:147], v[12:15], v[140:143]\n"
  "s_add_u32 s10, s10, L_h11-L_h10\n"
  "v_exp_f32 v248, v248\n"
  "v_mfma_f32_16x16x32_bf16 v[252:255], a[148:151], v[44:47], v[252:255]\n"
  "ds_read_b128 a[144:147], v216 offset:4096\n"
  "v_xor_b32 v221, 0x10000, v221\n"
  "v_mfma_f32_16x16x32_bf16 v[252:255], a[152:155], v[76:79], v[252:255]\n"
  "s_addc_u32 s11, s11, 0\n"
  "v_exp_f32 v249, v249\n"
  "v_mfma_f32_16x16x32_bf16 v[252:255], a[156:159], v[108:111], v[252:255]\n"
  "ds_read_b64_tr_b16 a[176:177], v222 offset:49152\n"
  "v_cvt_pk_bf16_f32 v182, v248, v249\n"
  "v_mfma_f32_16x16x32_bf16 a[120:123], a[184:187], v[48:51], a[120:123]\n"
  "s_mov_b32 s59, 36864\n"
  "v_exp_f32 v250, v250\n"
  "v_mfma_f32_16x16x32_bf16 a[120:123], a[188:191], v[52:55], a[120:123]\n"
  "buffer_load_dwordx4 a[228:231], v192, s[72:75], s59 offen sc1\n"
  "v_mfma_f32_16x16x32_bf16 a[124:127], a[184:187], v[56:59], a[124:127]\n"
  "s_waitcnt lgkmcnt(2)\n"
  "v_exp_f32 v251, v251\n"
  "v_mfma_f32_16x16x32_bf16 a[124:127], a[188:191], v[60:63], a[124:127]\n"
  "ds_read_b64_tr_b16 a[178:179], v222 offset:53248\n"
  "v_cvt_pk_bf16_f32 v183, v250, v251\n"
  "v_smfmac_f32_16x16x64_bf16 a[252:255], v[144:147], v[56:63], v148\n"
  "s_branch L_att\n"
  ".p2align 6\n"
  "L_h11:\n"
  "v_mfma_f32_16x16x32_bf16 a[100:103], a[176:179], v[24:27], a[100:103]\n"
  "v_exp_f32 v255, v255\n"
  "v_mfma_f32_16x16x32_bf16 a[100:103], a[180:183], v[28:31], a[100:103]\n"
  "ds_read_b64_tr_b16 a[166:167], v220 offset:61440\n"
  "v_cvt_pk_bf16_f32 v189, v254, v255\n"
  "v_mfma_f32_16x16x32_bf16 v[244:247], a[144:147], v[4:7], v[132:135]\n"
  "v_exp_f32 v240, v240\n"
  "v_mfma_f32_16x16x32_bf16 v[244:247], a[148:151], v[36:39], v[244:247]\n"
  "ds_read_b128 a[136:139], v218 offset:0\n"
  "v_xor_b32 v220, 0x10000, v220\n"
  "v_mfma_f32_16x16x32_bf16 v[244:247], a[152:155], v[68:71], v[244:247]\n"
  "v_exp_f32 v241, v241\n"
  "v_mfma_f32_16x16x32_bf16 v[244:247], a[156:159], v[100:103], v[244:247]\n"
  "ds_read_b64_tr_b16 a[168:169], v221 offset:49152\n"
  "v_cvt_pk_bf16_f32 v166, v240, v241\n"
  "v_mfma_f32_16x16x32_bf16 a[104:107], a[176:179], v[48:51], a[104:107]\n"
  "s_mov_b32 s57, 5120\n"
  "v_exp_f32 v242, v242\n"
  "v_mfma_f32_16x16x32_bf16 a[104:107], a[180:183], v[52:55], a[104:107]\n"
  "buffer_load_dwordx4 a[200:203], v192, s[72:75], s57 offen sc1\n"
  "v_mfma_f32_16x16x32_bf16 a[108:111], a[176:179], v[56:59], a[108:111]\n"
  "v_exp_f32 v243, v243\n"
  "v_mfma_f32_16x16x32_bf16 a[108:111], a[180:183], v[60:63], a[108:111]\n"
  "ds_read_b64_tr_b16 a[170:171], v221 offset:53248\n"
  "v_cvt_pk_bf16_f32 v167, v242, v243\n"
  "v_mfma_f32_16x16x32_bf16 v[248:251], a[144:147], v[8:11], v[136:139]\n"
  "v_exp_f32 v244, v244\n"
  "v_mfma_f32_16x16x32_bf16 v[248:251], a[148:151], v[40:43], v[248:251]\n"
  "ds_read_b128 a[140:143], v219 offset:0\n"
  "v_mfma_f32_16x16x32_bf16 v[248:251], a[152:155], v[72:75], v[248:251]\n"
  "v_exp_f32 v245, v245\n"
  "v_mfma_f32_16x16x32_bf16 v[248:251], a[156:159], v[104:107], v[248:251]\n"
  "ds_read_b64_tr_b16 a[172:173], v221 offset:57344\n"
  "v_cvt_pk_bf16_f32 v174, v244, v245\n"
  "v_mfma_f32_16x16x32_bf16 a[112:115], a[184:187], v[16:19], a[112:115]\n"
  "s_mov_b32 s58, 37888\n"
  "v_exp_f32 v246, v246\n"
  "v_mfma_f32_16x16x32_bf16 a[112:115], a[188:191], v[20:23], a[112:115]\n"
  "buffer_load_dwordx4 a[224:227], v192, s[72:75], s58 offen sc1\n"
  "v_mfma_f32_16x16x32_bf16 a[116:119], a[184:187], v[24:27], a[116:119]\n"
  "v_exp_f32 v247, v247\n"
  "v_mfma_f32_16x16x32_bf16 a[116:119], a[188:191], v[28:31], a[116:119]\n"
  "ds_read_b64_tr_b16 a[174:175], v221 offset:61440\n"
  "v_cvt_pk_bf16_f32 v175, v246, v247\n"
  "v_mfma_f32_16x16x32_bf16 v[252:255], a[144:147], v[12:15], v[140:143]\n"
  "s_add_u32 s10, s10, L_h12-L_h11\n"
  "v_exp_f32 v248, v248\n"
  "v_mfma_f32_16x16x32_bf16 v[252:255], a[148:151], v[44:47], v[252:255]\n"
  "ds_read_b128 a[144:147], v216 offset:4096\n"
  "v_xor_b32 v221, 0x10000, v221\n"
  "v_mfma_f32_16x16x32_bf16 v[252:255], a[152:155], v[76:79], v[252:255]\n"
  "s_addc_u32 s11, s11, 0\n"
  "v_exp_f32 v249, v249\n"
  "v_mfma_f32_16x16x32_bf16 v[252:255], a[156:159], v[108:111], v[252:255]\n"
  "ds_read_b64_tr_b16 a[176:177], v222 offset:49152\n"
  "v_cvt_pk_bf16_f32 v182, v248, v249\n"
  "v_mfma_f32_16x16x32_bf16 a[120:123], a[184:187], v[48:51], a[120:123]\n"
  "s_mov_b32 s59, 6144\n"
  "v_exp_f32 v250, v250\n"
  "v_mfma_f32_16x16x32_bf16 a[120:123], a[188:191], v[52:55], a[120:123]\n"
  "buffer_load_dwordx4 a[196:199], v192, s[72:75], s59 offen sc1\n"
  "v_mfma_f32_16x16x32_bf16 a[124:127], a[184:187], v[56:59], a[124:127]\n"
  "s_waitcnt lgkmcnt(2)\n"
  "v_exp_f32 v251, v251\n"
  "v_mfma_f32_16x16x32_bf16 a[124:127], a[188:191], v[60:63], a[124:127]\n"
  "ds_read_b64_tr_b16 a[178:179], v222 offset:53248\n"
  "v_cvt_pk_bf16_f32 v183, v250, v251\n"
  "v_smfmac_f32_16x16x64_bf16 a[252:255], v[144:147], v[56:63], v148\n"
  "s_branch L_att\n"
  ".p2align 6\n"
  "L_h12:\n"
  "v_mfma_f32_16x16x32_bf16 a[100:103], a[176:179], v[24:27], a[100:103]\n"
  "v_exp_f32 v255, v255\n"
  "v_mfma_f32_16x16x32_bf16 a[100:103], a[180:183], v[28:31], a[100:103]\n"
  "ds_read_b64_tr_b16 a[166:167], v220 offset:61440\n"
  "v_cvt_pk_bf16_f32 v189, v254, v255\n"
  "v_mfma_f32_16x16x32_bf16 v[244:247], a[144:147], v[4:7], v[132:135]\n"
  "v_exp_f32 v240, v240\n"
  "v_mfma_f32_16x16x32_bf16 v[244:247], a[148:151], v[36:39], v[244:247]\n"
  "ds_read_b128 a[136:139], v218 offset:0\n"
  "v_xor_b32 v220, 0x10000, v220\n"
  "v_mfma_f32_16x16x32_bf16 v[244:247], a[152:155], v[68:71], v[244:247]\n"
  "v_exp_f32 v241, v241\n"
  "v_mfma_f32_16x16x32_bf16 v[244:247], a[156:159], v[100:103], v[244:247]\n"
  "ds_read_b64_tr_b16 a[168:169], v221 offset:49152\n"
  "v_cvt_pk_bf16_f32 v166, v240, v241\n"
  "v_mfma_f32_16x16x32_bf16 a[104:107], a[176:179], v[48:51], a[104:107]\n"
  "s_mov_b32 s57, 38912\n"
  "v_exp_f32 v242, v242\n"
  "v_mfma_f32_16x16x32_bf16 a[104:107], a[180:183], v[52:55], a[104:107]\n"
  "buffer_load_dwordx4 a[220:223], v192, s[72:75], s57 offen sc1\n"
  "v_mfma_f32_16x16x32_bf16 a[108:111], a[176:179], v[56:59], a[108:111]\n"
  "v_exp_f32 v243, v243\n"
  "v_mfma_f32_16x16x32_bf16 a[108:111], a[180:183], v[60:63], a[108:111]\n"
  "ds_read_b64_tr_b16 a[170:171], v221 offset:53248\n"
  "v_cvt_pk_bf16_f32 v167, v242, v243\n"
  "v_mfma_f32_16x16x32_bf16 v[248:251], a[144:147], v[8:11], v[136:139]\n"
  "v_exp_f32 v244, v244\n"
  "v_mfma_f32_16x16x32_bf16 v[248:251], a[148:151], v[40:43], v[248:251]\n"
  "ds_read_b128 a[140:143], v219 offset:0\n"
  "v_mfma_f32_16x16x32_bf16 v[248:251], a[152:155], v[72:75], v[248:251]\n"
  "v_exp_f32 v245, v245\n"
  "v_mfma_f32_16x16x32_bf16 v[248:251], a[156:159], v[104:107], v[248:251]\n"
  "ds_read_b64_tr_b16 a[172:173], v221 offset:57344\n"
  "v_cvt_pk_bf16_f32 v174, v244, v245\n"
  "v_mfma_f32_16x16x32_bf16 a[112:115], a[184:187], v[16:19], a[112:115]\n"
  "s_mov_b32 s58, 7168\n"
  "v_exp_f32 v246, v246\n"
  "v_mfma_f32_16x16x32_bf16 a[112:115], a[188:191], v[20:23], a[112:115]\n"
  "buffer_load_dwordx4 a[192:195], v192, s[72:75], s58 offen sc1\n"
  "v_mfma_f32_16x16x32_bf16 a[116:119], a[184:187], v[24:27], a[116:119]\n"
  "v_exp_f32 v247, v247\n"
  "v_mfma_f32_16x16x32_bf16 a[116:119], a[188:191], v[28:31], a[116:119]\n"
  "ds_read_b64_tr_b16 a[174:175], v221 offset:61440\n"
  "v_cvt_pk_bf16_f32 v175, v246, v247\n"
  "v_mfma_f32_16x16x32_bf16 v[252:255], a[144:147], v[12:15], v[140:143]\n"
  "s_add_u32 s10, s10, L_h13-L_h12\n"
  "v_exp_f32 v248, v248\n"
  "v_mfma_f32_16x16x32_bf16 v[252:255], a[148:151], v[44:47], v[252:255]\n"
  "ds_read_b128 a[144:147], v216 offset:4096\n"
  "v_xor_b32 v221, 0x10000, v221\n"
  "v_mfma_f32_16x16x32_bf16 v[252:255], a[152:155], v[76:79], v[252:255]\n"
  "s_addc_u32 s11, s11, 0\n"
  "v_exp_f32 v249, v249\n"
  "v_mfma_f32_16x16x32_bf16 v[252:255], a[156:159], v[108:111], v[252:255]\n"
  "ds_read_b64_tr_b16 a[176:177], v222 offset:49152\n"
  "v_cvt_pk_bf16_f32 v182, v248, v249\n"
  "v_mfma_f32_16x16x32_bf16 a[120:123], a[184:187], v[48:51], a[120:123]\n"
  "s_mov_b32 s59, 39936\n"
  "v_exp_f32 v250, v250\n"
  "v_mfma_f32_16x16x32_bf16 a[120:123], a[188:191], v[52:55], a[120:123]\n"
  "buffer_load_dwordx4 a[216:219], v192, s[72:75], s59 offen sc1\n"
  "v_mfma_f32_16x16x32_bf16 a[124:127], a[184:187], v[56:59], a[124:127]\n"
  "s_waitcnt lgkmcnt(2)\n"
  "v_exp_f32 v251, v251\n"
  "v_mfma_f32_16x16x32_bf16 a[124:127], a[188:191], v[60:63], a[124:127]\n"
  "ds_read_b64_tr_b16 a[178:179], v222 offset:53248\n"
  "v_cvt_pk_bf16_f32 v183, v250, v251\n"
  "v_smfmac_f32_16x16x64_bf16 a[252:255], v[144:147], v[56:63], v148\n"
  "s_branch L_att\n"
  ".p2align 6\n"
  "L_h13:\n"
  "v_mfma_f32_16x16x32_bf16 a[100:103], a[176:179], v[24:27], a[100:103]\n"
  "v_exp_f32 v255, v255\n"
  "v_mfma_f32_16x16x32_bf16 a[100:103], a[180:183], v[28:31], a[100:103]\n"
  "ds_read_b64_tr_b16 a[166:167], v220 offset:61440\n"
  "v_cvt_pk_bf16_f32 v189, v254, v255\n"
  "v_mfma_f32_16x16x32_bf16 v[244:247], a[144:147], v[4:7], v[132:135]\n"
  "v_exp_f32 v240, v240\n"
  "v_mfma_f32_16x16x32_bf16 v[244:247], a[148:151], v[36:39], v[244:247]\n"
  "ds_read_b128 a[136:139], v218 offset:0\n"
  "v_xor_b32 v220, 0x10000, v220\n"
  "v_mfma_f32_16x16x32_bf16 v[244:247], a[152:155], v[68:71], v[244:247]\n"
  "v_exp_f32 v241, v241\n"
  "v_mfma_f32_16x16x32_bf16 v[244:247], a[156:159], v[100:103], v[244:247]\n"
  "ds_read_b64_tr_b16 a[168:169], v221 offset:49152\n"
  "v_cvt_pk_bf16_f32 v166, v240, v241\n"
  "v_mfma_f32_16x16x32_bf16 a[104:107], a[176:179], v[48:51], a[104:107]\n"
  "s_add_u32 m0, s63, 147456\n"
  "s_add_u32 s54, s13, 163840\n"
  "v_exp_f32 v242, v242\n"
  "v_mfma_f32_16x16x32_bf16 a[104:107], a[180:183], v[52:55], a[104:107]\n"
  "buffer_load_dwordx4 v198, s[16:19], s54 offen offset:0 lds\n"
  "v_mfma_f32_16x16x32_bf16 a[108:111], a[176:179], v[56:59], a[108:111]\n"
  "v_exp_f32 v243, v243\n"
  "v_mfma_f32_16x16x32_bf16 a[108:111], a[180:183], v[60:63], a[108:111]\n"
  "ds_read_b64_tr_b16 a[170:171], v221 offset:53248\n"
  "v_cvt_pk_bf16_f32 v167, v242, v243\n"
  "v_mfma_f32_16x16x32_bf16 v[248:251], a[144:147], v[8:11], v[136:139]\n"
  "v_exp_f32 v244, v244\n"
  "v_mfma_f32_16x16x32_bf16 v[248:251], a[148:151], v[40:43], v[248:251]\n"
  "ds_read_b128 a[140:143], v219 offset:0\n"
  "v_mfma_f32_16x16x32_bf16 v[248:251], a[152:155], v[72:75], v[248:251]\n"
  "v_exp_f32 v245, v245\n"
  "v_mfma_f32_16x16x32_bf16 v[248:251], a[156:159], v[104:107], v[248:251]\n"
  "ds_read_b64_tr_b16 a[172:173], v221 offset:57344\n"
  "v_cvt_pk_bf16_f32 v174, v244, v245\n"
  "v_mfma_f32_16x16x32_bf16 a[112:115], a[184:187], v[16:19], a[112:115]\n"
  "s_mov_b32 s9, 0x10000\n"
  "v_exp_f32 v246, v246\n"
  "v_mfma_f32_16x16x32_bf16 a[112:115], a[188:191], v[20:23], a[112:115]\n"
  "s_mov_b64 s[24:25], s[44:45]\n"
  "s_mov_b64 s[28:29], s[46:47]\n"
  "v_mov_b32 v228, v198\n"
  "v_mov_b32 v229, v199\n"
  "v_mov_b32 v230, v200\n"
  "v_mov_b32 v231, v201\n"
  "v_mfma_f32_16x16x32_bf16 a[116:119], a[184:187], v[24:27], a[116:119]\n"
  "v_exp_f32 v247, v247\n"
  "v_mfma_f32_16x16x32_bf16 a[116:119], a[188:191], v[28:31], a[116:119]\n"
  "ds_read_b64_tr_b16 a[174:175], v221 offset:61440\n"
  "v_cvt_pk_bf16_f32 v175, v246, v247\n"
  "v_mfma_f32_16x16x32_bf16 v[252:255], a[144:147], v[12:15], v[140:143]\n"
  "s_add_u32 s10, s10, L_h14-L_h13\n"
  "v_exp_f32 v248, v248\n"
  "v_mfma_f32_16x16x32_bf16 v[252:255], a[148:151], v[44:47], v[252:255]\n"
  "ds_read_b128 a[144:147], v216 offset:4096\n"
  "v_xor_b32 v221, 0x10000, v221\n"
  "v_mfma_f32_16x16x32_bf16 v[252:255], a[152:155], v[76:79], v[252:255]\n"
  "s_addc_u32 s11, s11, 0\n"
  "v_exp_f32 v249, v249\n"
  "v_mfma_f32_16x16x32_bf16 v[252:255], a[156:159], v[108:111], v[252:255]\n"
  "ds_read_b64_tr_b16 a[176:177], v222 offset:49152\n"
  "v_cvt_pk_bf16_f32 v182, v248, v249\n"
  "v_mfma_f32_16x16x32_bf16 a[120:123], a[184:187], v[48:51], a[120:123]\n"
  "s_mov_b32 s61, 0x70000\n"
  "v_exp_f32 v250, v250\n"
  "v_mfma_f32_16x16x32_bf16 a[120:123], a[188:191], v[52:55], a[120:123]\n"
  "global_load_dword v209, v205, s[10:11]\n"
  "v_mov_b32 v232, v198\n"
  "v_mov_b32 v233, v199\n"
  "v_mov_b32 v234, v200\n"
  "v_mov_b32 v235, v201\n"
  "v_mfma_f32_16x16x32_bf16 a[124:127], a[184:187], v[56:59], a[124:127]\n"
  "s_waitcnt lgkmcnt(2)\n"
  "v_exp_f32 v251, v251\n"
  "v_mfma_f32_16x16x32_bf16 a[124:127], a[188:191], v[60:63], a[124:127]\n"
  "ds_read_b64_tr_b16 a[178:179], v222 offset:53248\n"
  "v_cvt_pk_bf16_f32 v183, v250, v251\n"
  "v_smfmac_f32_16x16x64_bf16 a[252:255], v[144:147], v[56:63], v148\n"
  "s_branch L_att\n"
  ".p2align 6\n"
  "L_h14:\n"
  "v_mfma_f32_16x16x32_bf16 a[100:103], a[176:179], v[24:27], a[100:103]\n"
  "v_exp_f32 v255, v255\n"
  "v_mfma_f32_16x16x32_bf16 a[100:103], a[180:183], v[28:31], a[100:103]\n"
  "ds_read_b64_tr_b16 a[166:167], v220 offset:61440\n"
  "v_cvt_pk_bf16_f32 v189, v254, v255\n"
  "v_mfma_f32_16x16x32_bf16 v[244:247], a[144:147], v[4:7], v[132:135]\n"
  "v_exp_f32 v240, v240\n"
  "v_mfma_f32_16x16x32_bf16 v[244:247], a[148:151], v[36:39], v[244:247]\n"
  "ds_read_b128 a[136:139], v218 offset:0\n"
  "v_xor_b32 v220, 0x10000, v220\n"
  "v_mfma_f32_16x16x32_bf16 v[244:247], a[152:155], v[68:71], v[244:247]\n"
  "v_exp_f32 v241, v241\n"
  "v_mfma_f32_16x16x32_bf16 v[244:247], a[156:159], v[100:103], v[244:247]\n"
  "ds_read_b64_tr_b16 a[168:169], v221 offset:49152\n"
  "v_cvt_pk_bf16_f32 v166, v240, v241\n"
  "v_mfma_f32_16x16x32_bf16 a[104:107], a[176:179], v[48:51], a[104:107]\n"
  "s_add_u32 m0, s63, 147456\n"
  "s_add_u32 s54, s13, 163840\n"
  "v_exp_f32 v242, v242\n"
  "v_mfma_f32_16x16x32_bf16 a[104:107], a[180:183], v[52:55], a[104:107]\n"
  "buffer_load_dwordx4 v199, s[16:19], s54 offen offset:1024 lds\n"
  "v_mfma_f32_16x16x32_bf16 a[108:111], a[176:179], v[56:59], a[108:111]\n"
  "v_exp_f32 v243, v243\n"
  "v_mfma_f32_16x16x32_bf16 a[108:111], a[180:183], v[60:63], a[108:111]\n"
  "ds_read_b64_tr_b16 a[170:171], v221 offset:53248\n"
  "v_cvt_pk_bf16_f32 v167, v242, v243\n"
  "v_mfma_f32_16x16x32_bf16 v[248:251], a[144:147], v[8:11], v[136:139]\n"
  "v_exp_f32 v244, v244\n"
  "v_mfma_f32_16x16x32_bf16 v[248:251], a[148:151], v[40:43], v[248:251]\n"
  "ds_read_b128 a[140:143], v219 offset:0\n"
  "v_mfma_f32_16x16x32_bf16 v[248:251], a[152:155], v[72:75], v[248:251]\n"
  "v_exp_f32 v245, v245\n"
  "v_mfma_f32_16x16x32_bf16 v[248:251], a[156:159], v[104:107], v[248:251]\n"
  "ds_read_b64_tr_b16 a[172:173], v221 offset:57344\n"
  "v_cvt_pk_bf16_f32 v174, v244, v245\n"
  "v_mfma_f32_16x16x32_bf16 a[112:115], a[184:187], v[16:19], a[112:115]\n"
  "v_exp_f32 v246, v246\n"
  "v_mfma_f32_16x16x32_bf16 a[112:115], a[188:191], v[20:23], a[112:115]\n"
  "buffer_load_dwordx4 v200, s[16:19], s54 offen offset:2048 lds\n"
  "v_mfma_f32_16x16x32_bf16 a[116:119], a[184:187], v[24:27], a[116:119]\n"
  "v_exp_f32 v247, v247\n"
  "v_mfma_f32_16x16x32_bf16 a[116:119], a[188:191], v[28:31], a[116:119]\n"
  "ds_read_b64_tr_b16 a[174:175], v221 offset:61440\n"
  "v_cvt_pk_bf16_f32 v175, v246, v247\n"
  "v_mfma_f32_16x16x32_bf16 v[252:255], a[144:147], v[12:15], v[140:143]\n"
  "s_add_u32 s10, s10, L_h14-L_h14\n"
  "v_exp_f32 v248, v248\n"
  "v_mfma_f32_16x16x32_bf16 v[252:255], a[148:151], v[44:47], v[252:255]\n"
  "ds_read_b128 a[144:147], v216 offset:4096\n"
  "v_xor_b32 v221, 0x10000, v221\n"
  "v_mfma_f32_16x16x32_bf16 v[252:255], a[152:155], v[76:79], v[252:255]\n"
  "s_addc_u32 s11, s11, 0\n"
  "v_exp_f32 v249, v249\n"
  "v_mfma_f32_16x16x32_bf16 v[252:255], a[156:159], v[108:111], v[252:255]\n"
  "ds_read_b64_tr_b16 a[176:177], v222 offset:49152\n"
  "v_cvt_pk_bf16_f32 v182, v248, v249\n"
  "v_mfma_f32_16x16x32_bf16 a[120:123], a[184:187], v[48:51], a[120:123]\n"
  "v_exp_f32 v250, v250\n"
  "v_mfma_f32_16x16x32_bf16 a[120:123], a[188:191], v[52:55], a[120:123]\n"
  "buffer_load_dwordx4 v201, s[16:19], s54 offen offset:3072 lds\n"
  "v_mfma_f32_16x16x32_bf16 a[124:127], a[184:187], v[56:59], a[124:127]\n"
  "s_waitcnt lgkmcnt(2)\n"
  "v_exp_f32 v251, v251\n"
  "v_mfma_f32_16x16x32_bf16 a[124:127], a[188:191], v[60:63], a[124:127]\n"
  "ds_read_b64_tr_b16 a[178:179], v222 offset:53248\n"
  "v_cvt_pk_bf16_f32 v183, v250, v251\n"
  "v_smfmac_f32_16x16x64_bf16 a[252:255], v[144:147], v[56:63], v148\n"
  "v_mfma_f32_16x16x32_bf16 a[0:3], a[160:163], v[160:163], a[0:3]\n"
  "ds_read_b64_tr_b16 a[180:181], v222 offset:57344\n"
  "ds_read_b64_tr_b16 a[128:129], v194 offset:0\n"
  "v_exp_f32 v252, v252\n"
  "v_mfma_f32_16x16x32_bf16 a[0:3], a[164:167], v[164:167], a[0:3]\n"
  "ds_read_b64_tr_b16 a[182:183], v222 offset:61440\n"
  "ds_read_b64_tr_b16 a[130:131], v194 offset:2048\n"
  "v_exp_f32 v253, v253\n"
  "v_mfma_f32_16x16x32_bf16 a[4:7], a[160:163], v[168:171], a[4:7]\n"
  "s_add_u32 s72, s72, 0x1000000\n"
  "s_addc_u32 s73, s73, 0\n"
  "ds_read_b64_tr_b16 a[184:185], v223 offset:49152\n"
  "ds_read_b64_tr_b16 a[136:137], v195 offset:0\n"
  "v_exp_f32 v254, v254\n"
  "v_mfma_f32_16x16x32_bf16 a[4:7], a[164:167], v[172:175], a[4:7]\n"
  "ds_read_b64_tr_b16 a[186:187], v223 offset:53248\n"
  "ds_read_b64_tr_b16 a[138:139], v195 offset:2048\n"
  "v_exp_f32 v255, v255\n"
  "v_smfmac_f32_16x16x64_bf16 a[240:243], v[144:147], v[160:167], v148\n"
  "ds_read_b64_tr_b16 a[188:189], v223 offset:57344\n"
  "ds_read_b64_tr_b16 a[144:145], v196 offset:0\n"
  "v_cvt_pk_bf16_f32 v190, v252, v253\n"
  "v_smfmac_f32_16x16x64_bf16 a[244:247], v[144:147], v[168:175], v148\n"
  "ds_read_b64_tr_b16 a[190:191], v223 offset:61440\n"
  "ds_read_b64_tr_b16 a[146:147], v196 offset:2048\n"
  "v_cvt_pk_bf16_f32 v191, v254, v255\n"
  "v_smfmac_f32_16x16x64_bf16 a[248:251], v[144:147], v[176:183], v148\n"
  "ds_read_b64_tr_b16 a[152:153], v197 offset:0\n"
  "ds_read_b64_tr_b16 a[154:155], v197 offset:2048\n"
  "v_mfma_f32_16x16x32_bf16 a[8:11], a[160:163], v[176:179], a[8:11]\n"
  "s_waitcnt vmcnt(11)\n"
  "ds_read_b64_tr_b16 a[132:133], v194 offset:16384\n"
  "ds_read_b64_tr_b16 a[134:135], v194 offset:18432\n"
  "v_mfma_f32_16x16x32_bf16 a[8:11], a[164:167], v[180:183], a[8:11]\n"
  "ds_read_b64_tr_b16 a[140:141], v195 offset:16384\n"
  "ds_read_b64_tr_b16 a[142:143], v195 offset:18432\n"
  "v_mfma_f32_16x16x32_bf16 a[12:15], a[160:163], v[184:187], a[12:15]\n"
  "ds_read_b64_tr_b16 a[148:149], v196 offset:16384\n"
  "ds_read_b64_tr_b16 a[150:151], v196 offset:18432\n"
  "v_mfma_f32_16x16x32_bf16 a[12:15], a[164:167], v[188:191], a[12:15]\n"
  "ds_read_b64_tr_b16 a[160:161], v224 offset:49152\n"
  "ds_read_b64_tr_b16 a[156:157], v197 offset:16384\n"
  "v_smfmac_f32_16x16x64_bf16 a[252:255], v[144:147], v[184:191], v148\n"
  "ds_read_b64_tr_b16 a[162:163], v224 offset:53248\n"
  "ds_read_b64_tr_b16 a[158:159], v197 offset:18432\n"
  "v_mfma_f32_16x16x32_bf16 a[16:19], a[168:171], v[160:163], a[16:19]\n"
  "ds_read_b64_tr_b16 a[164:165], v224 offset:57344\n"
  "v_mfma_f32_16x16x32_bf16 a[16:19], a[172:175], v[164:167], a[16:19]\n"
  "s_add_u32 m0, s63, 0\n"
  "s_add_u32 s53, s13, 196608\n"
  "ds_read_b64_tr_b16 a[166:167], v224 offset:61440\n"
  "v_accvgpr_read_b32 v88, a240\n"
  "v_accvgpr_read_b32 v89, a241\n"
  "v_accvgpr_read_b32 v90, a244\n"
  "v_mfma_f32_16x16x32_bf16 a[20:23], a[168:171], v[168:171], a[20:23]\n"
  "s_waitcnt lgkmcnt(12)\n"
  "buffer_load_dwordx4 v198, s[16:19], s53 offen offset:0 lds\n"
  "v_accvgpr_read_b32 v91, a245\n"
  "v_accvgpr_read_b32 v92, a248\n"
  "v_accvgpr_read_b32 v93, a249\n"
  "v_mfma_f32_16x16x32_bf16 a[20:23], a[172:175], v[172:175], a[20:23]\n"
  "v_accvgpr_read_b32 v94, a252\n"
  "v_accvgpr_read_b32 v95, a253\n"
  "v_add_f32 v88, v88, v89\n"
  "v_mfma_f32_16x16x32_bf16 a[24:27], a[168:171], v[176:179], a[24:27]\n"
  "v_add_f32 v90, v90, v91\n"
  "v_add_f32 v92, v92, v93\n"
  "v_add_f32 v94, v94, v95\n"
  "v_mfma_f32_16x16x32_bf16 a[24:27], a[172:175], v[180:183], a[24:27]\n"
  "ds_bpermute_b32 v112, v212, v88\n"
  "v_mfma_f32_16x16x32_bf16 a[28:31], a[168:171], v[184:187], a[28:31]\n"
  "ds_bpermute_b32 v113, v212, v90\n"
  "buffer_load_dwordx4 v199, s[16:19], s53 offen offset:1024 lds\n"
  "v_mfma_f32_16x16x32_bf16 a[28:31], a[172:175], v[188:191], a[28:31]\n"
  "ds_read_b64_tr_b16 a[168:169], v225 offset:49152\n"
  "ds_bpermute_b32 v114, v212, v92\n"
  "v_mfma_f32_16x16x32_bf16 v[136:139], v[150:153], a[216:219], 0\n"
  "ds_read_b64_tr_b16 a[170:171], v225 offset:53248\n"
  "ds_bpermute_b32 v115, v212, v94\n"
  "v_add_u32 v228, 0x20000, v194\n"
  "v_mfma_f32_16x16x32_bf16 v[140:143], v[154:157], a[218:221], 0\n"
  "ds_read_b64_tr_b16 a[172:173], v225 offset:57344\n"
  "v_add_u32 v229, 0x20000, v195\n"
  "v_mfma_f32_16x16x32_bf16 v[136:139], v[154:157], a[222:225], v[136:139]\n"
  "ds_read_b64_tr_b16 a[174:175], v225 offset:61440\n"
  "buffer_load_dwordx4 v200, s[16:19], s53 offen offset:2048 lds\n"
  "v_add_u32 v230, 0x20000, v196\n"
  "v_mfma_f32_16x16x32_bf16 v[140:143], v[154:157], a[226:229], v[140:143]\n"
  "v_add_u32 v231, 0x20000, v197\n"
  "v_mfma_f32_16x16x32_bf16 v[136:139], v[154:157], a[230:233], v[136:139]\n"
  "v_mfma_f32_16x16x32_bf16 v[140:143], v[154:157], a[234:237], v[140:143]\n"
  "s_waitcnt lgkmcnt(2)\n"
  "s_waitcnt lgkmcnt(6)\n"
  "v_add_f32 v116, v112, v113\n"
  "v_add_f32 v117, v114, v115\n"
  "v_add_f32 v116, v116, v117\n"
  "v_mfma_f32_16x16x32_bf16 v[136:139], v[152:155], a[236:239], v[136:139]\n"
  "buffer_load_dwordx4 v201, s[16:19], s53 offen offset:3072 lds\n"
  "v_rcp_f32 v112, v112\n"
  "v_rcp_f32 v113, v113\n"
  "v_rcp_f32 v114, v114\n"
  "v_mfma_f32_16x16x32_bf16 a[32:35], a[176:179], v[160:163], a[32:35]\n"
  "v_rcp_f32 v115, v115\n"
  "v_fmac_f32 v116, 0x3b800000, v112\n"
  "v_fmac_f32 v116, 0x3b800000, v113\n"
  "v_mfma_f32_16x16x32_bf16 a[32:35], a[180:183], v[164:167], a[32:35]\n"
  "L_dry2_2:\n"
  "L_pad10:\n"
  "s_cbranch_execz L_dryr2\n"
  "v_fmac_f32 v116, 0x3b800000, v114\n"
  "v_fmac_f32 v116, 0x3b800000, v115\n"
  "v_cmp_ngt_f32 vcc, 0x53800000, v116\n"
  "v_mfma_f32_16x16x32_bf16 a[36:39], a[176:179], v[168:171], a[36:39]\n"
  "s_add_u32 m0, s63, 16384\n"
  "s_add_u32 s54, s13, 229376\n"
  "s_nop 0\n"
  "s_cbranch_vccnz L_slow\n"
  "v_mfma_f32_16x16x32_bf16 a[36:39], a[180:183], v[172:175], a[36:39]\n"
  "s_waitcnt vmcnt(11)\n"
  "buffer_load_dwordx4 v198, s[16:19], s54 offen offset:0 lds\n"
  "ds_read_b64_tr_b16 v[64:65], v194 offset:32768\n"
  "v_accvgpr_read_b32 v240, a0\n"
  "v_accvgpr_read_b32 v241, a1\n"
  "v_accvgpr_read_b32 v242, a2\n"
  "v_accvgpr_read_b32 v243, a3\n"
  "v_mfma_f32_16x16x32_bf16 v[128:131], v[150:153], a[192:195], 0\n"
  "ds_read_b64_tr_b16 v[66:67], v194 offset:34816\n"
  "ds_read_b64_tr_b16 v[72:73], v195 offset:32768\n"
  "v_accvgpr_read_b32 v244, a16\n"
  "v_accvgpr_read_b32 v245, a17\n"
  "v_accvgpr_read_b32 v246, a18\n"
  "v_accvgpr_read_b32 v247, a19\n"
  "v_mfma_f32_16x16x32_bf16 v[132:135], v[154:157], a[194:197], 0\n"
  "buffer_load_dwordx4 v199, s[16:19], s54 offen offset:1024 lds\n"
  "ds_read_b64_tr_b16 v[74:75], v195 offset:34816\n"
  "v_mul_f32 v240, v112, v240\n"
  "v_mul_f32 v241, v112, v241\n"
  "v_mul_f32 v242, v112, v242\n"
  "v_mul_f32 v243, v112, v243\n"
  "v_mfma_f32_16x16x32_bf16 v[128:131], v[154:157], a[198:201], v[128:131]\n"
  "ds_read_b64_tr_b16 v[96:97], v196 offset:32768\n"
  "ds_read_b64_tr_b16 v[98:99], v196 offset:34816\n"
  "v_mul_f32 v244, v112, v244\n"
  "v_mul_f32 v245, v112, v245\n"
  "v_mul_f32 v246, v112, v246\n"
  "v_mul_f32 v247, v112, v247\n"
  "v_mfma_f32_16x16x32_bf16 v[132:135], v[154:157], a[202:205], v[132:135]\n"
  "buffer_load_dwordx4 v200, s[16:19], s54 offen offset:2048 lds\n"
  "ds_read_b64_tr_b16 v[104:105], v197 offset:32768\n"
  "v_cvt_pk_bf16_f32 v0, v240, v241\n"
  "v_cvt_pk_bf16_f32 v1, v242, v243\n"
  "v_cvt_pk_bf16_f32 v2, v244, v245\n"
  "v_cvt_pk_bf16_f32 v3, v246, v247\n"
  "v_mfma_f32_16x16x32_bf16 v[128:131], v[154:157], a[206:209], v[128:131]\n"
  "s_waitcnt vmcnt(10)\n"
  "ds_read_b64_tr_b16 v[106:107], v197 offset:34816\n"
  "ds_read_b64_tr_b16 v[68:69], v194 offset:49152\n"
  "v_accvgpr_read_b32 v248, a4\n"
  "v_accvgpr_read_b32 v249, a5\n"
  "v_accvgpr_read_b32 v250, a6\n"
  "v_accvgpr_read_b32 v251, a7\n"
  "v_mfma_f32_16x16x32_bf16 v[132:135], v[154:157], a[210:213], v[132:135]\n"
  "buffer_load_dwordx4 v201, s[16:19], s54 offen offset:3072 lds\n"
  "ds_read_b64_tr_b16 v[70:71], v194 offset:51200\n"
  "v_accvgpr_read_b32 v252, a20\n"
  "v_accvgpr_read_b32 v253, a21\n"
  "v_accvgpr_read_b32 v254, a22\n"
  "v_accvgpr_read_b32 v255, a23\n"
  "v_mfma_f32_16x16x32_bf16 v[128:131], v[152:155], a[212:215], v[128:131]\n"
  "ds_read_b64_tr_b16 v[76:77], v195 offset:49152\n"
  "ds_read_b64_tr_b16 v[78:79], v195 offset:51200\n"
  "v_mul_f32 v248, v113, v248\n"
  "v_mul_f32 v249, v113, v249\n"
  "v_mul_f32 v250, v113, v250\n"
  "v_mul_f32 v251, v113, v251\n"
  "v_mfma_f32_16x16x32_bf16 a[40:43], a[176:179], v[176:179], a[40:43]\n"
  "ds_read_b64_tr_b16 v[100:101], v196 offset:49152\n"
  "ds_read_b64_tr_b16 v[102:103], v196 offset:51200\n"
  "v_mul_f32 v252, v113, v252\n"
  "v_mul_f32 v253, v113, v253\n"
  "v_mul_f32 v254, v113, v254\n"
  "v_mul_f32 v255, v113, v255\n"
  "v_mfma_f32_16x16x32_bf16 a[40:43], a[180:183], v[180:183], a[40:43]\n"
  "ds_read_b64_tr_b16 v[108:109], v197 offset:49152\n"
  "ds_read_b64_tr_b16 v[110:111], v197 offset:51200\n"
  "v_cvt_pk_bf16_f32 v4, v248, v249\n"
  "v_cvt_pk_bf16_f32 v5, v250, v251\n"
  "v_cvt_pk_bf16_f32 v6, v252, v253\n"
  "v_cvt_pk_bf16_f32 v7, v254, v255\n"
  "v_mfma_f32_16x16x32_bf16 a[44:47], a[176:179], v[184:187], a[44:47]\n"
  "ds_read_b64_tr_b16 a[224:225], v228 offset:0\n"
  "ds_read_b64_tr_b16 a[226:227], v228 offset:2048\n"
  "v_accvgpr_read_b32 v240, a8\n"
  "v_accvgpr_read_b32 v241, a9\n"
  "v_accvgpr_read_b32 v242, a10\n"
  "v_accvgpr_read_b32 v243, a11\n"
  "v_mfma_f32_16x16x32_bf16 a[44:47], a[180:183], v[188:191], a[44:47]\n"
  "s_add_u32 m0, s63, 32768\n"
  "s_add_u32 s53, s13, 262144\n"
  "ds_read_b64_tr_b16 a[176:177], v226 offset:49152\n"
  "ds_read_b64_tr_b16 a[232:233], v229 offset:0\n"
  "v_accvgpr_read_b32 v244, a24\n"
  "v_accvgpr_read_b32 v245, a25\n"
  "v_accvgpr_read_b32 v246, a26\n"
  "v_accvgpr_read_b32 v247, a27\n"
  "v_mfma_f32_16x16x32_bf16 v[128:131], v[150:153], v[80:83], v[128:131]\n"
  "s_waitcnt lgkmcnt(12)\n"
  "ds_read_b64_tr_b16 a[178:179], v226 offset:53248\n"
  "buffer_load_dwordx4 v198, s[16:19], s53 offen offset:0 lds\n"
  "v_mul_f32 v240, v114, v240\n"
  "v_mul_f32 v241, v114, v241\n"
  "v_mul_f32 v242, v114, v242\n"
  "v_mul_f32 v243, v114, v243\n"
  "v_mfma_f32_16x16x32_bf16 v[132:135], v[152:155], v[80:83], v[132:135]\n"
  "ds_read_b64_tr_b16 a[180:181], v226 offset:57344\n"
  "ds_read_b64_tr_b16 a[234:235], v229 offset:2048\n"
  "v_mul_f32 v244, v114, v244\n"
  "v_mul_f32 v245, v114, v245\n"
  "v_mul_f32 v246, v114, v246\n"
  "v_mul_f32 v247, v114, v247\n"
  "v_mfma_f32_16x16x32_bf16 v[136:139], v[150:153], v[84:87], v[136:139]\n"
  "ds_read_b64_tr_b16 a[182:183], v226 offset:61440\n"
  "buffer_load_dwordx4 v199, s[16:19], s53 offen offset:1024 lds\n"
  "v_cvt_pk_bf16_f32 v8, v240, v241\n"
  "v_cvt_pk_bf16_f32 v9, v242, v243\n"
  "v_cvt_pk_bf16_f32 v10, v244, v245\n"
  "v_cvt_pk_bf16_f32 v11, v246, v247\n"
  "v_mfma_f32_16x16x32_bf16 v[140:143], v[152:155], v[84:87], v[140:143]\n"
  "ds_read_b64_tr_b16 a[240:241], v230 offset:0\n"
  "ds_read_b64_tr_b16 a[242:243], v230 offset:2048\n"
  "v_accvgpr_read_b32 v248, a12\n"
  "v_accvgpr_read_b32 v249, a13\n"
  "v_accvgpr_read_b32 v250, a14\n"
  "v_accvgpr_read_b32 v251, a15\n"
  "v_mfma_f32_16x16x32_bf16 v[128:131], v[150:153], v[120:123], v[128:131]\n"
  "buffer_load_dwordx4 v200, s[16:19], s53 offen offset:2048 lds\n"
  "ds_read_b64_tr_b16 a[248:249], v231 offset:0\n"
  "v_accvgpr_read_b32 v252, a28\n"
  "v_accvgpr_read_b32 v253, a29\n"
  "v_accvgpr_read_b32 v254, a30\n"
  "v_accvgpr_read_b32 v255, a31\n"
  "v_mfma_f32_16x16x32_bf16 v[132:135], v[152:155], v[120:123], v[132:135]\n"
  "s_waitcnt vmcnt(11)\n"
  "ds_read_b64_tr_b16 a[250:251], v231 offset:2048\n"
  "ds_read_b64_tr_b16 a[228:229], v228 offset:16384\n"
  "v_mul_f32 v248, v115, v248\n"
  "v_mul_f32 v249, v115, v249\n"
  "v_mul_f32 v250, v115, v250\n"
  "v_mul_f32 v251, v115, v251\n"
  "v_mfma_f32_16x16x32_bf16 v[136:139], v[150:153], v[124:127], v[136:139]\n"
  "buffer_load_dwordx4 v201, s[16:19], s53 offen offset:3072 lds\n"
  "ds_read_b64_tr_b16 a[230:231], v228 offset:18432\n"
  "v_mul_f32 v252, v115, v252\n"
  "v_mul_f32 v253, v115, v253\n"
  "v_mul_f32 v254, v115, v254\n"
  "v_mul_f32 v255, v115, v255\n"
  "v_mfma_f32_16x16x32_bf16 a[48:51], a[184:187], v[160:163], a[48:51]\n"
  "s_add_u32 m0, s63, 49152\n"
  "s_add_u32 s54, s13, 294912\n"
  "ds_read_b64_tr_b16 a[236:237], v229 offset:16384\n"
  "ds_read_b64_tr_b16 a[238:239], v229 offset:18432\n"
  "v_cvt_pk_bf16_f32 v12, v248, v249\n"
  "v_cvt_pk_bf16_f32 v13, v250, v251\n"
  "v_cvt_pk_bf16_f32 v14, v252, v253\n"
  "v_cvt_pk_bf16_f32 v15, v254, v255\n"
  "v_mfma_f32_16x16x32_bf16 a[48:51], a[188:191], v[164:167], a[48:51]\n"
  "s_waitcnt lgkmcnt(12)\n"
  "buffer_load_dwordx4 v198, s[16:19], s54 offen offset:0 lds\n"
  "ds_read_b64_tr_b16 a[244:245], v230 offset:16384\n"
  "v_mfma_f32_16x16x32_bf16 a[52:55], a[184:187], v[168:171], a[52:55]\n"
  "ds_read_b64_tr_b16 a[246:247], v230 offset:18432\n"
  "ds_read_b64_tr_b16 a[252:253], v231 offset:16384\n"
  "v_mfma_f32_16x16x32_bf16 a[52:55], a[188:191], v[172:175], a[52:55]\n"
  "buffer_load_dwordx4 v199, s[16:19], s54 offen offset:1024 lds\n"
  "ds_read_b64_tr_b16 a[254:255], v231 offset:18432\n"
  "v_mfma_f32_16x16x32_bf16 v[140:143], v[152:155], v[124:127], v[140:143]\n"
  "s_waitcnt vmcnt(10)\n"
  "ds_read_b64_tr_b16 a[192:193], v194 offset:0\n"
  "ds_read_b64_tr_b16 a[194:195], v194 offset:2048\n"
  "v_mfma_f32_16x16x32_bf16 a[56:59], a[184:187], v[176:179], a[56:59]\n"
  "buffer_load_dwordx4 v200, s[16:19], s54 offen offset:2048 lds\n"
  "ds_read_b64_tr_b16 a[200:201], v195 offset:0\n"
  "v_accvgpr_read_b32 v240, a32\n"
  "v_accvgpr_read_b32 v241, a33\n"
  "v_accvgpr_read_b32 v242, a34\n"
  "v_accvgpr_read_b32 v243, a35\n"
  "v_mfma_f32_16x16x32_bf16 a[56:59], a[188:191], v[180:183], a[56:59]\n"
  "ds_read_b64_tr_b16 a[202:203], v195 offset:2048\n"
  "ds_read_b64_tr_b16 a[208:209], v196 offset:0\n"
  "v_accvgpr_read_b32 v244, a48\n"
  "v_accvgpr_read_b32 v245, a49\n"
  "v_accvgpr_read_b32 v246, a50\n"
  "v_accvgpr_read_b32 v247, a51\n"
  "v_mfma_f32_16x16x32_bf16 a[60:63], a[184:187], v[184:187], a[60:63]\n"
  "buffer_load_dwordx4 v201, s[16:19], s54 offen offset:3072 lds\n"
  "ds_read_b64_tr_b16 a[210:211], v196 offset:2048\n"
  "v_mul_f32 v240, v112, v240\n"
  "v_mul_f32 v241, v112, v241\n"
  "v_mul_f32 v242, v112, v242\n"
  "v_mul_f32 v243, v112, v243\n"
  "v_mfma_f32_16x16x32_bf16 a[60:63], a[188:191], v[188:191], a[60:63]\n"
  "s_add_u32 m0, s63, 131072\n"
  "s_add_u32 s53, s13, 327680\n"
  "ds_read_b64_tr_b16 a[184:185], v227 offset:49152\n"
  "ds_read_b64_tr_b16 a[216:217], v197 offset:0\n"
  "v_mul_f32 v244, v112, v244\n"
  "v_mul_f32 v245, v112, v245\n"
  "v_mul_f32 v246, v112, v246\n"
  "v_mul_f32 v247, v112, v247\n"
  "v_mfma_f32_16x16x32_bf16 a[64:67], a[160:163], v[160:163], a[64:67]\n"
  "s_waitcnt lgkmcnt(12)\n"
  "ds_read_b64_tr_b16 a[186:187], v227 offset:53248\n"
  "buffer_load_dwordx4 v198, s[16:19], s53 offen offset:0 lds\n"
  "v_cvt_pk_bf16_f32 v16, v240, v241\n"
  "v_cvt_pk_bf16_f32 v17, v242, v243\n"
  "v_cvt_pk_bf16_f32 v18, v244, v245\n"
  "v_cvt_pk_bf16_f32 v19, v246, v247\n"
  "v_mfma_f32_16x16x32_bf16 a[64:67], a[164:167], v[164:167], a[64:67]\n"
  "ds_read_b64_tr_b16 a[188:189], v227 offset:57344\n"
  "ds_read_b64_tr_b16 a[218:219], v197 offset:2048\n"
  "v_accvgpr_read_b32 v248, a36\n"
  "v_accvgpr_read_b32 v249, a37\n"
  "v_accvgpr_read_b32 v250, a38\n"
  "v_accvgpr_read_b32 v251, a39\n"
  "v_mfma_f32_16x16x32_bf16 a[68:71], a[160:163], v[168:171], a[68:71]\n"
  "ds_read_b64_tr_b16 a[190:191], v227 offset:61440\n"
  "buffer_load_dwordx4 v199, s[16:19], s53 offen offset:1024 lds\n"
  "v_accvgpr_read_b32 v252, a52\n"
  "v_accvgpr_read_b32 v253, a53\n"
  "v_accvgpr_read_b32 v254, a54\n"
  "v_accvgpr_read_b32 v255, a55\n"
  "v_mfma_f32_16x16x32_bf16 a[68:71], a[164:167], v[172:175], a[68:71]\n"
  "s_waitcnt vmcnt(10)\n"
  "ds_read_b64_tr_b16 a[196:197], v194 offset:16384\n"
  "ds_read_b64_tr_b16 a[198:199], v194 offset:18432\n"
  "v_mul_f32 v248, v113, v248\n"
  "v_mul_f32 v249, v113, v249\n"
  "v_mul_f32 v250, v113, v250\n"
  "v_mul_f32 v251, v113, v251\n"
  "v_mfma_f32_16x16x32_bf16 a[72:75], a[160:163], v[176:179], a[72:75]\n"
  "buffer_load_dwordx4 v200, s[16:19], s53 offen offset:2048 lds\n"
  "ds_read_b64_tr_b16 a[204:205], v195 offset:16384\n"
  "v_mul_f32 v252, v113, v252\n"
  "v_mul_f32 v253, v113, v253\n"
  "v_mul_f32 v254, v113, v254\n"
  "v_mul_f32 v255, v113, v255\n"
  "v_mfma_f32_16x16x32_bf16 a[72:75], a[164:167], v[180:183], a[72:75]\n"
  "ds_read_b64_tr_b16 a[206:207], v195 offset:18432\n"
  "ds_read_b64_tr_b16 a[212:213], v196 offset:16384\n"
  "v_cvt_pk_bf16_f32 v20, v248, v249\n"
  "v_cvt_pk_bf16_f32 v21, v250, v251\n"
  "v_cvt_pk_bf16_f32 v22, v252, v253\n"
  "v_cvt_pk_bf16_f32 v23, v254, v255\n"
  "v_mfma_f32_16x16x32_bf16 a[76:79], a[160:163], v[184:187], a[76:79]\n"
  "buffer_load_dwordx4 v201, s[16:19], s53 offen offset:3072 lds\n"
  "ds_read_b64_tr_b16 a[214:215], v196 offset:18432\n"
  "v_accvgpr_read_b32 v240, a40\n"
  "v_accvgpr_read_b32 v241, a41\n"
  "v_accvgpr_read_b32 v242, a42\n"
  "v_accvgpr_read_b32 v243, a43\n"
  "v_mfma_f32_16x16x32_bf16 a[76:79], a[164:167], v[188:191], a[76:79]\n"
  "s_add_u32 m0, s63, 147456\n"
  "s_add_u32 s54, s13, 360448\n"
  "ds_read_b64_tr_b16 a[220:221], v197 offset:16384\n"
  "ds_read_b64_tr_b16 a[222:223], v197 offset:18432\n"
  "v_accvgpr_read_b32 v244, a56\n"
  "v_accvgpr_read_b32 v245, a57\n"
  "v_accvgpr_read_b32 v246, a58\n"
  "v_accvgpr_read_b32 v247, a59\n"
  "s_waitcnt lgkmcnt(8)\n"
  "s_barrier\n"
  "v_mfma_f32_16x16x32_bf16 a[80:83], a[168:171], v[160:163], a[80:83]\n"
  "s_waitcnt vmcnt(8)\n"
  "buffer_load_dwordx4 v198, s[16:19], s54 offen offset:0 lds\n"
  "ds_read_b64_tr_b16 a[0:1], v194 offset:32768\n"
  "v_mul_f32 v240, v114, v240\n"
  "v_mul_f32 v241, v114, v241\n"
  "v_mul_f32 v242, v114, v242\n"
  "v_mul_f32 v243, v114, v243\n"
  "v_mfma_f32_16x16x32_bf16 a[80:83], a[172:175], v[164:167], a[80:83]\n"
  "ds_read_b64_tr_b16 a[2:3], v194 offset:34816\n"
  "ds_read_b64_tr_b16 a[8:9], v195 offset:32768\n"
  "v_mul_f32 v244, v114, v244\n"
  "v_mul_f32 v245, v114, v245\n"
  "v_mul_f32 v246, v114, v246\n"
  "v_mul_f32 v247, v114, v247\n"
  "v_mfma_f32_16x16x32_bf16 a[84:87], a[168:171], v[168:171], a[84:87]\n"
  "buffer_load_dwordx4 v199, s[16:19], s54 offen offset:1024 lds\n"
  "ds_read_b64_tr_b16 a[10:11], v195 offset:34816\n"
  "v_cvt_pk_bf16_f32 v24, v240, v241\n"
  "v_cvt_pk_bf16_f32 v25, v242, v243\n"
  "v_cvt_pk_bf16_f32 v26, v244, v245\n"
  "v_cvt_pk_bf16_f32 v27, v246, v247\n"
  "v_mfma_f32_16x16x32_bf16 a[84:87], a[172:175], v[172:175], a[84:87]\n"
  "ds_read_b64_tr_b16 a[16:17], v196 offset:32768\n"
  "ds_read_b64_tr_b16 a[18:19], v196 offset:34816\n"
  "v_accvgpr_read_b32 v248, a44\n"
  "v_accvgpr_read_b32 v249, a45\n"
  "v_accvgpr_read_b32 v250, a46\n"
  "v_accvgpr_read_b32 v251, a47\n"
  "v_mfma_f32_16x16x32_bf16 a[88:91], a[168:171], v[176:179], a[88:91]\n"
  "buffer_load_dwordx4 v200, s[16:19], s54 offen offset:2048 lds\n"
  "ds_read_b64_tr_b16 a[24:25], v197 offset:32768\n"
  "v_accvgpr_read_b32 v252, a60\n"
  "v_accvgpr_read_b32 v253, a61\n"
  "v_accvgpr_read_b32 v254, a62\n"
  "v_accvgpr_read_b32 v255, a63\n"
  "v_mfma_f32_16x16x32_bf16 a[88:91], a[172:175], v[180:183], a[88:91]\n"
  "s_waitcnt vmcnt(7)\n"
  "ds_read_b64_tr_b16 a[26:27], v197 offset:34816\n"
  "ds_read_b64_tr_b16 a[4:5], v194 offset:49152\n"
  "v_mul_f32 v248, v115, v248\n"
  "v_mul_f32 v249, v115, v249\n"
  "v_mul_f32 v250, v115, v250\n"
  "v_mul_f32 v251, v115, v251\n"
  "v_mfma_f32_16x16x32_bf16 a[92:95], a[168:171], v[184:187], a[92:95]\n"
  "buffer_load_dwordx4 v201, s[16:19], s54 offen offset:3072 lds\n"
  "ds_read_b64_tr_b16 a[6:7], v194 offset:51200\n"
  "v_mul_f32 v252, v115, v252\n"
  "v_mul_f32 v253, v115, v253\n"
  "v_mul_f32 v254, v115, v254\n"
  "v_mul_f32 v255, v115, v255\n"
  "v_mfma_f32_16x16x32_bf16 a[92:95], a[172:175], v[188:191], a[92:95]\n"
  "s_add_u32 m0, s63, 0\n"
  "s_add_u32 s53, s13, 393216\n"
  "ds_read_b64_tr_b16 a[12:13], v195 offset:49152\n"
  "ds_read_b64_tr_b16 a[14:15], v195 offset:51200\n"
  "v_cvt_pk_bf16_f32 v28, v248, v249\n"
  "v_cvt_pk_bf16_f32 v29, v250, v251\n"
  "v_cvt_pk_bf16_f32 v30, v252, v253\n"
  "v_cvt_pk_bf16_f32 v31, v254, v255\n"
  "v_mfma_f32_16x16x32_bf16 a[96:99], a[176:179], v[160:163], a[96:99]\n"
  "buffer_load_dwordx4 v198, s[16:19], s53 offen offset:0 lds\n"
  "ds_read_b64_tr_b16 a[20:21], v196 offset:49152\n"
  "v_accvgpr_read_b32 v240, a64\n"
  "v_accvgpr_read_b32 v241, a65\n"
  "v_accvgpr_read_b32 v242, a66\n"
  "v_accvgpr_read_b32 v243, a67\n"
  "v_mfma_f32_16x16x32_bf16 a[96:99], a[180:183], v[164:167], a[96:99]\n"
  "ds_read_b64_tr_b16 a[22:23], v196 offset:51200\n"
  "ds_read_b64_tr_b16 a[28:29], v197 offset:49152\n"
  "v_accvgpr_read_b32 v244, a80\n"
  "v_accvgpr_read_b32 v245, a81\n"
  "v_accvgpr_read_b32 v246, a82\n"
  "v_accvgpr_read_b32 v247, a83\n"
  "v_mfma_f32_16x16x32_bf16 a[100:103], a[176:179], v[168:171], a[100:103]\n"
  "buffer_load_dwordx4 v199, s[16:19], s53 offen offset:1024 lds\n"
  "ds_read_b64_tr_b16 a[30:31], v197 offset:51200\n"
  "v_mul_f32 v240, v112, v240\n"
  "v_mul_f32 v241, v112, v241\n"
  "v_mul_f32 v242, v112, v242\n"
  "v_mul_f32 v243, v112, v243\n"
  "v_mfma_f32_16x16x32_bf16 a[100:103], a[180:183], v[172:175], a[100:103]\n"
  "v_mul_f32 v244, v112, v244\n"
  "v_mul_f32 v245, v112, v245\n"
  "v_mul_f32 v246, v112, v246\n"
  "v_mul_f32 v247, v112, v247\n"
  "v_mfma_f32_16x16x32_bf16 a[104:107], a[176:179], v[176:179], a[104:107]\n"
  "ds_write_b128 v210, v[0:3] offset:0\n"
  "buffer_load_dwordx4 v200, s[16:19], s53 offen offset:2048 lds\n"
  "v_cvt_pk_bf16_f32 v32, v240, v241\n"
  "v_cvt_pk_bf16_f32 v33, v242, v243\n"
  "v_cvt_pk_bf16_f32 v34, v244, v245\n"
  "v_cvt_pk_bf16_f32 v35, v246, v247\n"
  "v_mfma_f32_16x16x32_bf16 a[104:107], a[180:183], v[180:183], a[104:107]\n"
  "v_accvgpr_read_b32 v248, a68\n"
  "v_accvgpr_read_b32 v249, a69\n"
  "v_accvgpr_read_b32 v250, a70\n"
  "v_accvgpr_read_b32 v251, a71\n"
  "v_mfma_f32_16x16x32_bf16 a[108:111], a[176:179], v[184:187], a[108:111]\n"
  "ds_write_b128 v210, v[4:7] offset:1024\n"
  "buffer_load_dwordx4 v201, s[16:19], s53 offen offset:3072 lds\n"
  "v_accvgpr_read_b32 v252, a84\n"
  "v_accvgpr_read_b32 v253, a85\n"
  "v_accvgpr_read_b32 v254, a86\n"
  "v_accvgpr_read_b32 v255, a87\n"
  "v_mfma_f32_16x16x32_bf16 a[108:111], a[180:183], v[188:191], a[108:111]\n"
  "s_add_u32 m0, s63, 16384\n"
  "s_add_u32 s54, s13, 425984\n"
  "v_mul_f32 v248, v113, v248\n"
  "v_mul_f32 v249, v113, v249\n"
  "v_mul_f32 v250, v113, v250\n"
  "v_mul_f32 v251, v113, v251\n"
  "v_mfma_f32_16x16x32_bf16 a[112:115], a[184:187], v[160:163], a[112:115]\n"
  "s_waitcnt lgkmcnt(6)\n"
  "ds_write_b128 v211, v[8:11] offset:0\n"
  "buffer_load_dwordx4 v198, s[16:19], s54 offen offset:0 lds\n"
  "v_mul_f32 v252, v113, v252\n"
  "v_mul_f32 v253, v113, v253\n"
  "v_mul_f32 v254, v113, v254\n"
  "v_mul_f32 v255, v113, v255\n"
  "v_mfma_f32_16x16x32_bf16 a[112:115], a[188:191], v[164:167], a[112:115]\n"
  "v_cvt_pk_bf16_f32 v36, v248, v249\n"
  "v_cvt_pk_bf16_f32 v37, v250, v251\n"
  "v_cvt_pk_bf16_f32 v38, v252, v253\n"
  "v_cvt_pk_bf16_f32 v39, v254, v255\n"
  "v_mfma_f32_16x16x32_bf16 a[116:119], a[184:187], v[168:171], a[116:119]\n"
  "ds_write_b128 v211, v[12:15] offset:1024\n"
  "buffer_load_dwordx4 v199, s[16:19], s54 offen offset:1024 lds\n"
  "v_accvgpr_read_b32 v240, a72\n"
  "v_accvgpr_read_b32 v241, a73\n"
  "v_accvgpr_read_b32 v242, a74\n"
  "v_accvgpr_read_b32 v243, a75\n"
  "v_mfma_f32_16x16x32_bf16 a[116:119], a[188:191], v[172:175], a[116:119]\n"
  "v_accvgpr_read_b32 v244, a88\n"
  "v_accvgpr_read_b32 v245, a89\n"
  "v_accvgpr_read_b32 v246, a90\n"
  "v_accvgpr_read_b32 v247, a91\n"
  "v_mfma_f32_16x16x32_bf16 a[120:123], a[184:187], v[176:179], a[120:123]\n"
  "ds_write_b128 v210, v[16:19] offset:4096\n"
  "buffer_load_dwordx4 v200, s[16:19], s54 offen offset:2048 lds\n"
  "v_mul_f32 v240, v114, v240\n"
  "v_mul_f32 v241, v114, v241\n"
  "v_mul_f32 v242, v114, v242\n"
  "v_mul_f32 v243, v114, v243\n"
  "v_mfma_f32_16x16x32_bf16 a[120:123], a[188:191], v[180:183], a[120:123]\n"
  "v_mul_f32 v244, v114, v244\n"
  "v_mul_f32 v245, v114, v245\n"
  "v_mul_f32 v246, v114, v246\n"
  "v_mul_f32 v247, v114, v247\n"
  "v_mfma_f32_16x16x32_bf16 a[124:127], a[184:187], v[184:187], a[124:127]\n"
  "ds_write_b128 v210, v[20:23] offset:5120\n"
  "buffer_load_dwordx4 v201, s[16:19], s54 offen offset:3072 lds\n"
  "v_cvt_pk_bf16_f32 v40, v240, v241\n"
  "v_cvt_pk_bf16_f32 v41, v242, v243\n"
  "v_cvt_pk_bf16_f32 v42, v244, v245\n"
  "v_cvt_pk_bf16_f32 v43, v246, v247\n"
  "v_mfma_f32_16x16x32_bf16 a[124:127], a[188:191], v[188:191], a[124:127]\n"
  "v_accvgpr_read_b32 v248, a76\n"
  "v_accvgpr_read_b32 v249, a77\n"
  "v_accvgpr_read_b32 v250, a78\n"
  "v_accvgpr_read_b32 v251, a79\n"
  "ds_write_b128 v211, v[24:27] offset:4096\n"
  "v_accvgpr_read_b32 v252, a92\n"
  "v_accvgpr_read_b32 v253, a93\n"
  "v_accvgpr_read_b32 v254, a94\n"
  "v_accvgpr_read_b32 v255, a95\n"
  "v_mul_f32 v248, v115, v248\n"
  "v_mul_f32 v249, v115, v249\n"
  "v_mul_f32 v250, v115, v250\n"
  "v_mul_f32 v251, v115, v251\n"
  "ds_write_b128 v211, v[28:31] offset:5120\n"
  "v_mul_f32 v252, v115, v252\n"
  "v_mul_f32 v253, v115, v253\n"
  "v_mul_f32 v254, v115, v254\n"
  "v_mul_f32 v255, v115, v255\n"
  "v_cvt_pk_bf16_f32 v44, v248, v249\n"
  "v_cvt_pk_bf16_f32 v45, v250, v251\n"
  "v_cvt_pk_bf16_f32 v46, v252, v253\n"
  "v_cvt_pk_bf16_f32 v47, v254, v255\n"
  "ds_write_b128 v210, v[32:35] offset:8192\n"
  "v_accvgpr_read_b32 v240, a96\n"
  "v_accvgpr_read_b32 v241, a97\n"
  "v_accvgpr_read_b32 v242, a98\n"
  "v_accvgpr_read_b32 v243, a99\n"
  "v_accvgpr_read_b32 v244, a112\n"
  "v_accvgpr_read_b32 v245, a113\n"
  "v_accvgpr_read_b32 v246, a114\n"
  "v_accvgpr_read_b32 v247, a115\n"
  "ds_write_b128 v210, v[36:39] offset:9216\n"
  "v_mul_f32 v240, v112, v240\n"
  "v_mul_f32 v241, v112, v241\n"
  "v_mul_f32 v242, v112, v242\n"
  "v_mul_f32 v243, v112, v243\n"
  "v_mul_f32 v244, v112, v244\n"
  "v_mul_f32 v245, v112, v245\n"
  "v_mul_f32 v246, v112, v246\n"
  "v_mul_f32 v247, v112, v247\n"
  "ds_write_b128 v211, v[40:43] offset:8192\n"
  "v_cvt_pk_bf16_f32 v48, v240, v241\n"
  "v_cvt_pk_bf16_f32 v49, v242, v243\n"
  "v_cvt_pk_bf16_f32 v50, v244, v245\n"
  "v_cvt_pk_bf16_f32 v51, v246, v247\n"
  "v_accvgpr_read_b32 v248, a100\n"
  "v_accvgpr_read_b32 v249, a101\n"
  "v_accvgpr_read_b32 v250, a102\n"
  "v_accvgpr_read_b32 v251, a103\n"
  "ds_write_b128 v211, v[44:47] offset:9216\n"
  "v_accvgpr_read_b32 v252, a116\n"
  "v_accvgpr_read_b32 v253, a117\n"
  "v_accvgpr_read_b32 v254, a118\n"
  "v_accvgpr_read_b32 v255, a119\n"
  "v_mul_f32 v248, v113, v248\n"
  "v_mul_f32 v249, v113, v249\n"
  "v_mul_f32 v250, v113, v250\n"
  "v_mul_f32 v251, v113, v251\n"
  "ds_write_b128 v210, v[48:51] offset:12288\n"
  "v_mul_f32 v252, v113, v252\n"
  "v_mul_f32 v253, v113, v253\n"
  "v_mul_f32 v254, v113, v254\n"
  "v_mul_f32 v255, v113, v255\n"
  "v_cvt_pk_bf16_f32 v52, v248, v249\n"
  "v_cvt_pk_bf16_f32 v53, v250, v251\n"
  "v_cvt_pk_bf16_f32 v54, v252, v253\n"
  "v_cvt_pk_bf16_f32 v55, v254, v255\n"
  "ds_write_b128 v210, v[52:55] offset:13312\n"
  "v_accvgpr_read_b32 v240, a104\n"
  "v_accvgpr_read_b32 v241, a105\n"
  "v_accvgpr_read_b32 v242, a106\n"
  "v_accvgpr_read_b32 v243, a107\n"
  "v_accvgpr_read_b32 v244, a120\n"
  "v_accvgpr_read_b32 v245, a121\n"
  "v_accvgpr_read_b32 v246, a122\n"
  "v_accvgpr_read_b32 v247, a123\n"
  "v_mul_f32 v240, v114, v240\n"
  "v_mul_f32 v241, v114, v241\n"
  "v_mul_f32 v242, v114, v242\n"
  "v_mul_f32 v243, v114, v243\n"
  "v_mul_f32 v244, v114, v244\n"
  "v_mul_f32 v245, v114, v245\n"
  "v_mul_f32 v246, v114, v246\n"
  "v_mul_f32 v247, v114, v247\n"
  "v_cvt_pk_bf16_f32 v56, v240, v241\n"
  "v_cvt_pk_bf16_f32 v57, v242, v243\n"
  "v_cvt_pk_bf16_f32 v58, v244, v245\n"
  "v_cvt_pk_bf16_f32 v59, v246, v247\n"
  "ds_write_b128 v211, v[56:59] offset:12288\n"
  "v_accvgpr_read_b32 v248, a108\n"
  "v_accvgpr_read_b32 v249, a109\n"
  "v_accvgpr_read_b32 v250, a110\n"
  "v_accvgpr_read_b32 v251, a111\n"
  "v_accvgpr_read_b32 v252, a124\n"
  "v_accvgpr_read_b32 v253, a125\n"
  "v_accvgpr_read_b32 v254, a126\n"
  "v_accvgpr_read_b32 v255, a127\n"
  "v_mul_f32 v248, v115, v248\n"
  "v_mul_f32 v249, v115, v249\n"
  "v_mul_f32 v250, v115, v250\n"
  "v_mul_f32 v251, v115, v251\n"
  "v_mul_f32 v252, v115, v252\n"
  "v_mul_f32 v253, v115, v253\n"
  "v_mul_f32 v254, v115, v254\n"
  "v_mul_f32 v255, v115, v255\n"
  "v_cvt_pk_bf16_f32 v60, v248, v249\n"
  "v_cvt_pk_bf16_f32 v61, v250, v251\n"
  "v_cvt_pk_bf16_f32 v62, v252, v253\n"
  "v_cvt_pk_bf16_f32 v63, v254, v255\n"
  "ds_write_b128 v211, v[60:63] offset:13312\n"
  "L_join:\n"
  "s_waitcnt lgkmcnt(0)\n"
  "s_barrier\n"
  "ds_read_b128 a[64:67], v202 offset:0\n"
  "ds_read_b128 a[68:71], v202 offset:4096\n"
  "ds_read_b128 a[72:75], v202 offset:1024\n"
  "ds_read_b128 a[76:79], v202 offset:5120\n"
  "ds_read_b128 a[80:83], v203 offset:0\n"
  "ds_read_b128 a[84:87], v203 offset:4096\n"
  "ds_read_b128 a[88:91], v203 offset:1024\n"
  "ds_read_b128 a[92:95], v203 offset:5120\n"
  "s_waitcnt lgkmcnt(7)\n"
  "v_mfma_f32_16x16x32_bf16 v[0:3], a[128:131], a[64:67], 0\n"
  "s_waitcnt lgkmcnt(6)\n"
  "s_waitcnt vmcnt(12)\n"
  "s_add_u32 m0, s63, 32768\n"
  "ds_read_b64_tr_b16 a[32:33], v228 offset:0\n"
  "v_mfma_f32_16x16x32_bf16 v[0:3], a[132:135], a[68:71], v[0:3]\n"
  "s_add_u32 s53, s13, 458752\n"
  "ds_read_b64_tr_b16 a[34:35], v228 offset:2048\n"
  "v_mfma_f32_16x16x32_bf16 v[16:19], a[136:139], a[64:67], 0\n"
  "buffer_load_dwordx4 v198, s[16:19], s53 offen offset:0 lds\n"
  "v_mfma_f32_16x16x32_bf16 v[16:19], a[140:143], a[68:71], v[16:19]\n"
  "ds_read_b64_tr_b16 a[40:41], v229 offset:0\n"
  "v_mfma_f32_16x16x32_bf16 v[32:35], a[144:147], a[64:67], 0\n"
  "ds_read_b64_tr_b16 a[42:43], v229 offset:2048\n"
  "v_mfma_f32_16x16x32_bf16 v[32:35], a[148:151], a[68:71], v[32:35]\n"
  "ds_read_b64_tr_b16 a[48:49], v230 offset:0\n"
  "v_mfma_f32_16x16x32_bf16 v[48:51], a[152:155], a[64:67], 0\n"
  "buffer_load_dwordx4 v199, s[16:19], s53 offen offset:1024 lds\n"
  "v_mfma_f32_16x16x32_bf16 v[48:51], a[156:159], a[68:71], v[48:51]\n"
  "s_waitcnt lgkmcnt(10)\n"
  "ds_read_b64_tr_b16 a[50:51], v230 offset:2048\n"
  "v_mfma_f32_16x16x32_bf16 v[4:7], a[128:131], a[72:75], 0\n"
  "s_waitcnt lgkmcnt(6)\n"
  "ds_read_b128 a[64:67], v202 offset:8192\n"
  "v_mfma_f32_16x16x32_bf16 v[4:7], a[132:135], a[76:79], v[4:7]\n"
  "ds_read_b64_tr_b16 a[56:57], v231 offset:0\n"
  "v_mfma_f32_16x16x32_bf16 v[20:23], a[136:139], a[72:75], 0\n"
  "buffer_load_dwordx4 v200, s[16:19], s53 offen offset:2048 lds\n"
  "v_mfma_f32_16x16x32_bf16 v[20:23], a[140:143], a[76:79], v[20:23]\n"
  "ds_read_b128 a[68:71], v202 offset:12288\n"
  "v_mfma_f32_16x16x32_bf16 v[36:39], a[144:147], a[72:75], 0\n"
  "ds_read_b64_tr_b16 a[58:59], v231 offset:2048\n"
  "v_mfma_f32_16x16x32_bf16 v[36:39], a[148:151], a[76:79], v[36:39]\n"
  "s_waitcnt vmcnt(11)\n"
  "ds_read_b64_tr_b16 a[36:37], v228 offset:16384\n"
  "v_mfma_f32_16x16x32_bf16 v[52:55], a[152:155], a[72:75], 0\n"
  "buffer_load_dwordx4 v201, s[16:19], s53 offen offset:3072 lds\n"
  "v_mfma_f32_16x16x32_bf16 v[52:55], a[156:159], a[76:79], v[52:55]\n"
  "ds_read_b64_tr_b16 a[38:39], v228 offset:18432\n"
  "v_mfma_f32_16x16x32_bf16 v[8:11], a[128:131], a[80:83], 0\n"
  "s_add_u32 m0, s63, 49152\n"
  "ds_read_b128 a[72:75], v202 offset:9216\n"
  "v_mfma_f32_16x16x32_bf16 v[8:11], a[132:135], a[84:87], v[8:11]\n"
  "s_add_u32 s54, s13, 491520\n"
  "ds_read_b64_tr_b16 a[44:45], v229 offset:16384\n"
  "v_mfma_f32_16x16x32_bf16 v[24:27], a[136:139], a[80:83], 0\n"
  "L_dry2_3:\n"
  "L_pad11:\n"
  "s_cbranch_execz L_dryr2\n"
  "buffer_load_dwordx4 v198, s[16:19], s54 offen offset:0 lds\n"
  "v_mfma_f32_16x16x32_bf16 v[24:27], a[140:143], a[84:87], v[24:27]\n"
  "ds_read_b128 a[76:79], v202 offset:13312\n"
  "v_mfma_f32_16x16x32_bf16 v[40:43], a[144:147], a[80:83], 0\n"
  "ds_read_b64_tr_b16 a[46:47], v229 offset:18432\n"
  "v_mfma_f32_16x16x32_bf16 v[40:43], a[148:151], a[84:87], v[40:43]\n"
  "ds_read_b64_tr_b16 a[52:53], v230 offset:16384\n"
  "v_mfma_f32_16x16x32_bf16 v[56:59], a[152:155], a[80:83], 0\n"
  "buffer_load_dwordx4 v199, s[16:19], s54 offen offset:1024 lds\n"
  "v_mfma_f32_16x16x32_bf16 v[56:59], a[156:159], a[84:87], v[56:59]\n"
  "ds_read_b64_tr_b16 a[54:55], v230 offset:18432\n"
  "v_mfma_f32_16x16x32_bf16 v[12:15], a[128:131], a[88:91], 0\n"
  "ds_read_b128 a[80:83], v203 offset:8192\n"
  "v_mfma_f32_16x16x32_bf16 v[12:15], a[132:135], a[92:95], v[12:15]\n"
  "ds_read_b64_tr_b16 a[60:61], v231 offset:16384\n"
  "v_mfma_f32_16x16x32_bf16 v[28:31], a[136:139], a[88:91], 0\n"
  "buffer_load_dwordx4 v200, s[16:19], s54 offen offset:2048 lds\n"
  "v_mfma_f32_16x16x32_bf16 v[28:31], a[140:143], a[92:95], v[28:31]\n"
  "ds_read_b128 a[84:87], v203 offset:12288\n"
  "v_mfma_f32_16x16x32_bf16 v[44:47], a[144:147], a[88:91], 0\n"
  "ds_read_b64_tr_b16 a[62:63], v231 offset:18432\n"
  "v_mfma_f32_16x16x32_bf16 v[44:47], a[148:151], a[92:95], v[44:47]\n"
  "v_mfma_f32_16x16x32_bf16 v[60:63], a[152:155], a[88:91], 0\n"
  "buffer_load_dwordx4 v201, s[16:19], s54 offen offset:3072 lds\n"
  "v_mfma_f32_16x16x32_bf16 v[60:63], a[156:159], a[92:95], v[60:63]\n"
  "s_waitcnt lgkmcnt(5)\n"
  "v_mfma_f32_16x16x32_bf16 v[0:3], v[64:67], a[64:67], v[0:3]\n"
  "s_add_u32 m0, s63, 131072\n"
  "ds_read_b128 a[88:91], v203 offset:9216\n"
  "v_mfma_f32_16x16x32_bf16 v[0:3], v[68:71], a[68:71], v[0:3]\n"
  "s_waitcnt vmcnt(12)\n"
  "s_add_u32 s53, s55, 458752\n"
  "ds_read_b64_tr_b16 a[160:161], v194 offset:0\n"
  "v_mfma_f32_16x16x32_bf16 v[16:19], v[72:75], a[64:67], v[16:19]\n"
  "buffer_load_dwordx4 v198, s[16:19], s53 offen offset:0 lds\n"
  "v_mfma_f32_16x16x32_bf16 v[16:19], v[76:79], a[68:71], v[16:19]\n"
  "ds_read_b128 a[92:95], v203 offset:13312\n"
  "v_mfma_f32_16x16x32_bf16 v[32:35], v[96:99], a[64:67], v[32:35]\n"
  "ds_read_b64_tr_b16 a[162:163], v194 offset:2048\n"
  "v_mfma_f32_16x16x32_bf16 v[32:35], v[100:103], a[68:71], v[32:35]\n"
  "ds_read_b64_tr_b16 a[168:169], v195 offset:0\n"
  "v_mfma_f32_16x16x32_bf16 v[48:51], v[104:107], a[64:67], v[48:51]\n"
  "buffer_load_dwordx4 v199, s[16:19], s53 offen offset:1024 lds\n"
  "v_mfma_f32_16x16x32_bf16 v[48:51], v[108:111], a[68:71], v[48:51]\n"
  "ds_read_b64_tr_b16 a[170:171], v195 offset:2048\n"
  "v_mfma_f32_16x16x32_bf16 v[4:7], v[64:67], a[72:75], v[4:7]\n"
  "ds_read_b128 a[64:67], v202 offset:16384\n"
  "v_mfma_f32_16x16x32_bf16 v[4:7], v[68:71], a[76:79], v[4:7]\n"
  "ds_read_b64_tr_b16 a[176:177], v196 offset:0\n"
  "v_mfma_f32_16x16x32_bf16 v[20:23], v[72:75], a[72:75], v[20:23]\n"
  "buffer_load_dwordx4 v200, s[16:19], s53 offen offset:2048 lds\n"
  "v_mfma_f32_16x16x32_bf16 v[20:23], v[76:79], a[76:79], v[20:23]\n"
  "ds_read_b128 a[68:71], v202 offset:20480\n"
  "v_mfma_f32_16x16x32_bf16 v[36:39], v[96:99], a[72:75], v[36:39]\n"
  "ds_read_b64_tr_b16 a[178:179], v196 offset:2048\n"
  "v_mfma_f32_16x16x32_bf16 v[36:39], v[100:103], a[76:79], v[36:39]\n"
  "ds_read_b64_tr_b16 a[184:185], v197 offset:0\n"
  "v_mfma_f32_16x16x32_bf16 v[52:55], v[104:107], a[72:75], v[52:55]\n"
  "buffer_load_dwordx4 v201, s[16:19], s53 offen offset:3072 lds\n"
  "v_mfma_f32_16x16x32_bf16 v[52:55], v[108:111], a[76:79], v[52:55]\n"
  "s_waitcnt lgkmcnt(6)\n"
  "ds_read_b64_tr_b16 a[186:187], v197 offset:2048\n"
  "v_mfma_f32_16x16x32_bf16 v[8:11], v[64:67], a[80:83], v[8:11]\n"
  "s_add_u32 m0, s63, 147456\n"
  "ds_read_b128 a[72:75], v202 offset:17408\n"
  "v_mfma_f32_16x16x32_bf16 v[8:11], v[68:71], a[84:87], v[8:11]\n"
  "s_waitcnt vmcnt(12)\n"
  "s_add_u32 s54, s55, 491520\n"
  "ds_read_b64_tr_b16 a[164:165], v194 offset:16384\n"
  "v_mfma_f32_16x16x32_bf16 v[24:27], v[72:75], a[80:83], v[24:27]\n"
  "buffer_load_dwordx4 v198, s[16:19], s54 offen offset:0 lds\n"
  "v_mfma_f32_16x16x32_bf16 v[24:27], v[76:79], a[84:87], v[24:27]\n"
  "ds_read_b128 a[76:79], v202 offset:21504\n"
  "v_mfma_f32_16x16x32_bf16 v[40:43], v[96:99], a[80:83], v[40:43]\n"
  "ds_read_b64_tr_b16 a[166:167], v194 offset:18432\n"
  "v_mfma_f32_16x16x32_bf16 v[40:43], v[100:103], a[84:87], v[40:43]\n"
  "ds_read_b64_tr_b16 a[172:173], v195 offset:16384\n"
  "v_mfma_f32_16x16x32_bf16 v[56:59], v[104:107], a[80:83], v[56:59]\n"
  "buffer_load_dwordx4 v199, s[16:19], s54 offen offset:1024 lds\n"
  "v_mfma_f32_16x16x32_bf16 v[56:59], v[108:111], a[84:87], v[56:59]\n"
  "ds_read_b64_tr_b16 a[174:175], v195 offset:18432\n"
  "v_mfma_f32_16x16x32_bf16 v[12:15], v[64:67], a[88:91], v[12:15]\n"
  "ds_read_b128 a[80:83], v203 offset:16384\n"
  "v_mfma_f32_16x16x32_bf16 v[12:15], v[68:71], a[92:95], v[12:15]\n"
  "ds_read_b64_tr_b16 a[180:181], v196 offset:16384\n"
  "v_mfma_f32_16x16x32_bf16 v[28:31], v[72:75], a[88:91], v[28:31]\n"
  "buffer_load_dwordx4 v200, s[16:19], s54 offen offset:2048 lds\n"
  "v_mfma_f32_16x16x32_bf16 v[28:31], v[76:79], a[92:95], v[28:31]\n"
  "ds_read_b128 a[84:87], v203 offset:20480\n"
  "v_mfma_f32_16x16x32_bf16 v[44:47], v[96:99], a[88:91], v[44:47]\n"
  "ds_read_b64_tr_b16 a[182:183], v196 offset:18432\n"
  "v_mfma_f32_16x16x32_bf16 v[44:47], v[100:103], a[92:95], v[44:47]\n"
  "ds_read_b64_tr_b16 a[188:189], v197 offset:16384\n"
  "v_mfma_f32_16x16x32_bf16 v[60:63], v[104:107], a[88:91], v[60:63]\n"
  "buffer_load_dwordx4 v201, s[16:19], s54 offen offset:3072 lds\n"
  "v_mfma_f32_16x16x32_bf16 v[60:63], v[108:111], a[92:95], v[60:63]\n"
  "s_waitcnt lgkmcnt(6)\n"
  "ds_read_b64_tr_b16 a[190:191], v197 offset:18432\n"
  "v_mfma_f32_16x16x32_bf16 v[0:3], a[224:227], a[64:67], v[0:3]\n"
  "s_add_u32 m0, s63, 0\n"
  "ds_read_b128 a[88:91], v203 offset:17408\n"
  "v_mfma_f32_16x16x32_bf16 v[0:3], a[228:231], a[68:71], v[0:3]\n"
  "s_waitcnt vmcnt(12)\n"
  "s_add_u32 s53, s55, 393216\n"
  "ds_read_b64_tr_b16 a[96:97], v194 offset:32768\n"
  "v_mfma_f32_16x16x32_bf16 v[16:19], a[232:235], a[64:67], v[16:19]\n"
  "buffer_load_dwordx4 v198, s[16:19], s53 offen offset:0 lds\n"
  "v_mfma_f32_16x16x32_bf16 v[16:19], a[236:239], a[68:71], v[16:19]\n"
  "ds_read_b128 a[92:95], v203 offset:21504\n"
  "v_mfma_f32_16x16x32_bf16 v[32:35], a[240:243], a[64:67], v[32:35]\n"
  "ds_read_b64_tr_b16 a[98:99], v194 offset:34816\n"
  "v_mfma_f32_16x16x32_bf16 v[32:35], a[244:247], a[68:71], v[32:35]\n"
  "ds_read_b64_tr_b16 a[104:105], v195 offset:32768\n"
  "v_mfma_f32_16x16x32_bf16 v[48:51], a[248:251], a[64:67], v[48:51]\n"
  "buffer_load_dwordx4 v199, s[16:19], s53 offen offset:1024 lds\n"
  "v_mfma_f32_16x16x32_bf16 v[48:51], a[252:255], a[68:71], v[48:51]\n"
  "ds_read_b64_tr_b16 a[106:107], v195 offset:34816\n"
  "v_mfma_f32_16x16x32_bf16 v[4:7], a[224:227], a[72:75], v[4:7]\n"
  "ds_read_b128 a[64:67], v202 offset:24576\n"
  "v_mfma_f32_16x16x32_bf16 v[4:7], a[228:231], a[76:79], v[4:7]\n"
  "ds_read_b64_tr_b16 a[112:113], v196 offset:32768\n"
  "v_mfma_f32_16x16x32_bf16 v[20:23], a[232:235], a[72:75], v[20:23]\n"
  "buffer_load_dwordx4 v200, s[16:19], s53 offen offset:2048 lds\n"
  "v_mfma_f32_16x16x32_bf16 v[20:23], a[236:239], a[76:79], v[20:23]\n"
  "ds_read_b128 a[68:71], v202 offset:28672\n"
  "v_mfma_f32_16x16x32_bf16 v[36:39], a[240:243], a[72:75], v[36:39]\n"
  "ds_read_b64_tr_b16 a[114:115], v196 offset:34816\n"
  "v_mfma_f32_16x16x32_bf16 v[36:39], a[244:247], a[76:79], v[36:39]\n"
  "ds_read_b64_tr_b16 a[120:121], v197 offset:32768\n"
  "v_mfma_f32_16x16x32_bf16 v[52:55], a[248:251], a[72:75], v[52:55]\n"
  "buffer_load_dwordx4 v201, s[16:19], s53 offen offset:3072 lds\n"
  "v_mfma_f32_16x16x32_bf16 v[52:55], a[252:255], a[76:79], v[52:55]\n"
  "s_waitcnt lgkmcnt(6)\n"
  "ds_read_b64_tr_b16 a[122:123], v197 offset:34816\n"
  "v_mfma_f32_16x16x32_bf16 v[8:11], a[224:227], a[80:83], v[8:11]\n"
  "s_add_u32 m0, s63, 16384\n"
  "ds_read_b128 a[72:75], v202 offset:25600\n"
  "v_mfma_f32_16x16x32_bf16 v[8:11], a[228:231], a[84:87], v[8:11]\n"
  "s_waitcnt vmcnt(12)\n"
  "s_add_u32 s54, s55, 425984\n"
  "ds_read_b64_tr_b16 a[100:101], v194 offset:49152\n"
  "v_mfma_f32_16x16x32_bf16 v[24:27], a[232:235], a[80:83], v[24:27]\n"
  "buffer_load_dwordx4 v198, s[16:19], s54 offen offset:0 lds\n"
  "v_mfma_f32_16x16x32_bf16 v[24:27], a[236:239], a[84:87], v[24:27]\n"
  "ds_read_b128 a[76:79], v202 offset:29696\n"
  "v_mfma_f32_16x16x32_bf16 v[40:43], a[240:243], a[80:83], v[40:43]\n"
  "ds_read_b64_tr_b16 a[102:103], v194 offset:51200\n"
  "v_mfma_f32_16x16x32_bf16 v[40:43], a[244:247], a[84:87], v[40:43]\n"
  "ds_read_b64_tr_b16 a[108:109], v195 offset:49152\n"
  "v_mfma_f32_16x16x32_bf16 v[56:59], a[248:251], a[80:83], v[56:59]\n"
  "buffer_load_dwordx4 v199, s[16:19], s54 offen offset:1024 lds\n"
  "v_mfma_f32_16x16x32_bf16 v[56:59], a[252:255], a[84:87], v[56:59]\n"
  "ds_read_b64_tr_b16 a[110:111], v195 offset:51200\n"
  "v_mfma_f32_16x16x32_bf16 v[12:15], a[224:227], a[88:91], v[12:15]\n"
  "ds_read_b128 a[80:83], v203 offset:24576\n"
  "v_mfma_f32_16x16x32_bf16 v[12:15], a[228:231], a[92:95], v[12:15]\n"
  "ds_read_b64_tr_b16 a[116:117], v196 offset:49152\n"
  "v_mfma_f32_16x16x32_bf16 v[28:31], a[232:235], a[88:91], v[28:31]\n"
  "buffer_load_dwordx4 v200, s[16:19], s54 offen offset:2048 lds\n"
  "v_mfma_f32_16x16x32_bf16 v[28:31], a[236:239], a[92:95], v[28:31]\n"
  "ds_read_b128 a[84:87], v203 offset:28672\n"
  "v_mfma_f32_16x16x32_bf16 v[44:47], a[240:243], a[88:91], v[44:47]\n"
  "ds_read_b64_tr_b16 a[118:119], v196 offset:51200\n"
  "v_mfma_f32_16x16x32_bf16 v[44:47], a[244:247], a[92:95], v[44:47]\n"
  "ds_read_b64_tr_b16 a[124:125], v197 offset:49152\n"
  "v_mfma_f32_16x16x32_bf16 v[60:63], a[248:251], a[88:91], v[60:63]\n"
  "buffer_load_dwordx4 v201, s[16:19], s54 offen offset:3072 lds\n"
  "v_mfma_f32_16x16x32_bf16 v[60:63], a[252:255], a[92:95], v[60:63]\n"
  "s_waitcnt lgkmcnt(6)\n"
  "ds_read_b64_tr_b16 a[126:127], v197 offset:51200\n"
  "v_mfma_f32_16x16x32_bf16 v[0:3], a[192:195], a[64:67], v[0:3]\n"
  "s_add_u32 m0, s63, 32768\n"
  "ds_read_b128 a[88:91], v203 offset:25600\n"
  "v_mfma_f32_16x16x32_bf16 v[0:3], a[196:199], a[68:71], v[0:3]\n"
  "s_waitcnt vmcnt(12)\n"
  "s_add_u32 s53, s55, 327680\n"
  "ds_read_b64_tr_b16 a[128:129], v228 offset:0\n"
  "v_mfma_f32_16x16x32_bf16 v[16:19], a[200:203], a[64:67], v[16:19]\n"
  "buffer_load_dwordx4 v198, s[16:19], s53 offen offset:0 lds\n"
  "v_mfma_f32_16x16x32_bf16 v[16:19], a[204:207], a[68:71], v[16:19]\n"
  "ds_read_b128 a[92:95], v203 offset:29696\n"
  "v_mfma_f32_16x16x32_bf16 v[32:35], a[208:211], a[64:67], v[32:35]\n"
  "ds_read_b64_tr_b16 a[130:131], v228 offset:2048\n"
  "v_mfma_f32_16x16x32_bf16 v[32:35], a[212:215], a[68:71], v[32:35]\n"
  "ds_read_b64_tr_b16 a[136:137], v229 offset:0\n"
  "v_mfma_f32_16x16x32_bf16 v[48:51], a[216:219], a[64:67], v[48:51]\n"
  "buffer_load_dwordx4 v199, s[16:19], s53 offen offset:1024 lds\n"
  "v_mfma_f32_16x16x32_bf16 v[48:51], a[220:223], a[68:71], v[48:51]\n"
  "ds_read_b64_tr_b16 a[138:139], v229 offset:2048\n"
  "v_mfma_f32_16x16x32_bf16 v[4:7], a[192:195], a[72:75], v[4:7]\n"
  "ds_read_b128 a[64:67], v202 offset:32768\n"
  "v_mfma_f32_16x16x32_bf16 v[4:7], a[196:199], a[76:79], v[4:7]\n"
  "ds_read_b64_tr_b16 a[144:145], v230 offset:0\n"
  "v_mfma_f32_16x16x32_bf16 v[20:23], a[200:203], a[72:75], v[20:23]\n"
  "buffer_load_dwordx4 v200, s[16:19], s53 offen offset:2048 lds\n"
  "v_mfma_f32_16x16x32_bf16 v[20:23], a[204:207], a[76:79], v[20:23]\n"
  "ds_read_b128 a[68:71], v202 offset:36864\n"
  "v_mfma_f32_16x16x32_bf16 v[36:39], a[208:211], a[72:75], v[36:39]\n"
  "ds_read_b64_tr_b16 a[146:147], v230 offset:2048\n"
  "v_mfma_f32_16x16x32_bf16 v[36:39], a[212:215], a[76:79], v[36:39]\n"
  "ds_read_b64_tr_b16 a[152:153], v231 offset:0\n"
  "v_mfma_f32_16x16x32_bf16 v[52:55], a[216:219], a[72:75], v[52:55]\n"
  "buffer_load_dwordx4 v201, s[16:19], s53 offen offset:3072 lds\n"
  "v_mfma_f32_16x16x32_bf16 v[52:55], a[220:223], a[76:79], v[52:55]\n"
  "s_waitcnt lgkmcnt(6)\n"
  "ds_read_b64_tr_b16 a[154:155], v231 offset:2048\n"
  "v_mfma_f32_16x16x32_bf16 v[8:11], a[192:195], a[80:83], v[8:11]\n"
  "s_add_u32 m0, s63, 49152\n"
  "ds_read_b128 a[72:75], v202 offset:33792\n"
  "v_mfma_f32_16x16x32_bf16 v[8:11], a[196:199], a[84:87], v[8:11]\n"
  "s_waitcnt vmcnt(12)\n"
  "s_add_u32 s54, s55, 360448\n"
  "ds_read_b64_tr_b16 a[132:133], v228 offset:16384\n"
  "v_mfma_f32_16x16x32_bf16 v[24:27], a[200:203], a[80:83], v[24:27]\n"
  "buffer_load_dwordx4 v198, s[16:19], s54 offen offset:0 lds\n"
  "v_mfma_f32_16x16x32_bf16 v[24:27], a[204:207], a[84:87], v[24:27]\n"
  "ds_read_b128 a[76:79], v202 offset:37888\n"
  "v_mfma_f32_16x16x32_bf16 v[40:43], a[208:211], a[80:83], v[40:43]\n"
  "ds_read_b64_tr_b16 a[134:135], v228 offset:18432\n"
  "v_mfma_f32_16x16x32_bf16 v[40:43], a[212:215], a[84:87], v[40:43]\n"
  "ds_read_b64_tr_b16 a[140:141], v229 offset:16384\n"
  "v_mfma_f32_16x16x32_bf16 v[56:59], a[216:219], a[80:83], v[56:59]\n"
  "buffer_load_dwordx4 v199, s[16:19], s54 offen offset:1024 lds\n"
  "v_mfma_f32_16x16x32_bf16 v[56:59], a[220:223], a[84:87], v[56:59]\n"
  "ds_read_b64_tr_b16 a[142:143], v229 offset:18432\n"
  "v_mfma_f32_16x16x32_bf16 v[12:15], a[192:195], a[88:91], v[12:15]\n"
  "ds_read_b128 a[80:83], v203 offset:32768\n"
  "v_mfma_f32_16x16x32_bf16 v[12:15], a[196:199], a[92:95], v[12:15]\n"
  "ds_read_b64_tr_b16 a[148:149], v230 offset:16384\n"
  "v_mfma_f32_16x16x32_bf16 v[28:31], a[200:203], a[88:91], v[28:31]\n"
  "buffer_load_dwordx4 v200, s[16:19], s54 offen offset:2048 lds\n"
  "v_mfma_f32_16x16x32_bf16 v[28:31], a[204:207], a[92:95], v[28:31]\n"
  "ds_read_b128 a[84:87], v203 offset:36864\n"
  "v_mfma_f32_16x16x32_bf16 v[44:47], a[208:211], a[88:91], v[44:47]\n"
  "ds_read_b64_tr_b16 a[150:151], v230 offset:18432\n"
  "v_mfma_f32_16x16x32_bf16 v[44:47], a[212:215], a[92:95], v[44:47]\n"
  "ds_read_b64_tr_b16 a[156:157], v231 offset:16384\n"
  "v_mfma_f32_16x16x32_bf16 v[60:63], a[216:219], a[88:91], v[60:63]\n"
  "buffer_load_dwordx4 v201, s[16:19], s54 offen offset:3072 lds\n"
  "v_mfma_f32_16x16x32_bf16 v[60:63], a[220:223], a[92:95], v[60:63]\n"
  "s_waitcnt lgkmcnt(6)\n"
  "ds_read_b64_tr_b16 a[158:159], v231 offset:18432\n"
  "v_mfma_f32_16x16x32_bf16 v[0:3], a[0:3], a[64:67], v[0:3]\n"
  "s_add_u32 m0, s63, 131072\n"
  "ds_read_b128 a[88:91], v203 offset:33792\n"
  "v_mfma_f32_16x16x32_bf16 v[0:3], a[4:7], a[68:71], v[0:3]\n"
  "s_waitcnt vmcnt(12)\n"
  "s_add_u32 s53, s55, 262144\n"
  "ds_read_b64_tr_b16 a[224:225], v194 offset:0\n"
  "v_mfma_f32_16x16x32_bf16 v[16:19], a[8:11], a[64:67], v[16:19]\n"
  "buffer_load_dwordx4 v198, s[16:19], s53 offen offset:0 lds\n"
  "v_mfma_f32_16x16x32_bf16 v[16:19], a[12:15], a[68:71], v[16:19]\n"
  "ds_read_b128 a[92:95], v203 offset:37888\n"
  "v_mfma_f32_16x16x32_bf16 v[32:35], a[16:19], a[64:67], v[32:35]\n"
  "ds_read_b64_tr_b16 a[226:227], v194 offset:2048\n"
  "v_mfma_f32_16x16x32_bf16 v[32:35], a[20:23], a[68:71], v[32:35]\n"
  "ds_read_b64_tr_b16 a[232:233], v195 offset:0\n"
  "v_mfma_f32_16x16x32_bf16 v[48:51], a[24:27], a[64:67], v[48:51]\n"
  "buffer_load_dwordx4 v199, s[16:19], s53 offen offset:1024 lds\n"
  "v_mfma_f32_16x16x32_bf16 v[48:51], a[28:31], a[68:71], v[48:51]\n"
  "ds_read_b64_tr_b16 a[234:235], v195 offset:2048\n"
  "v_mfma_f32_16x16x32_bf16 v[4:7], a[0:3], a[72:75], v[4:7]\n"
  "ds_read_b128 a[64:67], v202 offset:40960\n"
  "v_mfma_f32_16x16x32_bf16 v[4:7], a[4:7], a[76:79], v[4:7]\n"
  "ds_read_b64_tr_b16 a[240:241], v196 offset:0\n"
  "v_mfma_f32_16x16x32_bf16 v[20:23], a[8:11], a[72:75], v[20:23]\n"
  "buffer_load_dwordx4 v200, s[16:19], s53 offen offset:2048 lds\n"
  "v_mfma_f32_16x16x32_bf16 v[20:23], a[12:15], a[76:79], v[20:23]\n"
  "ds_read_b128 a[68:71], v202 offset:45056\n"
  "v_mfma_f32_16x16x32_bf16 v[36:39], a[16:19], a[72:75], v[36:39]\n"
  "ds_read_b64_tr_b16 a[242:243], v196 offset:2048\n"
  "v_mfma_f32_16x16x32_bf16 v[36:39], a[20:23], a[76:79], v[36:39]\n"
  "ds_read_b64_tr_b16 a[248:249], v197 offset:0\n"
  "v_mfma_f32_16x16x32_bf16 v[52:55], a[24:27], a[72:75], v[52:55]\n"
  "buffer_load_dwordx4 v201, s[16:19], s53 offen offset:3072 lds\n"
  "v_mfma_f32_16x16x32_bf16 v[52:55], a[28:31], a[76:79], v[52:55]\n"
  "s_waitcnt lgkmcnt(6)\n"
  "ds_read_b64_tr_b16 a[250:251], v197 offset:2048\n"
  "v_mfma_f32_16x16x32_bf16 v[8:11], a[0:3], a[80:83], v[8:11]\n"
  "s_add_u32 m0, s63, 147456\n"
  "ds_read_b128 a[72:75], v202 offset:41984\n"
  "v_mfma_f32_16x16x32_bf16 v[8:11], a[4:7], a[84:87], v[8:11]\n"
  "s_waitcnt vmcnt(12)\n"
  "s_add_u32 s54, s55, 294912\n"
  "ds_read_b64_tr_b16 a[228:229], v194 offset:16384\n"
  "v_mfma_f32_16x16x32_bf16 v[24:27], a[8:11], a[80:83], v[24:27]\n"
  "buffer_load_dwordx4 v198, s[16:19], s54 offen offset:0 lds\n"
  "v_mfma_f32_16x16x32_bf16 v[24:27], a[12:15], a[84:87], v[24:27]\n"
  "ds_read_b128 a[76:79], v202 offset:46080\n"
  "v_mfma_f32_16x16x32_bf16 v[40:43], a[16:19], a[80:83], v[40:43]\n"
  "ds_read_b64_tr_b16 a[230:231], v194 offset:18432\n"
  "v_mfma_f32_16x16x32_bf16 v[40:43], a[20:23], a[84:87], v[40:43]\n"
  "ds_read_b64_tr_b16 a[236:237], v195 offset:16384\n"
  "v_mfma_f32_16x16x32_bf16 v[56:59], a[24:27], a[80:83], v[56:59]\n"
  "buffer_load_dwordx4 v199, s[16:19], s54 offen offset:1024 lds\n"
  "v_mfma_f32_16x16x32_bf16 v[56:59], a[28:31], a[84:87], v[56:59]\n"
  "ds_read_b64_tr_b16 a[238:239], v195 offset:18432\n"
  "v_mfma_f32_16x16x32_bf16 v[12:15], a[0:3], a[88:91], v[12:15]\n"
  "ds_read_b128 a[80:83], v203 offset:40960\n"
  "v_mfma_f32_16x16x32_bf16 v[12:15], a[4:7], a[92:95], v[12:15]\n"
  "ds_read_b64_tr_b16 a[244:245], v196 offset:16384\n"
  "v_mfma_f32_16x16x32_bf16 v[28:31], a[8:11], a[88:91], v[28:31]\n"
  "buffer_load_dwordx4 v200, s[16:19], s54 offen offset:2048 lds\n"
  "v_mfma_f32_16x16x32_bf16 v[28:31], a[12:15], a[92:95], v[28:31]\n"
  "ds_read_b128 a[84:87], v203 offset:45056\n"
  "v_mfma_f32_16x16x32_bf16 v[44:47], a[16:19], a[88:91], v[44:47]\n"
  "ds_read_b64_tr_b16 a[246:247], v196 offset:18432\n"
  "v_mfma_f32_16x16x32_bf16 v[44:47], a[20:23], a[92:95], v[44:47]\n"
  "ds_read_b64_tr_b16 a[252:253], v197 offset:16384\n"
  "v_mfma_f32_16x16x32_bf16 v[60:63], a[24:27], a[88:91], v[60:63]\n"
  "buffer_load_dwordx4 v201, s[16:19], s54 offen offset:3072 lds\n"
  "v_mfma_f32_16x16x32_bf16 v[60:63], a[28:31], a[92:95], v[60:63]\n"
  "s_waitcnt lgkmcnt(6)\n"
  "ds_read_b64_tr_b16 a[254:255], v197 offset:18432\n"
  "v_mfma_f32_16x16x32_bf16 v[0:3], a[32:35], a[64:67], v[0:3]\n"
  "s_add_u32 m0, s63, 0\n"
  "ds_read_b128 a[88:91], v203 offset:41984\n"
  "v_mfma_f32_16x16x32_bf16 v[0:3], a[36:39], a[68:71], v[0:3]\n"
  "s_waitcnt vmcnt(12)\n"
  "s_add_u32 s53, s55, 196608\n"
  "ds_read_b64_tr_b16 a[192:193], v194 offset:32768\n"
  "v_mfma_f32_16x16x32_bf16 v[16:19], a[40:43], a[64:67], v[16:19]\n"
  "buffer_load_dwordx4 v198, s[16:19], s53 offen offset:0 lds\n"
  "v_mfma_f32_16x16x32_bf16 v[16:19], a[44:47], a[68:71], v[16:19]\n"
  "ds_read_b128 a[92:95], v203 offset:46080\n"
  "v_mfma_f32_16x16x32_bf16 v[32:35], a[48:51], a[64:67], v[32:35]\n"
  "ds_read_b64_tr_b16 a[194:195], v194 offset:34816\n"
  "v_mfma_f32_16x16x32_bf16 v[32:35], a[52:55], a[68:71], v[32:35]\n"
  "ds_read_b64_tr_b16 a[200:201], v195 offset:32768\n"
  "v_mfma_f32_16x16x32_bf16 v[48:51], a[56:59], a[64:67], v[48:51]\n"
  "buffer_load_dwordx4 v199, s[16:19], s53 offen offset:1024 lds\n"
  "v_mfma_f32_16x16x32_bf16 v[48:51], a[60:63], a[68:71], v[48:51]\n"
  "ds_read_b64_tr_b16 a[202:203], v195 offset:34816\n"
  "v_mfma_f32_16x16x32_bf16 v[4:7], a[32:35], a[72:75], v[4:7]\n"
  "ds_read_b128 a[64:67], v202 offset:49152\n"
  "v_mfma_f32_16x16x32_bf16 v[4:7], a[36:39], a[76:79], v[4:7]\n"
  "ds_read_b64_tr_b16 a[208:209], v196 offset:32768\n"
  "v_mfma_f32_16x16x32_bf16 v[20:23], a[40:43], a[72:75], v[20:23]\n"
  "buffer_load_dwordx4 v200, s[16:19], s53 offen offset:2048 lds\n"
  "v_mfma_f32_16x16x32_bf16 v[20:23], a[44:47], a[76:79], v[20:23]\n"
  "ds_read_b128 a[68:71], v202 offset:53248\n"
  "v_mfma_f32_16x16x32_bf16 v[36:39], a[48:51], a[72:75], v[36:39]\n"
  "ds_read_b64_tr_b16 a[210:211], v196 offset:34816\n"
  "v_mfma_f32_16x16x32_bf16 v[36:39], a[52:55], a[76:79], v[36:39]\n"
  "ds_read_b64_tr_b16 a[216:217], v197 offset:32768\n"
  "v_mfma_f32_16x16x32_bf16 v[52:55], a[56:59], a[72:75], v[52:55]\n"
  "buffer_load_dwordx4 v201, s[16:19], s53 offen offset:3072 lds\n"
  "v_mfma_f32_16x16x32_bf16 v[52:55], a[60:63], a[76:79], v[52:55]\n"
  "s_waitcnt lgkmcnt(6)\n"
  "ds_read_b64_tr_b16 a[218:219], v197 offset:34816\n"
  "v_mfma_f32_16x16x32_bf16 v[8:11], a[32:35], a[80:83], v[8:11]\n"
  "s_add_u32 m0, s63, 16384\n"
  "ds_read_b128 a[72:75], v202 offset:50176\n"
  "v_mfma_f32_16x16x32_bf16 v[8:11], a[36:39], a[84:87], v[8:11]\n"
  "s_waitcnt vmcnt(12)\n"
  "s_add_u32 s54, s55, 229376\n"
  "ds_read_b64_tr_b16 a[196:197], v194 offset:49152\n"
  "v_mfma_f32_16x16x32_bf16 v[24:27], a[40:43], a[80:83], v[24:27]\n"
  "buffer_load_dwordx4 v198, s[16:19], s54 offen offset:0 lds\n"
  "v_mfma_f32_16x16x32_bf16 v[24:27], a[44:47], a[84:87], v[24:27]\n"
  "ds_read_b128 a[76:79], v202 offset:54272\n"
  "v_mfma_f32_16x16x32_bf16 v[40:43], a[48:51], a[80:83], v[40:43]\n"
  "ds_read_b64_tr_b16 a[198:199], v194 offset:51200\n"
  "v_mfma_f32_16x16x32_bf16 v[40:43], a[52:55], a[84:87], v[40:43]\n"
  "ds_read_b64_tr_b16 a[204:205], v195 offset:49152\n"
  "v_mfma_f32_16x16x32_bf16 v[56:59], a[56:59], a[80:83], v[56:59]\n"
  "buffer_load_dwordx4 v199, s[16:19], s54 offen offset:1024 lds\n"
  "v_mfma_f32_16x16x32_bf16 v[56:59], a[60:63], a[84:87], v[56:59]\n"
  "ds_read_b64_tr_b16 a[206:207], v195 offset:51200\n"
  "v_mfma_f32_16x16x32_bf16 v[12:15], a[32:35], a[88:91], v[12:15]\n"
  "ds_read_b128 a[80:83], v203 offset:49152\n"
  "v_mfma_f32_16x16x32_bf16 v[12:15], a[36:39], a[92:95], v[12:15]\n"
  "ds_read_b64_tr_b16 a[212:213], v196 offset:49152\n"
  "v_mfma_f32_16x16x32_bf16 v[28:31], a[40:43], a[88:91], v[28:31]\n"
  "buffer_load_dwordx4 v200, s[16:19], s54 offen offset:2048 lds\n"
  "v_mfma_f32_16x16x32_bf16 v[28:31], a[44:47], a[92:95], v[28:31]\n"
  "ds_read_b128 a[84:87], v203 offset:53248\n"
  "v_mfma_f32_16x16x32_bf16 v[44:47], a[48:51], a[88:91], v[44:47]\n"
  "ds_read_b64_tr_b16 a[214:215], v196 offset:51200\n"
  "v_mfma_f32_16x16x32_bf16 v[44:47], a[52:55], a[92:95], v[44:47]\n"
  "ds_read_b64_tr_b16 a[220:221], v197 offset:49152\n"
  "v_mfma_f32_16x16x32_bf16 v[60:63], a[56:59], a[88:91], v[60:63]\n"
  "buffer_load_dwordx4 v201, s[16:19], s54 offen offset:3072 lds\n"
  "v_mfma_f32_16x16x32_bf16 v[60:63], a[60:63], a[92:95], v[60:63]\n"
  "s_waitcnt lgkmcnt(6)\n"
  "ds_read_b64_tr_b16 a[222:223], v197 offset:51200\n"
  "v_mfma_f32_16x16x32_bf16 v[0:3], a[160:163], a[64:67], v[0:3]\n"
  "s_add_u32 m0, s63, 32768\n"
  "ds_read_b128 a[88:91], v203 offset:50176\n"
  "v_mfma_f32_16x16x32_bf16 v[0:3], a[164:167], a[68:71], v[0:3]\n"
  "s_waitcnt vmcnt(12)\n"
  "s_add_u32 s53, s55, 131072\n"
  "ds_read_b64_tr_b16 a[0:1], v228 offset:0\n"
  "v_mfma_f32_16x16x32_bf16 v[16:19], a[168:171], a[64:67], v[16:19]\n"
  "buffer_load_dwordx4 v198, s[16:19], s53 offen offset:0 lds\n"
  "v_mfma_f32_16x16x32_bf16 v[16:19], a[172:175], a[68:71], v[16:19]\n"
  "ds_read_b128 a[92:95], v203 offset:54272\n"
  "v_mfma_f32_16x16x32_bf16 v[32:35], a[176:179], a[64:67], v[32:35]\n"
  "ds_read_b64_tr_b16 a[2:3], v228 offset:2048\n"
  "v_mfma_f32_16x16x32_bf16 v[32:35], a[180:183], a[68:71], v[32:35]\n"
  "ds_read_b64_tr_b16 a[8:9], v229 offset:0\n"
  "v_mfma_f32_16x16x32_bf16 v[48:51], a[184:187], a[64:67], v[48:51]\n"
  "buffer_load_dwordx4 v199, s[16:19], s53 offen offset:1024 lds\n"
  "v_mfma_f32_16x16x32_bf16 v[48:51], a[188:191], a[68:71], v[48:51]\n"
  "ds_read_b64_tr_b16 a[10:11], v229 offset:2048\n"
  "v_mfma_f32_16x16x32_bf16 v[4:7], a[160:163], a[72:75], v[4:7]\n"
  "ds_read_b128 a[64:67], v202 offset:57344\n"
  "v_mfma_f32_16x16x32_bf16 v[4:7], a[164:167], a[76:79], v[4:7]\n"
  "ds_read_b64_tr_b16 a[16:17], v230 offset:0\n"
  "v_mfma_f32_16x16x32_bf16 v[20:23], a[168:171], a[72:75], v[20:23]\n"
  "buffer_load_dwordx4 v200, s[16:19], s53 offen offset:2048 lds\n"
  "v_mfma_f32_16x16x32_bf16 v[20:23], a[172:175], a[76:79], v[20:23]\n"
  "ds_read_b128 a[68:71], v202 offset:61440\n"
  "v_mfma_f32_16x16x32_bf16 v[36:39], a[176:179], a[72:75], v[36:39]\n"
  "ds_read_b64_tr_b16 a[18:19], v230 offset:2048\n"
  "v_mfma_f32_16x16x32_bf16 v[36:39], a[180:183], a[76:79], v[36:39]\n"
  "ds_read_b64_tr_b16 a[24:25], v231 offset:0\n"
  "v_mfma_f32_16x16x32_bf16 v[52:55], a[184:187], a[72:75], v[52:55]\n"
  "buffer_load_dwordx4 v201, s[16:19], s53 offen offset:3072 lds\n"
  "v_mfma_f32_16x16x32_bf16 v[52:55], a[188:191], a[76:79], v[52:55]\n"
  "s_waitcnt lgkmcnt(6)\n"
  "ds_read_b64_tr_b16 a[26:27], v231 offset:2048\n"
  "v_mfma_f32_16x16x32_bf16 v[8:11], a[160:163], a[80:83], v[8:11]\n"
  "s_add_u32 m0, s63, 49152\n"
  "ds_read_b128 a[72:75], v202 offset:58368\n"
  "v_mfma_f32_16x16x32_bf16 v[8:11], a[164:167], a[84:87], v[8:11]\n"
  "s_waitcnt vmcnt(12)\n"
  "s_add_u32 s54, s55, 163840\n"
  "ds_read_b64_tr_b16 a[4:5], v228 offset:16384\n"
  "v_mfma_f32_16x16x32_bf16 v[24:27], a[168:171], a[80:83], v[24:27]\n"
  "buffer_load_dwordx4 v198, s[16:19], s54 offen offset:0 lds\n"
  "v_mfma_f32_16x16x32_bf16 v[24:27], a[172:175], a[84:87], v[24:27]\n"
  "ds_read_b128 a[76:79], v202 offset:62464\n"
  "v_mfma_f32_16x16x32_bf16 v[40:43], a[176:179], a[80:83], v[40:43]\n"
  "ds_read_b64_tr_b16 a[6:7], v228 offset:18432\n"
  "v_mfma_f32_16x16x32_bf16 v[40:43], a[180:183], a[84:87], v[40:43]\n"
  "ds_read_b64_tr_b16 a[12:13], v229 offset:16384\n"
  "v_mfma_f32_16x16x32_bf16 v[56:59], a[184:187], a[80:83], v[56:59]\n"
  "buffer_load_dwordx4 v199, s[16:19], s54 offen offset:1024 lds\n"
  "v_mfma_f32_16x16x32_bf16 v[56:59], a[188:191], a[84:87], v[56:59]\n"
  "ds_read_b64_tr_b16 a[14:15], v229 offset:18432\n"
  "v_mfma_f32_16x16x32_bf16 v[12:15], a[160:163], a[88:91], v[12:15]\n"
  "ds_read_b128 a[80:83], v203 offset:57344\n"
  "v_mfma_f32_16x16x32_bf16 v[12:15], a[164:167], a[92:95], v[12:15]\n"
  "ds_read_b64_tr_b16 a[20:21], v230 offset:16384\n"
  "v_mfma_f32_16x16x32_bf16 v[28:31], a[168:171], a[88:91], v[28:31]\n"
  "buffer_load_dwordx4 v200, s[16:19], s54 offen offset:2048 lds\n"
  "v_mfma_f32_16x16x32_bf16 v[28:31], a[172:175], a[92:95], v[28:31]\n"
  "ds_read_b128 a[84:87], v203 offset:61440\n"
  "v_mfma_f32_16x16x32_bf16 v[44:47], a[176:179], a[88:91], v[44:47]\n"
  "ds_read_b64_tr_b16 a[22:23], v230 offset:18432\n"
  "v_mfma_f32_16x16x32_bf16 v[44:47], a[180:183], a[92:95], v[44:47]\n"
  "ds_read_b64_tr_b16 a[28:29], v231 offset:16384\n"
  "v_mfma_f32_16x16x32_bf16 v[60:63], a[184:187], a[88:91], v[60:63]\n"
  "buffer_load_dwordx4 v201, s[16:19], s54 offen offset:3072 lds\n"
  "v_mfma_f32_16x16x32_bf16 v[60:63], a[188:191], a[92:95], v[60:63]\n"
  "s_waitcnt lgkmcnt(6)\n"
  "ds_read_b64_tr_b16 a[30:31], v231 offset:18432\n"
  "v_mfma_f32_16x16x32_bf16 v[0:3], a[96:99], a[64:67], v[0:3]\n"
  "s_add_u32 m0, s63, 131072\n"
  "ds_read_b128 a[88:91], v203 offset:58368\n"
  "v_mfma_f32_16x16x32_bf16 v[0:3], a[100:103], a[68:71], v[0:3]\n"
  "s_waitcnt vmcnt(12)\n"
  "s_add_u32 s53, s55, 65536\n"
  "ds_read_b64_tr_b16 a[32:33], v194 offset:0\n"
  "v_mfma_f32_16x16x32_bf16 v[16:19], a[104:107], a[64:67], v[16:19]\n"
  "buffer_load_dwordx4 v198, s[16:19], s53 offen offset:0 lds\n"
  "v_mfma_f32_16x16x32_bf16 v[16:19], a[108:111], a[68:71], v[16:19]\n"
  "ds_read_b128 a[92:95], v203 offset:62464\n"
  "v_mfma_f32_16x16x32_bf16 v[32:35], a[112:115], a[64:67], v[32:35]\n"
  "ds_read_b64_tr_b16 a[34:35], v194 offset:2048\n"
  "v_mfma_f32_16x16x32_bf16 v[32:35], a[116:119], a[68:71], v[32:35]\n"
  "ds_read_b64_tr_b16 a[40:41], v195 offset:0\n"
  "v_mfma_f32_16x16x32_bf16 v[48:51], a[120:123], a[64:67], v[48:51]\n"
  "buffer_load_dwordx4 v199, s[16:19], s53 offen offset:1024 lds\n"
  "v_mfma_f32_16x16x32_bf16 v[48:51], a[124:127], a[68:71], v[48:51]\n"
  "ds_read_b64_tr_b16 a[42:43], v195 offset:2048\n"
  "v_mfma_f32_16x16x32_bf16 v[4:7], a[96:99], a[72:75], v[4:7]\n"
  "ds_read_b64_tr_b16 a[48:49], v196 offset:0\n"
  "v_cvt_pk_bf16_f32 v0, v0, v1\n"
  "v_mfma_f32_16x16x32_bf16 v[4:7], a[100:103], a[76:79], v[4:7]\n"
  "ds_read_b64_tr_b16 a[50:51], v196 offset:2048\n"
  "v_cvt_pk_bf16_f32 v1, v2, v3\n"
  "v_mfma_f32_16x16x32_bf16 v[20:23], a[104:107], a[72:75], v[20:23]\n"
  "buffer_load_dwordx4 v200, s[16:19], s53 offen offset:2048 lds\n"
  "v_mfma_f32_16x16x32_bf16 v[20:23], a[108:111], a[76:79], v[20:23]\n"
  "ds_read_b64_tr_b16 a[56:57], v197 offset:0\n"
  "v_cvt_pk_bf16_f32 v2, v16, v17\n"
  "v_mfma_f32_16x16x32_bf16 v[36:39], a[112:115], a[72:75], v[36:39]\n"
  "ds_read_b64_tr_b16 a[58:59], v197 offset:2048\n"
  "v_cvt_pk_bf16_f32 v3, v18, v19\n"
  "v_mfma_f32_16x16x32_bf16 v[36:39], a[116:119], a[76:79], v[36:39]\n"
  "s_waitcnt vmcnt(11)\n"
  "ds_read_b64_tr_b16 a[36:37], v194 offset:16384\n"
  "v_cvt_pk_bf16_f32 v32, v32, v33\n"
  "v_mfma_f32_16x16x32_bf16 v[52:55], a[120:123], a[72:75], v[52:55]\n"
  "buffer_load_dwordx4 v201, s[16:19], s53 offen offset:3072 lds\n"
  "v_mfma_f32_16x16x32_bf16 v[52:55], a[124:127], a[76:79], v[52:55]\n"
  "s_waitcnt lgkmcnt(6)\n"
  "ds_read_b64_tr_b16 a[38:39], v194 offset:18432\n"
  "v_cvt_pk_bf16_f32 v33, v34, v35\n"
  "v_mfma_f32_16x16x32_bf16 v[8:11], a[96:99], a[80:83], v[8:11]\n"
  "s_add_u32 m0, s63, 147456\n"
  "ds_read_b64_tr_b16 a[44:45], v195 offset:16384\n"
  "v_mfma_f32_16x16x32_bf16 v[8:11], a[100:103], a[84:87], v[8:11]\n"
  "s_add_u32 s54, s55, 98304\n"
  "ds_read_b64_tr_b16 a[46:47], v195 offset:18432\n"
  "v_mfma_f32_16x16x32_bf16 v[24:27], a[104:107], a[80:83], v[24:27]\n"
  "buffer_load_dwordx4 v198, s[16:19], s54 offen offset:0 lds\n"
  "v_mfma_f32_16x16x32_bf16 v[24:27], a[108:111], a[84:87], v[24:27]\n"
  "ds_read_b64_tr_b16 a[52:53], v196 offset:16384\n"
  "v_cvt_pk_bf16_f32 v34, v48, v49\n"
  "v_mfma_f32_16x16x32_bf16 v[40:43], a[112:115], a[80:83], v[40:43]\n"
  "ds_read_b64_tr_b16 a[54:55], v196 offset:18432\n"
  "v_cvt_pk_bf16_f32 v35, v50, v51\n"
  "v_mfma_f32_16x16x32_bf16 v[40:43], a[116:119], a[84:87], v[40:43]\n"
  "ds_read_b64_tr_b16 a[60:61], v197 offset:16384\n"
  "v_cvt_pk_bf16_f32 v4, v4, v5\n"
  "v_mfma_f32_16x16x32_bf16 v[56:59], a[120:123], a[80:83], v[56:59]\n"
  "buffer_load_dwordx4 v199, s[16:19], s54 offen offset:1024 lds\n"
  "v_mfma_f32_16x16x32_bf16 v[56:59], a[124:127], a[84:87], v[56:59]\n"
  "ds_read_b64_tr_b16 a[62:63], v197 offset:18432\n"
  "v_cvt_pk_bf16_f32 v5, v6, v7\n"
  "v_mfma_f32_16x16x32_bf16 v[12:15], a[96:99], a[88:91], v[12:15]\n"
  "v_cvt_pk_bf16_f32 v6, v20, v21\n"
  "v_cvt_pk_bf16_f32 v7, v22, v23\n"
  "v_mfma_f32_16x16x32_bf16 v[12:15], a[100:103], a[92:95], v[12:15]\n"
  "L_dry3_0:\n"
  "L_pad12:\n"
  "s_cbranch_execz L_dryr3\n"
  "v_cvt_pk_bf16_f32 v36, v36, v37\n"
  "v_cvt_pk_bf16_f32 v37, v38, v39\n"
  "v_mfma_f32_16x16x32_bf16 v[28:31], a[104:107], a[88:91], v[28:31]\n"
  "buffer_load_dwordx4 v200, s[16:19], s54 offen offset:2048 lds\n"
  "v_mfma_f32_16x16x32_bf16 v[28:31], a[108:111], a[92:95], v[28:31]\n"
  "v_cvt_pk_bf16_f32 v38, v52, v53\n"
  "v_cvt_pk_bf16_f32 v39, v54, v55\n"
  "v_mfma_f32_16x16x32_bf16 v[44:47], a[112:115], a[88:91], v[44:47]\n"
  "v_cvt_pk_bf16_f32 v24, v24, v25\n"
  "v_cvt_pk_bf16_f32 v25, v26, v27\n"
  "v_mfma_f32_16x16x32_bf16 v[44:47], a[116:119], a[92:95], v[44:47]\n"
  "v_cvt_pk_bf16_f32 v26, v8, v9\n"
  "v_cvt_pk_bf16_f32 v27, v10, v11\n"
  "v_mfma_f32_16x16x32_bf16 v[60:63], a[120:123], a[88:91], v[60:63]\n"
  "buffer_load_dwordx4 v201, s[16:19], s54 offen offset:3072 lds\n"
  "v_mfma_f32_16x16x32_bf16 v[60:63], a[124:127], a[92:95], v[60:63]\n"
  "v_cvt_pk_bf16_f32 v56, v56, v57\n"
  "v_cvt_pk_bf16_f32 v57, v58, v59\n"
  "v_mfma_f32_16x16x32_bf16 v[64:67], a[128:131], a[64:67], 0\n"
  "s_waitcnt vmcnt(12)\n"
  "s_add_u32 m0, s63, 0\n"
  "ds_read_b64_tr_b16 a[160:161], v194 offset:32768\n"
  "v_mfma_f32_16x16x32_bf16 v[64:67], a[132:135], a[68:71], v[64:67]\n"
  "s_add_u32 s53, s55, 0\n"
  "ds_read_b64_tr_b16 a[162:163], v194 offset:34816\n"
  "v_mfma_f32_16x16x32_bf16 v[80:83], a[136:139], a[64:67], 0\n"
  "s_waitcnt lgkmcnt(2)\n"
  "buffer_load_dwordx4 v198, s[16:19], s53 offen offset:0 lds\n"
  "v_mfma_f32_16x16x32_bf16 v[80:83], a[140:143], a[68:71], v[80:83]\n"
  "ds_read_b64_tr_b16 a[168:169], v195 offset:32768\n"
  "v_cvt_pk_bf16_f32 v58, v40, v41\n"
  "v_mfma_f32_16x16x32_bf16 v[96:99], a[144:147], a[64:67], 0\n"
  "ds_read_b64_tr_b16 a[170:171], v195 offset:34816\n"
  "v_cvt_pk_bf16_f32 v59, v42, v43\n"
  "v_mfma_f32_16x16x32_bf16 v[96:99], a[148:151], a[68:71], v[96:99]\n"
  "ds_read_b64_tr_b16 a[176:177], v196 offset:32768\n"
  "v_cvt_pk_bf16_f32 v28, v28, v29\n"
  "v_mfma_f32_16x16x32_bf16 v[112:115], a[152:155], a[64:67], 0\n"
  "buffer_load_dwordx4 v199, s[16:19], s53 offen offset:1024 lds\n"
  "v_mfma_f32_16x16x32_bf16 v[112:115], a[156:159], a[68:71], v[112:115]\n"
  "ds_read_b64_tr_b16 a[178:179], v196 offset:34816\n"
  "v_cvt_pk_bf16_f32 v29, v30, v31\n"
  "v_mfma_f32_16x16x32_bf16 v[68:71], a[128:131], a[72:75], 0\n"
  "ds_read_b128 a[64:67], v202 offset:49152\n"
  "v_cvt_pk_bf16_f32 v30, v12, v13\n"
  "v_mfma_f32_16x16x32_bf16 v[68:71], a[132:135], a[76:79], v[68:71]\n"
  "ds_read_b64_tr_b16 a[184:185], v197 offset:32768\n"
  "v_cvt_pk_bf16_f32 v31, v14, v15\n"
  "v_mfma_f32_16x16x32_bf16 v[84:87], a[136:139], a[72:75], 0\n"
  "buffer_load_dwordx4 v200, s[16:19], s53 offen offset:2048 lds\n"
  "v_mfma_f32_16x16x32_bf16 v[84:87], a[140:143], a[76:79], v[84:87]\n"
  "ds_read_b128 a[68:71], v202 offset:53248\n"
  "v_cvt_pk_bf16_f32 v60, v60, v61\n"
  "v_mfma_f32_16x16x32_bf16 v[100:103], a[144:147], a[72:75], 0\n"
  "ds_read_b64_tr_b16 a[186:187], v197 offset:34816\n"
  "v_cvt_pk_bf16_f32 v61, v62, v63\n"
  "v_mfma_f32_16x16x32_bf16 v[100:103], a[148:151], a[76:79], v[100:103]\n"
  "s_waitcnt vmcnt(11)\n"
  "ds_read_b64_tr_b16 a[164:165], v194 offset:49152\n"
  "v_cvt_pk_bf16_f32 v62, v44, v45\n"
  "v_mfma_f32_16x16x32_bf16 v[116:119], a[152:155], a[72:75], 0\n"
  "buffer_load_dwordx4 v201, s[16:19], s53 offen offset:3072 lds\n"
  "v_mfma_f32_16x16x32_bf16 v[116:119], a[156:159], a[76:79], v[116:119]\n"
  "ds_read_b64_tr_b16 a[166:167], v194 offset:51200\n"
  "v_cvt_pk_bf16_f32 v63, v46, v47\n"
  "v_mfma_f32_16x16x32_bf16 v[72:75], a[128:131], a[80:83], 0\n"
  "s_add_u32 m0, s63, 16384\n"
  "ds_read_b128 a[72:75], v202 offset:50176\n"
  "v_mfma_f32_16x16x32_bf16 v[72:75], a[132:135], a[84:87], v[72:75]\n"
  "s_add_u32 s54, s55, 32768\n"
  "ds_read_b64_tr_b16 a[172:173], v195 offset:49152\n"
  "v_mfma_f32_16x16x32_bf16 v[88:91], a[136:139], a[80:83], 0\n"
  "buffer_load_dwordx4 v198, s[16:19], s54 offen offset:0 lds\n"
  "v_mfma_f32_16x16x32_bf16 v[88:91], a[140:143], a[84:87], v[88:91]\n"
  "ds_read_b128 a[76:79], v202 offset:54272\n"
  "v_mfma_f32_16x16x32_bf16 v[104:107], a[144:147], a[80:83], 0\n"
  "ds_read_b64_tr_b16 a[174:175], v195 offset:51200\n"
  "v_mfma_f32_16x16x32_bf16 v[104:107], a[148:151], a[84:87], v[104:107]\n"
  "ds_read_b64_tr_b16 a[180:181], v196 offset:49152\n"
  "v_mfma_f32_16x16x32_bf16 v[120:123], a[152:155], a[80:83], 0\n"
  "buffer_load_dwordx4 v199, s[16:19], s54 offen offset:1024 lds\n"
  "v_mfma_f32_16x16x32_bf16 v[120:123], a[156:159], a[84:87], v[120:123]\n"
  "ds_read_b64_tr_b16 a[182:183], v196 offset:51200\n"
  "v_mfma_f32_16x16x32_bf16 v[76:79], a[128:131], a[88:91], 0\n"
  "ds_read_b128 a[80:83], v203 offset:49152\n"
  "v_mfma_f32_16x16x32_bf16 v[76:79], a[132:135], a[92:95], v[76:79]\n"
  "ds_read_b64_tr_b16 a[188:189], v197 offset:49152\n"
  "v_mfma_f32_16x16x32_bf16 v[92:95], a[136:139], a[88:91], 0\n"
  "buffer_load_dwordx4 v200, s[16:19], s54 offen offset:2048 lds\n"
  "v_mfma_f32_16x16x32_bf16 v[92:95], a[140:143], a[92:95], v[92:95]\n"
  "ds_read_b128 a[84:87], v203 offset:53248\n"
  "v_mfma_f32_16x16x32_bf16 v[108:111], a[144:147], a[88:91], 0\n"
  "ds_read_b64_tr_b16 a[190:191], v197 offset:51200\n"
  "v_mfma_f32_16x16x32_bf16 v[108:111], a[148:151], a[92:95], v[108:111]\n"
  "v_mfma_f32_16x16x32_bf16 v[124:127], a[152:155], a[88:91], 0\n"
  "buffer_load_dwordx4 v201, s[16:19], s54 offen offset:3072 lds\n"
  "v_mfma_f32_16x16x32_bf16 v[124:127], a[156:159], a[92:95], v[124:127]\n"
  "s_waitcnt lgkmcnt(5)\n"
  "global_load_dwordx4 v[248:251], v206, s[36:37]\n"
  "global_load_dwordx4 v[252:255], v207, s[36:37]\n"
  "v_mfma_f32_16x16x32_bf16 v[64:67], a[224:227], a[64:67], v[64:67]\n"
  "s_xor_b32 s57, s12, 7168\n"
  "ds_read_b128 a[88:91], v203 offset:50176\n"
  "buffer_store_dwordx4 v[0:3], v192, s[32:35], s57 offen sc1\n"
  "v_mfma_f32_16x16x32_bf16 v[64:67], a[228:231], a[68:71], v[64:67]\n"
  "s_waitcnt vmcnt(15)\n"
  "ds_read_b64_tr_b16 a[96:97], v228 offset:0\n"
  "v_mul_f32 v160, 0xc13504f3, v128\n"
  "v_mfma_f32_16x16x32_bf16 v[80:83], a[232:235], a[64:67], v[80:83]\n"
  "ds_read_b64_tr_b16 a[98:99], v228 offset:2048\n"
  "v_mul_f32 v161, 0xc13504f3, v129\n"
  "v_mfma_f32_16x16x32_bf16 v[80:83], a[236:239], a[68:71], v[80:83]\n"
  "ds_read_b128 a[92:95], v203 offset:54272\n"
  "v_mul_f32 v162, 0xc13504f3, v130\n"
  "v_mfma_f32_16x16x32_bf16 v[96:99], a[240:243], a[64:67], v[96:99]\n"
  "ds_read_b64_tr_b16 a[104:105], v229 offset:0\n"
  "v_mul_f32 v163, 0xc13504f3, v131\n"
  "v_mfma_f32_16x16x32_bf16 v[96:99], a[244:247], a[68:71], v[96:99]\n"
  "ds_read_b64_tr_b16 a[106:107], v229 offset:2048\n"
  "v_mul_f32 v164, 0xc13504f3, v132\n"
  "v_mfma_f32_16x16x32_bf16 v[112:115], a[248:251], a[64:67], v[112:115]\n"
  "ds_read_b64_tr_b16 a[112:113], v230 offset:0\n"
  "v_mul_f32 v165, 0xc13504f3, v133\n"
  "v_mfma_f32_16x16x32_bf16 v[112:115], a[252:255], a[68:71], v[112:115]\n"
  "ds_read_b64_tr_b16 a[114:115], v230 offset:2048\n"
  "v_mul_f32 v166, 0xc13504f3, v134\n"
  "v_mfma_f32_16x16x32_bf16 v[68:71], a[224:227], a[72:75], v[68:71]\n"
  "ds_read_b128 a[64:67], v202 offset:40960\n"
  "v_mul_f32 v167, 0xc13504f3, v135\n"
  "v_mfma_f32_16x16x32_bf16 v[68:71], a[228:231], a[76:79], v[68:71]\n"
  "ds_read_b64_tr_b16 a[120:121], v231 offset:0\n"
  "v_mul_f32 v168, 0xc13504f3, v136\n"
  "v_mfma_f32_16x16x32_bf16 v[84:87], a[232:235], a[72:75], v[84:87]\n"
  "ds_read_b64_tr_b16 a[122:123], v231 offset:2048\n"
  "v_mul_f32 v169, 0xc13504f3, v137\n"
  "v_mfma_f32_16x16x32_bf16 v[84:87], a[236:239], a[76:79], v[84:87]\n"
  "ds_read_b128 a[68:71], v202 offset:45056\n"
  "v_mul_f32 v170, 0xc13504f3, v138\n"
  "v_mfma_f32_16x16x32_bf16 v[100:103], a[240:243], a[72:75], v[100:103]\n"
  "v_mul_f32 v171, 0xc13504f3, v139\n"
  "v_mul_f32 v172, 0xc13504f3, v140\n"
  "v_mfma_f32_16x16x32_bf16 v[100:103], a[244:247], a[76:79], v[100:103]\n"
  "s_waitcnt vmcnt(11)\n"
  "ds_read_b64_tr_b16 a[100:101], v228 offset:16384\n"
  "v_mul_f32 v173, 0xc13504f3, v141\n"
  "v_mfma_f32_16x16x32_bf16 v[116:119], a[248:251], a[72:75], v[116:119]\n"
  "s_xor_b32 s57, s12, 136192\n"
  "ds_read_b64_tr_b16 a[102:103], v228 offset:18432\n"
  "buffer_store_dwordx4 v[32:35], v192, s[32:35], s57 offen sc1\n"
  "v_mfma_f32_16x16x32_bf16 v[116:119], a[252:255], a[76:79], v[116:119]\n"
  "s_waitcnt lgkmcnt(7)\n"
  "ds_read_b64_tr_b16 a[108:109], v229 offset:16384\n"
  "v_mul_f32 v174, 0xc13504f3, v142\n"
  "v_mfma_f32_16x16x32_bf16 v[72:75], a[224:227], a[80:83], v[72:75]\n"
  "ds_read_b128 a[72:75], v202 offset:41984\n"
  "v_mul_f32 v175, 0xc13504f3, v143\n"
  "v_mfma_f32_16x16x32_bf16 v[72:75], a[228:231], a[84:87], v[72:75]\n"
  "ds_read_b64_tr_b16 a[110:111], v229 offset:18432\n"
  "v_mfma_f32_16x16x32_bf16 v[88:91], a[232:235], a[80:83], v[88:91]\n"
  "ds_read_b64_tr_b16 a[116:117], v230 offset:16384\n"
  "v_mfma_f32_16x16x32_bf16 v[88:91], a[236:239], a[84:87], v[88:91]\n"
  "ds_read_b128 a[76:79], v202 offset:46080\n"
  "v_mfma_f32_16x16x32_bf16 v[104:107], a[240:243], a[80:83], v[104:107]\n"
  "ds_read_b64_tr_b16 a[118:119], v230 offset:18432\n"
  "v_mfma_f32_16x16x32_bf16 v[104:107], a[244:247], a[84:87], v[104:107]\n"
  "ds_read_b64_tr_b16 a[124:125], v231 offset:16384\n"
  "v_mfma_f32_16x16x32_bf16 v[120:123], a[248:251], a[80:83], v[120:123]\n"
  "ds_read_b64_tr_b16 a[126:127], v231 offset:18432\n"
  "v_mfma_f32_16x16x32_bf16 v[120:123], a[252:255], a[84:87], v[120:123]\n"
  "v_exp_f32 v160, v160\n"
  "v_mfma_f32_16x16x32_bf16 v[76:79], a[224:227], a[88:91], v[76:79]\n"
  "ds_read_b128 a[80:83], v203 offset:40960\n"
  "v_mfma_f32_16x16x32_bf16 v[76:79], a[228:231], a[92:95], v[76:79]\n"
  "v_exp_f32 v161, v161\n"
  "v_mfma_f32_16x16x32_bf16 v[92:95], a[232:235], a[88:91], v[92:95]\n"
  "v_exp_f32 v162, v162\n"
  "v_mfma_f32_16x16x32_bf16 v[92:95], a[236:239], a[92:95], v[92:95]\n"
  "ds_read_b128 a[84:87], v203 offset:45056\n"
  "v_mfma_f32_16x16x32_bf16 v[108:111], a[240:243], a[88:91], v[108:111]\n"
  "s_xor_b32 s57, s12, 39936\n"
  "buffer_store_dwordx4 v[4:7], v192, s[32:35], s57 offen sc1\n"
  "v_mfma_f32_16x16x32_bf16 v[108:111], a[244:247], a[92:95], v[108:111]\n"
  "v_exp_f32 v163, v163\n"
  "v_mfma_f32_16x16x32_bf16 v[124:127], a[248:251], a[88:91], v[124:127]\n"
  "v_exp_f32 v164, v164\n"
  "v_mfma_f32_16x16x32_bf16 v[124:127], a[252:255], a[92:95], v[124:127]\n"
  "s_waitcnt lgkmcnt(2)\n"
  "v_exp_f32 v165, v165\n"
  "v_mfma_f32_16x16x32_bf16 v[64:67], a[192:195], a[64:67], v[64:67]\n"
  "ds_read_b128 a[88:91], v203 offset:41984\n"
  "v_mfma_f32_16x16x32_bf16 v[64:67], a[196:199], a[68:71], v[64:67]\n"
  "s_waitcnt vmcnt(9)\n"
  "ds_read_b64_tr_b16 a[128:129], v194 offset:0\n"
  "v_mfma_f32_16x16x32_bf16 v[80:83], a[200:203], a[64:67], v[80:83]\n"
  "ds_read_b64_tr_b16 a[130:131], v194 offset:2048\n"
  "v_mfma_f32_16x16x32_bf16 v[80:83], a[204:207], a[68:71], v[80:83]\n"
  "ds_read_b128 a[92:95], v203 offset:46080\n"
  "v_mfma_f32_16x16x32_bf16 v[96:99], a[208:211], a[64:67], v[96:99]\n"
  "ds_read_b64_tr_b16 a[136:137], v195 offset:0\n"
  "v_mfma_f32_16x16x32_bf16 v[96:99], a[212:215], a[68:71], v[96:99]\n"
  "ds_read_b64_tr_b16 a[138:139], v195 offset:2048\n"
  "v_mfma_f32_16x16x32_bf16 v[112:115], a[216:219], a[64:67], v[112:115]\n"
  "ds_read_b64_tr_b16 a[144:145], v196 offset:0\n"
  "v_mfma_f32_16x16x32_bf16 v[112:115], a[220:223], a[68:71], v[112:115]\n"
  "ds_read_b64_tr_b16 a[146:147], v196 offset:2048\n"
  "v_mfma_f32_16x16x32_bf16 v[68:71], a[192:195], a[72:75], v[68:71]\n"
  "ds_read_b128 a[64:67], v202 offset:32768\n"
  "v_mfma_f32_16x16x32_bf16 v[68:71], a[196:199], a[76:79], v[68:71]\n"
  "ds_read_b64_tr_b16 a[152:153], v197 offset:0\n"
  "v_mfma_f32_16x16x32_bf16 v[84:87], a[200:203], a[72:75], v[84:87]\n"
  "s_xor_b32 s57, s12, 168960\n"
  "ds_read_b64_tr_b16 a[154:155], v197 offset:2048\n"
  "buffer_store_dwordx4 v[36:39], v192, s[32:35], s57 offen sc1\n"
  "v_mfma_f32_16x16x32_bf16 v[84:87], a[204:207], a[76:79], v[84:87]\n"
  "ds_read_b128 a[68:71], v202 offset:36864\n"
  "v_mfma_f32_16x16x32_bf16 v[100:103], a[208:211], a[72:75], v[100:103]\n"
  "v_exp_f32 v166, v166\n"
  "v_mfma_f32_16x16x32_bf16 v[100:103], a[212:215], a[76:79], v[100:103]\n"
  "s_waitcnt vmcnt(6)\n"
  "ds_read_b64_tr_b16 a[132:133], v194 offset:16384\n"
  "v_mfma_f32_16x16x32_bf16 v[116:119], a[216:219], a[72:75], v[116:119]\n"
  "ds_read_b64_tr_b16 a[134:135], v194 offset:18432\n"
  "v_mfma_f32_16x16x32_bf16 v[116:119], a[220:223], a[76:79], v[116:119]\n"
  "s_waitcnt lgkmcnt(7)\n"
  "ds_read_b64_tr_b16 a[140:141], v195 offset:16384\n"
  "v_mfma_f32_16x16x32_bf16 v[72:75], a[192:195], a[80:83], v[72:75]\n"
  "ds_read_b128 a[72:75], v202 offset:33792\n"
  "v_mfma_f32_16x16x32_bf16 v[72:75], a[196:199], a[84:87], v[72:75]\n"
  "ds_read_b64_tr_b16 a[142:143], v195 offset:18432\n"
  "v_mfma_f32_16x16x32_bf16 v[88:91], a[200:203], a[80:83], v[88:91]\n"
  "ds_read_b64_tr_b16 a[148:149], v196 offset:16384\n"
  "v_mfma_f32_16x16x32_bf16 v[88:91], a[204:207], a[84:87], v[88:91]\n"
  "ds_read_b128 a[76:79], v202 offset:37888\n"
  "v_mfma_f32_16x16x32_bf16 v[104:107], a[208:211], a[80:83], v[104:107]\n"
  "ds_read_b64_tr_b16 a[150:151], v196 offset:18432\n"
  "v_mfma_f32_16x16x32_bf16 v[104:107], a[212:215], a[84:87], v[104:107]\n"
  "ds_read_b64_tr_b16 a[156:157], v197 offset:16384\n"
  "v_mfma_f32_16x16x32_bf16 v[120:123], a[216:219], a[80:83], v[120:123]\n"
  "ds_read_b64_tr_b16 a[158:159], v197 offset:18432\n"
  "v_mfma_f32_16x16x32_bf16 v[120:123], a[220:223], a[84:87], v[120:123]\n"
  "v_exp_f32 v167, v167\n"
  "v_mfma_f32_16x16x32_bf16 v[76:79], a[192:195], a[88:91], v[76:79]\n"
  "s_xor_b32 s57, s12, 71680\n"
  "ds_read_b128 a[80:83], v203 offset:32768\n"
  "buffer_store_dwordx4 v[24:27], v192, s[32:35], s57 offen sc1\n"
  "v_mfma_f32_16x16x32_bf16 v[76:79], a[196:199], a[92:95], v[76:79]\n"
  "v_exp_f32 v168, v168\n"
  "v_mfma_f32_16x16x32_bf16 v[92:95], a[200:203], a[88:91], v[92:95]\n"
  "v_exp_f32 v169, v169\n"
  "v_mfma_f32_16x16x32_bf16 v[92:95], a[204:207], a[92:95], v[92:95]\n"
  "ds_read_b128 a[84:87], v203 offset:36864\n"
  "v_mfma_f32_16x16x32_bf16 v[108:111], a[208:211], a[88:91], v[108:111]\n"
  "v_exp_f32 v170, v170\n"
  "v_mfma_f32_16x16x32_bf16 v[108:111], a[212:215], a[92:95], v[108:111]\n"
  "v_exp_f32 v171, v171\n"
  "v_mfma_f32_16x16x32_bf16 v[124:127], a[216:219], a[88:91], v[124:127]\n"
  "v_exp_f32 v172, v172\n"
  "v_mfma_f32_16x16x32_bf16 v[124:127], a[220:223], a[92:95], v[124:127]\n"
  "s_waitcnt lgkmcnt(2)\n"
  "v_exp_f32 v173, v173\n"
  "v_mfma_f32_16x16x32_bf16 v[64:67], a[0:3], a[64:67], v[64:67]\n"
  "ds_read_b128 a[88:91], v203 offset:33792\n"
  "v_mfma_f32_16x16x32_bf16 v[64:67], a[4:7], a[68:71], v[64:67]\n"
  "ds_read_b128 a[192:195], v202 offset:8192\n"
  "v_mfma_f32_16x16x32_bf16 v[80:83], a[8:11], a[64:67], v[80:83]\n"
  "v_exp_f32 v174, v174\n"
  "v_mfma_f32_16x16x32_bf16 v[80:83], a[12:15], a[68:71], v[80:83]\n"
  "ds_read_b128 a[92:95], v203 offset:37888\n"
  "v_mfma_f32_16x16x32_bf16 v[96:99], a[16:19], a[64:67], v[96:99]\n"
  "ds_read_b128 a[196:199], v202 offset:12288\n"
  "v_mfma_f32_16x16x32_bf16 v[96:99], a[20:23], a[68:71], v[96:99]\n"
  "v_exp_f32 v175, v175\n"
  "v_mfma_f32_16x16x32_bf16 v[112:115], a[24:27], a[64:67], v[112:115]\n"
  "s_xor_b32 s57, s12, 200704\n"
  "buffer_store_dwordx4 v[56:59], v192, s[32:35], s57 offen sc1\n"
  "v_mfma_f32_16x16x32_bf16 v[112:115], a[28:31], a[68:71], v[112:115]\n"
  "ds_read_b128 a[200:203], v202 offset:9216\n"
  "v_mul_f32 v128, 0x40faf233, v128\n"
  "v_mfma_f32_16x16x32_bf16 v[68:71], a[0:3], a[72:75], v[68:71]\n"
  "ds_read_b128 a[64:67], v202 offset:24576\n"
  "v_mul_f32 v129, 0x40faf233, v129\n"
  "v_mfma_f32_16x16x32_bf16 v[68:71], a[4:7], a[76:79], v[68:71]\n"
  "v_mul_f32 v130, 0x40faf233, v130\n"
  "v_mul_f32 v131, 0x40faf233, v131\n"
  "v_mfma_f32_16x16x32_bf16 v[84:87], a[8:11], a[72:75], v[84:87]\n"
  "ds_read_b128 a[204:207], v202 offset:13312\n"
  "v_mul_f32 v132, 0x40faf233, v132\n"
  "v_mfma_f32_16x16x32_bf16 v[84:87], a[12:15], a[76:79], v[84:87]\n"
  "ds_read_b128 a[68:71], v202 offset:28672\n"
  "v_mul_f32 v133, 0x40faf233, v133\n"
  "v_mfma_f32_16x16x32_bf16 v[100:103], a[16:19], a[72:75], v[100:103]\n"
  "v_mul_f32 v134, 0x40faf233, v134\n"
  "v_mul_f32 v135, 0x40faf233, v135\n"
  "v_mfma_f32_16x16x32_bf16 v[100:103], a[20:23], a[76:79], v[100:103]\n"
  "ds_read_b128 a[208:211], v203 offset:8192\n"
  "v_mul_f32 v136, 0x40faf233, v136\n"
  "v_mfma_f32_16x16x32_bf16 v[116:119], a[24:27], a[72:75], v[116:119]\n"
  "v_mul_f32 v137, 0x40faf233, v137\n"
  "v_mul_f32 v138, 0x40faf233, v138\n"
  "v_mfma_f32_16x16x32_bf16 v[116:119], a[28:31], a[76:79], v[116:119]\n"
  "s_waitcnt lgkmcnt(5)\n"
  "v_mul_f32 v139, 0x40faf233, v139\n"
  "v_mul_f32 v140, 0x40faf233, v140\n"
  "v_mfma_f32_16x16x32_bf16 v[72:75], a[0:3], a[80:83], v[72:75]\n"
  "ds_read_b128 a[72:75], v202 offset:25600\n"
  "v_mul_f32 v141, 0x40faf233, v141\n"
  "v_mfma_f32_16x16x32_bf16 v[72:75], a[4:7], a[84:87], v[72:75]\n"
  "ds_read_b128 a[212:215], v203 offset:12288\n"
  "v_mul_f32 v142, 0x40faf233, v142\n"
  "v_mfma_f32_16x16x32_bf16 v[88:91], a[8:11], a[80:83], v[88:91]\n"
  "v_mul_f32 v143, 0x40faf233, v143\n"
  "v_add_f32 v160, 1.0, v160\n"
  "v_mfma_f32_16x16x32_bf16 v[88:91], a[12:15], a[84:87], v[88:91]\n"
  "ds_read_b128 a[76:79], v202 offset:29696\n"
  "v_add_f32 v161, 1.0, v161\n"
  "v_mfma_f32_16x16x32_bf16 v[104:107], a[16:19], a[80:83], v[104:107]\n"
  "s_xor_b32 s57, s12, 104448\n"
  "ds_read_b128 a[216:219], v203 offset:9216\n"
  "buffer_store_dwordx4 v[28:31], v192, s[32:35], s57 offen sc1\n"
  "v_mfma_f32_16x16x32_bf16 v[104:107], a[20:23], a[84:87], v[104:107]\n"
  "v_add_f32 v162, 1.0, v162\n"
  "v_add_f32 v163, 1.0, v163\n"
  "v_mfma_f32_16x16x32_bf16 v[120:123], a[24:27], a[80:83], v[120:123]\n"
  "v_add_f32 v164, 1.0, v164\n"
  "v_add_f32 v165, 1.0, v165\n"
  "v_mfma_f32_16x16x32_bf16 v[120:123], a[28:31], a[84:87], v[120:123]\n"
  "ds_read_b128 a[220:223], v203 offset:13312\n"
  "v_add_f32 v166, 1.0, v166\n"
  "v_mfma_f32_16x16x32_bf16 v[76:79], a[0:3], a[88:91], v[76:79]\n"
  "ds_read_b128 a[80:83], v203 offset:24576\n"
  "v_add_f32 v167, 1.0, v167\n"
  "v_mfma_f32_16x16x32_bf16 v[76:79], a[4:7], a[92:95], v[76:79]\n"
  "v_add_f32 v168, 1.0, v168\n"
  "v_add_f32 v169, 1.0, v169\n"
  "v_mfma_f32_16x16x32_bf16 v[92:95], a[8:11], a[88:91], v[92:95]\n"
  "v_add_f32 v170, 1.0, v170\n"
  "v_add_f32 v171, 1.0, v171\n"
  "v_mfma_f32_16x16x32_bf16 v[92:95], a[12:15], a[92:95], v[92:95]\n"
  "ds_read_b128 a[84:87], v203 offset:28672\n"
  "v_add_f32 v172, 1.0, v172\n"
  "v_mfma_f32_16x16x32_bf16 v[108:111], a[16:19], a[88:91], v[108:111]\n"
  "v_add_f32 v173, 1.0, v173\n"
  "v_add_f32 v174, 1.0, v174\n"
  "v_mfma_f32_16x16x32_bf16 v[108:111], a[20:23], a[92:95], v[108:111]\n"
  "v_add_f32 v175, 1.0, v175\n"
  "v_mfma_f32_16x16x32_bf16 v[124:127], a[24:27], a[88:91], v[124:127]\n"
  "v_rcp_f32 v160, v160\n"
  "v_mfma_f32_16x16x32_bf16 v[124:127], a[28:31], a[92:95], v[124:127]\n"
  "s_waitcnt lgkmcnt(3)\n"
  "v_rcp_f32 v161, v161\n"
  "v_mfma_f32_16x16x32_bf16 v[64:67], a[32:35], a[64:67], v[64:67]\n"
  "ds_read_b128 a[88:91], v203 offset:25600\n"
  "v_mfma_f32_16x16x32_bf16 v[64:67], a[36:39], a[68:71], v[64:67]\n"
  "ds_read_b128 a[0:3], v202 offset:0\n"
  "v_mfma_f32_16x16x32_bf16 v[80:83], a[40:43], a[64:67], v[80:83]\n"
  "s_xor_b32 s57, s12, 233472\n"
  "buffer_store_dwordx4 v[60:63], v192, s[32:35], s57 offen sc1\n"
  "v_mfma_f32_16x16x32_bf16 v[80:83], a[44:47], a[68:71], v[80:83]\n"
  "ds_read_b128 a[92:95], v203 offset:29696\n"
  "v_mfma_f32_16x16x32_bf16 v[96:99], a[48:51], a[64:67], v[96:99]\n"
  "ds_read_b128 a[4:7], v202 offset:4096\n"
  "v_mfma_f32_16x16x32_bf16 v[96:99], a[52:55], a[68:71], v[96:99]\n"
  "v_rcp_f32 v162, v162\n"
  "v_mfma_f32_16x16x32_bf16 v[112:115], a[56:59], a[64:67], v[112:115]\n"
  "v_rcp_f32 v163, v163\n"
  "v_mfma_f32_16x16x32_bf16 v[112:115], a[60:63], a[68:71], v[112:115]\n"
  "ds_read_b128 a[8:11], v202 offset:1024\n"
  "v_mfma_f32_16x16x32_bf16 v[68:71], a[32:35], a[72:75], v[68:71]\n"
  "ds_read_b128 a[64:67], v202 offset:16384\n"
  "v_mfma_f32_16x16x32_bf16 v[68:71], a[36:39], a[76:79], v[68:71]\n"
  "v_rcp_f32 v164, v164\n"
  "v_mfma_f32_16x16x32_bf16 v[84:87], a[40:43], a[72:75], v[84:87]\n"
  "ds_read_b128 a[12:15], v202 offset:5120\n"
  "v_mfma_f32_16x16x32_bf16 v[84:87], a[44:47], a[76:79], v[84:87]\n"
  "ds_read_b128 a[68:71], v202 offset:20480\n"
  "v_mfma_f32_16x16x32_bf16 v[100:103], a[48:51], a[72:75], v[100:103]\n"
  "v_rcp_f32 v165, v165\n"
  "v_mfma_f32_16x16x32_bf16 v[100:103], a[52:55], a[76:79], v[100:103]\n"
  "ds_read_b128 a[16:19], v203 offset:0\n"
  "v_mfma_f32_16x16x32_bf16 v[116:119], a[56:59], a[72:75], v[116:119]\n"
  "v_rcp_f32 v166, v166\n"
  "v_mfma_f32_16x16x32_bf16 v[116:119], a[60:63], a[76:79], v[116:119]\n"
  "s_waitcnt lgkmcnt(5)\n"
  "v_rcp_f32 v167, v167\n"
  "v_mfma_f32_16x16x32_bf16 v[72:75], a[32:35], a[80:83], v[72:75]\n"
  "ds_read_b128 a[72:75], v202 offset:17408\n"
  "v_mfma_f32_16x16x32_bf16 v[72:75], a[36:39], a[84:87], v[72:75]\n"
  "ds_read_b128 a[20:23], v203 offset:4096\n"
  "v_mfma_f32_16x16x32_bf16 v[88:91], a[40:43], a[80:83], v[88:91]\n"
  "v_rcp_f32 v168, v168\n"
  "v_mfma_f32_16x16x32_bf16 v[88:91], a[44:47], a[84:87], v[88:91]\n"
  "ds_read_b128 a[76:79], v202 offset:21504\n"
  "v_mfma_f32_16x16x32_bf16 v[104:107], a[48:51], a[80:83], v[104:107]\n"
  "ds_read_b128 a[24:27], v203 offset:1024\n"
  "v_mfma_f32_16x16x32_bf16 v[104:107], a[52:55], a[84:87], v[104:107]\n"
  "v_rcp_f32 v169, v169\n"
  "v_mfma_f32_16x16x32_bf16 v[120:123], a[56:59], a[80:83], v[120:123]\n"
  "v_rcp_f32 v170, v170\n"
  "v_mfma_f32_16x16x32_bf16 v[120:123], a[60:63], a[84:87], v[120:123]\n"
  "ds_read_b128 a[28:31], v203 offset:5120\n"
  "v_mfma_f32_16x16x32_bf16 v[76:79], a[32:35], a[88:91], v[76:79]\n"
  "ds_read_b128 a[80:83], v203 offset:16384\n"
  "v_mfma_f32_16x16x32_bf16 v[76:79], a[36:39], a[92:95], v[76:79]\n"
  "v_rcp_f32 v171, v171\n"
  "v_mfma_f32_16x16x32_bf16 v[92:95], a[40:43], a[88:91], v[92:95]\n"
  "v_rcp_f32 v172, v172\n"
  "v_mfma_f32_16x16x32_bf16 v[92:95], a[44:47], a[92:95], v[92:95]\n"
  "ds_read_b128 a[84:87], v203 offset:20480\n"
  "v_mfma_f32_16x16x32_bf16 v[108:111], a[48:51], a[88:91], v[108:111]\n"
  "v_rcp_f32 v173, v173\n"
  "v_mfma_f32_16x16x32_bf16 v[108:111], a[52:55], a[92:95], v[108:111]\n"
  "v_rcp_f32 v174, v174\n"
  "v_mfma_f32_16x16x32_bf16 v[124:127], a[56:59], a[88:91], v[124:127]\n"
  "v_rcp_f32 v175, v175\n"
  "v_mfma_f32_16x16x32_bf16 v[124:127], a[60:63], a[92:95], v[124:127]\n"
  "s_waitcnt lgkmcnt(3)\n"
  "v_mul_f32 v128, v128, v160\n"
  "v_mul_f32 v129, v129, v161\n"
  "v_mfma_f32_16x16x32_bf16 v[64:67], a[160:163], a[64:67], v[64:67]\n"
  "ds_read_b128 a[88:91], v203 offset:17408\n"
  "v_mul_f32 v130, v130, v162\n"
  "v_mfma_f32_16x16x32_bf16 v[64:67], a[164:167], a[68:71], v[64:67]\n"
  "s_waitcnt vmcnt(9)\n"
  "ds_bpermute_b32 v240, v214, v248\n"
  "v_mul_f32 v131, v131, v163\n"
  "v_mfma_f32_16x16x32_bf16 v[64:67], a[96:99], a[192:195], v[64:67]\n"
  "ds_bpermute_b32 v241, v214, v249\n"
  "v_mul_f32 v132, v132, v164\n"
  "v_mfma_f32_16x16x32_bf16 v[64:67], a[100:103], a[196:199], v[64:67]\n"
  "ds_read_b128 a[92:95], v203 offset:21504\n"
  "v_mul_f32 v133, v133, v165\n"
  "v_mfma_f32_16x16x32_bf16 v[64:67], a[128:131], a[0:3], v[64:67]\n"
  "ds_bpermute_b32 v242, v214, v250\n"
  "v_mul_f32 v134, v134, v166\n"
  "v_mfma_f32_16x16x32_bf16 v[64:67], a[132:135], a[4:7], v[64:67]\n"
  "ds_bpermute_b32 v243, v214, v251\n"
  "v_mul_f32 v135, v135, v167\n"
  "v_mfma_f32_16x16x32_bf16 v[80:83], a[168:171], a[64:67], v[80:83]\n"
  "s_waitcnt vmcnt(8)\n"
  "ds_bpermute_b32 v244, v214, v252\n"
  "v_mul_f32 v136, v136, v168\n"
  "v_mfma_f32_16x16x32_bf16 v[80:83], a[172:175], a[68:71], v[80:83]\n"
  "ds_bpermute_b32 v245, v214, v253\n"
  "v_mul_f32 v137, v137, v169\n"
  "v_mfma_f32_16x16x32_bf16 v[80:83], a[104:107], a[192:195], v[80:83]\n"
  "ds_bpermute_b32 v246, v214, v254\n"
  "v_mul_f32 v138, v138, v170\n"
  "v_mfma_f32_16x16x32_bf16 v[80:83], a[108:111], a[196:199], v[80:83]\n"
  "ds_bpermute_b32 v247, v214, v255\n"
  "v_mul_f32 v139, v139, v171\n"
  "v_mfma_f32_16x16x32_bf16 v[80:83], a[136:139], a[0:3], v[80:83]\n"
  "v_mul_f32 v140, v140, v172\n"
  "v_mul_f32 v141, v141, v173\n"
  "v_mfma_f32_16x16x32_bf16 v[80:83], a[140:143], a[4:7], v[80:83]\n"
  "v_mul_f32 v142, v142, v174\n"
  "v_mul_f32 v143, v143, v175\n"
  "v_mfma_f32_16x16x32_bf16 v[96:99], a[176:179], a[64:67], v[96:99]\n"
  "v_mfma_f32_16x16x32_bf16 v[96:99], a[180:183], a[68:71], v[96:99]\n"
  "v_mfma_f32_16x16x32_bf16 v[96:99], a[112:115], a[192:195], v[96:99]\n"
  "v_mfma_f32_16x16x32_bf16 v[96:99], a[116:119], a[196:199], v[96:99]\n"
  "v_mfma_f32_16x16x32_bf16 v[96:99], a[144:147], a[0:3], v[96:99]\n"
  "v_cvt_pk_bf16_f32 v64, v64, v65\n"
  "v_cvt_pk_bf16_f32 v65, v66, v67\n"
  "v_mfma_f32_16x16x32_bf16 v[96:99], a[148:151], a[4:7], v[96:99]\n"
  "v_cvt_pk_bf16_f32 v66, v80, v81\n"
  "v_cvt_pk_bf16_f32 v67, v82, v83\n"
  "v_mfma_f32_16x16x32_bf16 v[112:115], a[184:187], a[64:67], v[112:115]\n"
  "s_xor_b32 s57, s12, 265216\n"
  "buffer_store_dwordx4 v[64:67], v192, s[32:35], s57 offen sc1\n"
  "v_mfma_f32_16x16x32_bf16 v[112:115], a[188:191], a[68:71], v[112:115]\n"
  "s_waitcnt lgkmcnt(0)\n"
  "v_permlane16_swap_b32 v240, v242\n"
  "v_permlane16_swap_b32 v241, v243\n"
  "v_mfma_f32_16x16x32_bf16 v[112:115], a[120:123], a[192:195], v[112:115]\n"
  "v_permlane16_swap_b32 v244, v246\n"
  "v_permlane16_swap_b32 v245, v247\n"
  "v_mfma_f32_16x16x32_bf16 v[112:115], a[124:127], a[196:199], v[112:115]\n"
  "v_lshlrev_b32 v176, 16, v240\n"
  "v_and_b32 v177, s64, v240\n"
  "v_mfma_f32_16x16x32_bf16 v[112:115], a[152:155], a[0:3], v[112:115]\n"
  "v_lshlrev_b32 v178, 16, v241\n"
  "v_and_b32 v179, s64, v241\n"
  "v_mfma_f32_16x16x32_bf16 v[112:115], a[156:159], a[4:7], v[112:115]\n"
  "v_lshlrev_b32 v180, 16, v242\n"
  "v_and_b32 v181, s64, v242\n"
  "v_mfma_f32_16x16x32_bf16 v[68:71], a[160:163], a[72:75], v[68:71]\n"
  "v_lshlrev_b32 v182, 16, v243\n"
  "v_and_b32 v183, s64, v243\n"
  "v_mfma_f32_16x16x32_bf16 v[68:71], a[164:167], a[76:79], v[68:71]\n"
  "v_lshlrev_b32 v184, 16, v244\n"
  "v_and_b32 v185, s64, v244\n"
  "v_mfma_f32_16x16x32_bf16 v[68:71], a[96:99], a[200:203], v[68:71]\n"
  "v_lshlrev_b32 v186, 16, v245\n"
  "v_and_b32 v187, s64, v245\n"
  "v_mfma_f32_16x16x32_bf16 v[68:71], a[100:103], a[204:207], v[68:71]\n"
  "v_lshlrev_b32 v188, 16, v246\n"
  "v_and_b32 v189, s64, v246\n"
  "v_mfma_f32_16x16x32_bf16 v[68:71], a[128:131], a[8:11], v[68:71]\n"
  "v_cvt_pk_bf16_f32 v96, v96, v97\n"
  "v_cvt_pk_bf16_f32 v97, v98, v99\n"
  "v_mfma_f32_16x16x32_bf16 v[68:71], a[132:135], a[12:15], v[68:71]\n"
  "v_cvt_pk_bf16_f32 v98, v112, v113\n"
  "v_cvt_pk_bf16_f32 v99, v114, v115\n"
  "v_mfma_f32_16x16x32_bf16 v[84:87], a[168:171], a[72:75], v[84:87]\n"
  "s_xor_b32 s57, s12, 394240\n"
  "buffer_store_dwordx4 v[96:99], v192, s[32:35], s57 offen sc1\n"
  "v_mfma_f32_16x16x32_bf16 v[84:87], a[172:175], a[76:79], v[84:87]\n"
  "v_lshlrev_b32 v190, 16, v247\n"
  "v_and_b32 v191, s64, v247\n"
  "v_mfma_f32_16x16x32_bf16 v[84:87], a[104:107], a[200:203], v[84:87]\n"
  "v_mfma_f32_16x16x32_bf16 v[84:87], a[108:111], a[204:207], v[84:87]\n"
  "v_mfma_f32_16x16x32_bf16 v[84:87], a[136:139], a[8:11], v[84:87]\n"
  "v_mfma_f32_16x16x32_bf16 v[84:87], a[140:143], a[12:15], v[84:87]\n"
  "v_mfma_f32_16x16x32_bf16 v[100:103], a[176:179], a[72:75], v[100:103]\n"
  "v_mfma_f32_16x16x32_bf16 v[100:103], a[180:183], a[76:79], v[100:103]\n"
  "v_mfma_f32_16x16x32_bf16 v[100:103], a[112:115], a[200:203], v[100:103]\n"
  "v_mfma_f32_16x16x32_bf16 v[100:103], a[116:119], a[204:207], v[100:103]\n"
  "v_mfma_f32_16x16x32_bf16 v[100:103], a[144:147], a[8:11], v[100:103]\n"
  "v_cvt_pk_bf16_f32 v68, v68, v69\n"
  "v_cvt_pk_bf16_f32 v69, v70, v71\n"
  "v_mfma_f32_16x16x32_bf16 v[100:103], a[148:151], a[12:15], v[100:103]\n"
  "v_cvt_pk_bf16_f32 v70, v84, v85\n"
  "v_cvt_pk_bf16_f32 v71, v86, v87\n"
  "v_mfma_f32_16x16x32_bf16 v[116:119], a[184:187], a[72:75], v[116:119]\n"
  "s_xor_b32 s57, s12, 297984\n"
  "buffer_store_dwordx4 v[68:71], v192, s[32:35], s57 offen sc1\n"
  "v_mfma_f32_16x16x32_bf16 v[116:119], a[188:191], a[76:79], v[116:119]\n"
  "v_mfma_f32_16x16x32_bf16 v[116:119], a[120:123], a[200:203], v[116:119]\n"
  "v_mfma_f32_16x16x32_bf16 v[116:119], a[124:127], a[204:207], v[116:119]\n"
  "v_mfma_f32_16x16x32_bf16 v[116:119], a[152:155], a[8:11], v[116:119]\n"
  "v_mfma_f32_16x16x32_bf16 v[116:119], a[156:159], a[12:15], v[116:119]\n"
  "v_mfma_f32_16x16x32_bf16 v[72:75], a[160:163], a[80:83], v[72:75]\n"
  "v_mfma_f32_16x16x32_bf16 v[72:75], a[164:167], a[84:87], v[72:75]\n"
  "v_mfma_f32_16x16x32_bf16 v[72:75], a[96:99], a[208:211], v[72:75]\n"
  "v_mfma_f32_16x16x32_bf16 v[72:75], a[100:103], a[212:215], v[72:75]\n"
  "v_mfma_f32_16x16x32_bf16 v[72:75], a[128:131], a[16:19], v[72:75]\n"
  "v_cvt_pk_bf16_f32 v100, v100, v101\n"
  "v_cvt_pk_bf16_f32 v101, v102, v103\n"
  "v_mfma_f32_16x16x32_bf16 v[72:75], a[132:135], a[20:23], v[72:75]\n"
  "v_cvt_pk_bf16_f32 v102, v116, v117\n"
  "v_cvt_pk_bf16_f32 v103, v118, v119\n"
  "v_mfma_f32_16x16x32_bf16 v[88:91], a[168:171], a[80:83], v[88:91]\n"
  "s_xor_b32 s57, s12, 427008\n"
  "buffer_store_dwordx4 v[100:103], v192, s[32:35], s57 offen sc1\n"
  "v_mfma_f32_16x16x32_bf16 v[88:91], a[172:175], a[84:87], v[88:91]\n"
  "v_mfma_f32_16x16x32_bf16 v[88:91], a[104:107], a[208:211], v[88:91]\n"
  "v_mfma_f32_16x16x32_bf16 v[88:91], a[108:111], a[212:215], v[88:91]\n"
  "v_mfma_f32_16x16x32_bf16 v[88:91], a[136:139], a[16:19], v[88:91]\n"
  "v_mfma_f32_16x16x32_bf16 v[88:91], a[140:143], a[20:23], v[88:91]\n"
  "v_mfma_f32_16x16x32_bf16 v[76:79], a[160:163], a[88:91], v[76:79]\n"
  "v_mfma_f32_16x16x32_bf16 v[76:79], a[164:167], a[92:95], v[76:79]\n"
  "v_mfma_f32_16x16x32_bf16 v[76:79], a[96:99], a[216:219], v[76:79]\n"
  "v_mfma_f32_16x16x32_bf16 v[76:79], a[100:103], a[220:223], v[76:79]\n"
  "v_mfma_f32_16x16x32_bf16 v[76:79], a[128:131], a[24:27], v[76:79]\n"
  "s_nop 2\n"
  "v_cvt_pk_bf16_f32 v88, v88, v89\n"
  "v_cvt_pk_bf16_f32 v89, v90, v91\n"
  "v_mfma_f32_16x16x32_bf16 v[76:79], a[132:135], a[28:31], v[76:79]\n"
  "v_cvt_pk_bf16_f32 v90, v72, v73\n"
  "v_cvt_pk_bf16_f32 v91, v74, v75\n"
  "v_mfma_f32_16x16x32_bf16 v[92:95], a[168:171], a[88:91], v[92:95]\n"
  "s_xor_b32 s57, s12, 329728\n"
  "buffer_store_dwordx4 v[88:91], v192, s[32:35], s57 offen sc1\n"
  "v_mfma_f32_16x16x32_bf16 v[92:95], a[172:175], a[92:95], v[92:95]\n"
  "L_dry3_1:\n"
  "L_pad13:\n"
  "s_cbranch_execz L_dryr3\n"
  "v_mfma_f32_16x16x32_bf16 v[92:95], a[104:107], a[216:219], v[92:95]\n"
  "v_mfma_f32_16x16x32_bf16 v[92:95], a[108:111], a[220:223], v[92:95]\n"
  "v_mfma_f32_16x16x32_bf16 v[92:95], a[136:139], a[24:27], v[92:95]\n"
  "v_mfma_f32_16x16x32_bf16 v[92:95], a[140:143], a[28:31], v[92:95]\n"
  "v_mfma_f32_16x16x32_bf16 v[104:107], a[176:179], a[80:83], v[104:107]\n"
  "v_mfma_f32_16x16x32_bf16 v[104:107], a[180:183], a[84:87], v[104:107]\n"
  "v_mfma_f32_16x16x32_bf16 v[104:107], a[112:115], a[208:211], v[104:107]\n"
  "v_mfma_f32_16x16x32_bf16 v[104:107], a[116:119], a[212:215], v[104:107]\n"
  "v_mfma_f32_16x16x32_bf16 v[104:107], a[144:147], a[16:19], v[104:107]\n"
  "s_nop 2\n"
  "v_cvt_pk_bf16_f32 v92, v92, v93\n"
  "v_cvt_pk_bf16_f32 v93, v94, v95\n"
  "v_mfma_f32_16x16x32_bf16 v[104:107], a[148:151], a[20:23], v[104:107]\n"
  "v_cvt_pk_bf16_f32 v94, v76, v77\n"
  "v_cvt_pk_bf16_f32 v95, v78, v79\n"
  "v_mfma_f32_16x16x32_bf16 v[120:123], a[184:187], a[80:83], v[120:123]\n"
  "s_xor_b32 s57, s12, 362496\n"
  "buffer_store_dwordx4 v[92:95], v192, s[32:35], s57 offen sc1\n"
  "v_mfma_f32_16x16x32_bf16 v[120:123], a[188:191], a[84:87], v[120:123]\n"
  "v_mfma_f32_16x16x32_bf16 v[120:123], a[120:123], a[208:211], v[120:123]\n"
  "v_mfma_f32_16x16x32_bf16 v[120:123], a[124:127], a[212:215], v[120:123]\n"
  "v_mfma_f32_16x16x32_bf16 v[120:123], a[152:155], a[16:19], v[120:123]\n"
  "v_mfma_f32_16x16x32_bf16 v[120:123], a[156:159], a[20:23], v[120:123]\n"
  "v_mfma_f32_16x16x32_bf16 v[108:111], a[176:179], a[88:91], v[108:111]\n"
  "v_mfma_f32_16x16x32_bf16 v[108:111], a[180:183], a[92:95], v[108:111]\n"
  "v_mfma_f32_16x16x32_bf16 v[108:111], a[112:115], a[216:219], v[108:111]\n"
  "v_mfma_f32_16x16x32_bf16 v[108:111], a[116:119], a[220:223], v[108:111]\n"
  "v_mfma_f32_16x16x32_bf16 v[108:111], a[144:147], a[24:27], v[108:111]\n"
  "v_mfma_f32_16x16x32_bf16 v[108:111], a[148:151], a[28:31], v[108:111]\n"
  "v_mfma_f32_16x16x32_bf16 v[124:127], a[184:187], a[88:91], v[124:127]\n"
  "v_mfma_f32_16x16x32_bf16 v[124:127], a[188:191], a[92:95], v[124:127]\n"
  "v_mfma_f32_16x16x32_bf16 v[124:127], a[120:123], a[216:219], v[124:127]\n"
  "v_mfma_f32_16x16x32_bf16 v[124:127], a[124:127], a[220:223], v[124:127]\n"
  "v_mfma_f32_16x16x32_bf16 v[124:127], a[152:155], a[24:27], v[124:127]\n"
  "v_mfma_f32_16x16x32_bf16 v[124:127], a[156:159], a[28:31], v[124:127]\n"
  "s_mov_b64 exec, 0xff0000\n"
  "buffer_load_dword v209, v238, s[68:71], 0 offen sc1\n"
  "s_mov_b64 exec, -1\n"
  "s_mov_b32 s8, 1\n"
  "s_mov_b64 exec, 0xff\n"
  "s_waitcnt vmcnt(0)\n"
  "buffer_store_dword v193, v239, s[68:71], 0 offen offset:32 sc1\n"
  "buffer_store_dword v193, v239, s[68:71], 0 offen offset:0 sc1\n"
  "s_mov_b64 exec, -1\n"
  "v_cmp_eq_u32 vcc, s65, v209\n"
  "s_nop 3\n"
  "s_bfe_u32 s57, vcc_lo, 0x80010\n"
  "s_branch L_tst\n"
  "L_poll2:\n"
  "s_mov_b64 exec, 0xff0000\n"
  "buffer_load_dword v209, v238, s[68:71], 0 offen sc1\n"
  "s_mov_b64 exec, -1\n"
  "s_add_u32 s66, s66, 1\n"
  "s_cmp_gt_u32 s66, 0x80000\n"
  "s_cbranch_scc1 L_got\n"
  "s_waitcnt vmcnt(0)\n"
  "v_cmp_eq_u32 vcc, s65, v209\n"
  "s_nop 3\n"
  "s_bfe_u32 s57, vcc_lo, 0x80010\n"
  "L_tst:\n"
  "s_andn2_b32 s58, s57, s8\n"
  "s_cbranch_scc0 L_poll2\n"
  "s_or_b32 s8, s8, s57\n"
  "s_bitcmp1_b32 s58, 7\n"
  "s_cbranch_scc0 L_sk7\n"
  "s_mov_b32 s57, 7168\n"
  "buffer_load_dwordx4 a[192:195], v192, s[72:75], s57 offen sc1\n"
  "s_mov_b32 s60, 39936\n"
  "buffer_load_dwordx4 a[160:163], v192, s[72:75], s60 offen sc1\n"
  "L_sk7:\n"
  "s_bitcmp1_b32 s58, 6\n"
  "s_cbranch_scc0 L_sk6\n"
  "s_mov_b32 s57, 6144\n"
  "buffer_load_dwordx4 a[196:199], v192, s[72:75], s57 offen sc1\n"
  "s_mov_b32 s60, 38912\n"
  "buffer_load_dwordx4 a[164:167], v192, s[72:75], s60 offen sc1\n"
  "L_sk6:\n"
  "s_bitcmp1_b32 s58, 5\n"
  "s_cbranch_scc0 L_sk5\n"
  "s_mov_b32 s57, 5120\n"
  "buffer_load_dwordx4 a[200:203], v192, s[72:75], s57 offen sc1\n"
  "s_mov_b32 s60, 37888\n"
  "buffer_load_dwordx4 a[168:171], v192, s[72:75], s60 offen sc1\n"
  "L_sk5:\n"
  "s_bitcmp1_b32 s58, 4\n"
  "s_cbranch_scc0 L_sk4\n"
  "s_mov_b32 s57, 4096\n"
  "buffer_load_dwordx4 a[204:207], v192, s[72:75], s57 offen sc1\n"
  "s_mov_b32 s60, 36864\n"
  "buffer_load_dwordx4 a[172:175], v192, s[72:75], s60 offen sc1\n"
  "L_sk4:\n"
  "s_bitcmp1_b32 s58, 3\n"
  "s_cbranch_scc0 L_sk3\n"
  "s_mov_b32 s57, 3072\n"
  "buffer_load_dwordx4 a[208:211], v192, s[72:75], s57 offen sc1\n"
  "s_mov_b32 s60, 35840\n"
  "buffer_load_dwordx4 a[176:179], v192, s[72:75], s60 offen sc1\n"
  "L_sk3:\n"
  "s_bitcmp1_b32 s58, 2\n"
  "s_cbranch_scc0 L_sk2\n"
  "s_mov_b32 s57, 2048\n"
  "buffer_load_dwordx4 a[212:215], v192, s[72:75], s57 offen sc1\n"
  "s_mov_b32 s60, 34816\n"
  "buffer_load_dwordx4 a[180:183], v192, s[72:75], s60 offen sc1\n"
  "L_sk2:\n"
  "s_bitcmp1_b32 s58, 1\n"
  "s_cbranch_scc0 L_sk1\n"
  "s_mov_b32 s57, 1024\n"
  "buffer_load_dwordx4 a[216:219], v192, s[72:75], s57 offen sc1\n"
  "s_mov_b32 s60, 33792\n"
  "buffer_load_dwordx4 a[184:187], v192, s[72:75], s60 offen sc1\n"
  "L_sk1:\n"
  "s_cmp_eq_u32 s8, 0xff\n"
  "s_cbranch_scc0 L_poll2\n"
  "L_got:\n"
  "s_waitcnt vmcnt(0)\n"
  "v_mfma_f32_16x16x32_bf16 v[104:107], v[150:153], a[192:195], v[104:107]\n"
  "v_mfma_f32_16x16x32_bf16 v[108:111], v[150:153], a[160:163], v[108:111]\n"
  "v_mfma_f32_16x16x32_bf16 v[120:123], v[154:157], a[194:197], v[120:123]\n"
  "v_mfma_f32_16x16x32_bf16 v[124:127], v[154:157], a[162:165], v[124:127]\n"
  "v_mfma_f32_16x16x32_bf16 v[104:107], v[154:157], a[198:201], v[104:107]\n"
  "v_mfma_f32_16x16x32_bf16 v[108:111], v[154:157], a[166:169], v[108:111]\n"
  "v_mfma_f32_16x16x32_bf16 v[120:123], v[154:157], a[202:205], v[120:123]\n"
  "v_mfma_f32_16x16x32_bf16 v[124:127], v[154:157], a[170:173], v[124:127]\n"
  "v_mfma_f32_16x16x32_bf16 v[104:107], v[154:157], a[206:209], v[104:107]\n"
  "v_mfma_f32_16x16x32_bf16 v[108:111], v[154:157], a[174:177], v[108:111]\n"
  "v_mfma_f32_16x16x32_bf16 v[120:123], v[154:157], a[210:213], v[120:123]\n"
  "v_mfma_f32_16x16x32_bf16 v[124:127], v[154:157], a[178:181], v[124:127]\n"
  "v_mfma_f32_16x16x32_bf16 v[104:107], v[154:157], a[214:217], v[104:107]\n"
  "v_mfma_f32_16x16x32_bf16 v[108:111], v[154:157], a[182:185], v[108:111]\n"
  "v_mfma_f32_16x16x32_bf16 v[120:123], v[152:155], a[216:219], v[120:123]\n"
  "v_mfma_f32_16x16x32_bf16 v[124:127], v[152:155], a[184:187], v[124:127]\n"
  "s_nop 4\n"
  "v_fmac_f32 v176, v104, v128\n"
  "v_fmac_f32 v177, v105, v129\n"
  "v_fmac_f32 v178, v106, v130\n"
  "v_fmac_f32 v179, v107, v131\n"
  "v_cvt_pk_bf16_f32 v160, v176, v177\n"
  "v_cvt_pk_bf16_f32 v161, v178, v179\n"
  "v_fmac_f32 v180, v120, v132\n"
  "v_fmac_f32 v181, v121, v133\n"
  "v_fmac_f32 v182, v122, v134\n"
  "v_fmac_f32 v183, v123, v135\n"
  "v_cvt_pk_bf16_f32 v162, v180, v181\n"
  "v_cvt_pk_bf16_f32 v163, v182, v183\n"
  "s_nop 0\n"
  "v_permlane16_swap_b32 v160, v162\n"
  "v_permlane16_swap_b32 v161, v163\n"
  "ds_bpermute_b32 v168, v204, v160\n"
  "ds_bpermute_b32 v169, v204, v161\n"
  "ds_bpermute_b32 v170, v204, v162\n"
  "ds_bpermute_b32 v171, v204, v163\n"
  "v_fmac_f32 v184, v108, v136\n"
  "v_fmac_f32 v185, v109, v137\n"
  "v_fmac_f32 v186, v110, v138\n"
  "v_fmac_f32 v187, v111, v139\n"
  "v_cvt_pk_bf16_f32 v164, v184, v185\n"
  "v_cvt_pk_bf16_f32 v165, v186, v187\n"
  "v_fmac_f32 v188, v124, v140\n"
  "v_fmac_f32 v189, v125, v141\n"
  "v_fmac_f32 v190, v126, v142\n"
  "v_fmac_f32 v191, v127, v143\n"
  "v_cvt_pk_bf16_f32 v166, v188, v189\n"
  "v_cvt_pk_bf16_f32 v167, v190, v191\n"
  "s_nop 0\n"
  "v_permlane16_swap_b32 v164, v166\n"
  "v_permlane16_swap_b32 v165, v167\n"
  "ds_bpermute_b32 v172, v204, v164\n"
  "ds_bpermute_b32 v173, v204, v165\n"
  "ds_bpermute_b32 v174, v204, v166\n"
  "ds_bpermute_b32 v175, v204, v167\n"
  "s_waitcnt lgkmcnt(4)\n"
  "global_store_dwordx4 v206, v[168:171], s[40:41]\n"
  "s_waitcnt lgkmcnt(0)\n"
  "global_store_dwordx4 v207, v[172:175], s[40:41]\n"
  "s_endpgm\n"
  "L_cend:\n"
  ".p2align 6\n"
  "L_even:\n"
  "s_waitcnt lgkmcnt(7)\n"
  "v_mfma_f32_16x16x32_bf16 v[16:19], a[64:67], a[32:35], 0\n"
  "s_waitcnt lgkmcnt(6)\n"
  "s_add_u32 m0, s51, 0\n"
  "ds_read_b128 a[56:59], v203 offset:33792\n"
  "v_mfma_f32_16x16x32_bf16 v[16:19], a[68:71], a[36:39], v[16:19]\n"
  "s_add_u32 s53, s13, 786432\n"
  "L_dry3_2:\n"
  "L_pad14:\n"
  "s_cbranch_execz L_dryr3\n"
  "ds_read_b128 a[60:63], v203 offset:37888\n"
  "v_mfma_f32_16x16x32_bf16 v[128:131], a[72:75], a[32:35], 0\n"
  "s_waitcnt lgkmcnt(6)\n"
  "buffer_load_dwordx4 v198, s[44:47], s53 offen offset:0 lds\n"
  "v_mfma_f32_16x16x32_bf16 v[128:131], a[76:79], a[36:39], v[128:131]\n"
  "ds_read_b128 a[160:163], v202 offset:40960\n"
  "v_cvt_pk_bf16_f32 v74, v88, v89\n"
  "v_mfma_f32_16x16x32_bf16 v[48:51], a[80:83], a[32:35], 0\n"
  "s_waitcnt lgkmcnt(5)\n"
  "ds_read_b128 a[164:167], v202 offset:45056\n"
  "v_cvt_pk_bf16_f32 v75, v90, v91\n"
  "v_mfma_f32_16x16x32_bf16 v[48:51], a[84:87], a[36:39], v[48:51]\n"
  "s_waitcnt vmcnt(13)\n"
  "ds_read_b64_tr_b16 a[96:97], v194 offset:8192\n"
  "v_cvt_pk_bf16_f32 v104, v104, v105\n"
  "v_mfma_f32_16x16x32_bf16 v[144:147], a[88:91], a[32:35], 0\n"
  "s_waitcnt lgkmcnt(5)\n"
  "buffer_load_dwordx4 v199, s[44:47], s53 offen offset:1024 lds\n"
  "v_mfma_f32_16x16x32_bf16 v[144:147], a[92:95], a[36:39], v[144:147]\n"
  "ds_read_b128 a[168:171], v202 offset:41984\n"
  "v_cvt_pk_bf16_f32 v105, v106, v107\n"
  "v_mfma_f32_16x16x32_bf16 v[20:23], a[64:67], a[40:43], 0\n"
  "ds_read_b128 a[172:175], v202 offset:46080\n"
  "v_cvt_pk_bf16_f32 v106, v120, v121\n"
  "v_mfma_f32_16x16x32_bf16 v[20:23], a[68:71], a[44:47], v[20:23]\n"
  "ds_read_b64_tr_b16 a[98:99], v194 offset:10240\n"
  "v_cvt_pk_bf16_f32 v107, v122, v123\n"
  "v_mfma_f32_16x16x32_bf16 v[132:135], a[72:75], a[40:43], 0\n"
  "buffer_load_dwordx4 v200, s[44:47], s53 offen offset:2048 lds\n"
  "v_mfma_f32_16x16x32_bf16 v[132:135], a[76:79], a[44:47], v[132:135]\n"
  "ds_read_b64_tr_b16 a[104:105], v195 offset:8192\n"
  "v_cvt_pk_bf16_f32 v76, v76, v77\n"
  "v_mfma_f32_16x16x32_bf16 v[52:55], a[80:83], a[40:43], 0\n"
  "ds_read_b128 a[176:179], v203 offset:40960\n"
  "v_cvt_pk_bf16_f32 v77, v78, v79\n"
  "v_mfma_f32_16x16x32_bf16 v[52:55], a[84:87], a[44:47], v[52:55]\n"
  "ds_read_b128 a[180:183], v203 offset:45056\n"
  "v_cvt_pk_bf16_f32 v78, v92, v93\n"
  "v_mfma_f32_16x16x32_bf16 v[148:151], a[88:91], a[40:43], 0\n"
  "buffer_load_dwordx4 v201, s[44:47], s53 offen offset:3072 lds\n"
  "v_mfma_f32_16x16x32_bf16 v[148:151], a[92:95], a[44:47], v[148:151]\n"
  "ds_read_b64_tr_b16 a[106:107], v195 offset:10240\n"
  "v_cvt_pk_bf16_f32 v79, v94, v95\n"
  "v_mfma_f32_16x16x32_bf16 v[24:27], a[64:67], a[48:51], 0\n"
  "s_add_u32 m0, s51, 4096\n"
  "ds_read_b64_tr_b16 a[112:113], v196 offset:8192\n"
  "v_mfma_f32_16x16x32_bf16 v[24:27], a[68:71], a[52:55], v[24:27]\n"
  "s_add_u32 s54, s13, 819200\n"
  "ds_read_b64_tr_b16 a[114:115], v196 offset:10240\n"
  "v_mfma_f32_16x16x32_bf16 v[136:139], a[72:75], a[48:51], 0\n"
  "buffer_load_dwordx4 v198, s[44:47], s54 offen offset:0 lds\n"
  "v_mfma_f32_16x16x32_bf16 v[136:139], a[76:79], a[52:55], v[136:139]\n"
  "ds_read_b128 a[184:187], v203 offset:41984\n"
  "v_cvt_pk_bf16_f32 v108, v108, v109\n"
  "v_mfma_f32_16x16x32_bf16 v[56:59], a[80:83], a[48:51], 0\n"
  "ds_read_b128 a[188:191], v203 offset:46080\n"
  "v_cvt_pk_bf16_f32 v109, v110, v111\n"
  "v_mfma_f32_16x16x32_bf16 v[56:59], a[84:87], a[52:55], v[56:59]\n"
  "ds_read_b64_tr_b16 a[120:121], v197 offset:8192\n"
  "v_cvt_pk_bf16_f32 v110, v124, v125\n"
  "v_mfma_f32_16x16x32_bf16 v[152:155], a[88:91], a[48:51], 0\n"
  "buffer_load_dwordx4 v199, s[44:47], s54 offen offset:1024 lds\n"
  "v_mfma_f32_16x16x32_bf16 v[152:155], a[92:95], a[52:55], v[152:155]\n"
  "s_waitcnt lgkmcnt(6)\n"
  "ds_read_b64_tr_b16 a[122:123], v197 offset:10240\n"
  "v_cvt_pk_bf16_f32 v111, v126, v127\n"
  "v_mfma_f32_16x16x32_bf16 v[28:31], a[64:67], a[56:59], 0\n"
  "s_waitcnt vmcnt(14)\n"
  "ds_read_b64_tr_b16 a[100:101], v194 offset:12288\n"
  "v_permlane16_swap_b32 v0, v2\n"
  "v_mfma_f32_16x16x32_bf16 v[28:31], a[68:71], a[60:63], v[28:31]\n"
  "ds_read_b64_tr_b16 a[102:103], v194 offset:14336\n"
  "v_permlane16_swap_b32 v1, v3\n"
  "v_mfma_f32_16x16x32_bf16 v[140:143], a[72:75], a[56:59], 0\n"
  "buffer_load_dwordx4 v200, s[44:47], s54 offen offset:2048 lds\n"
  "v_mfma_f32_16x16x32_bf16 v[140:143], a[76:79], a[60:63], v[140:143]\n"
  "ds_read_b64_tr_b16 a[108:109], v195 offset:12288\n"
  "v_permlane16_swap_b32 v32, v34\n"
  "v_mfma_f32_16x16x32_bf16 v[60:63], a[80:83], a[56:59], 0\n"
  "ds_read_b64_tr_b16 a[110:111], v195 offset:14336\n"
  "v_permlane16_swap_b32 v33, v35\n"
  "v_mfma_f32_16x16x32_bf16 v[60:63], a[84:87], a[60:63], v[60:63]\n"
  "ds_read_b64_tr_b16 a[116:117], v196 offset:12288\n"
  "v_permlane16_swap_b32 v64, v66\n"
  "v_mfma_f32_16x16x32_bf16 v[156:159], a[88:91], a[56:59], 0\n"
  "buffer_load_dwordx4 v201, s[44:47], s54 offen offset:3072 lds\n"
  "ds_read_b64_tr_b16 a[124:125], v197 offset:12288\n"
  "v_mfma_f32_16x16x32_bf16 v[156:159], a[92:95], a[60:63], v[156:159]\n"
  "ds_read_b64_tr_b16 a[118:119], v196 offset:14336\n"
  "ds_read_b64_tr_b16 a[126:127], v197 offset:14336\n"
  "v_mfma_f32_16x16x32_bf16 v[16:19], a[96:99], a[160:163], v[16:19]\n"
  "s_waitcnt lgkmcnt(6)\n"
  "s_add_u32 m0, s51, 8192\n"
  "ds_read_b128 a[0:3], v202 offset:49152\n"
  "v_mfma_f32_16x16x32_bf16 v[16:19], a[100:103], a[164:167], v[16:19]\n"
  "s_add_u32 s53, s55, 786432\n"
  "ds_read_b128 a[4:7], v202 offset:53248\n"
  "v_mfma_f32_16x16x32_bf16 v[128:131], a[104:107], a[160:163], v[128:131]\n"
  "s_waitcnt lgkmcnt(6)\n"
  "s_mov_b32 s16, s42\n"
  "buffer_load_dwordx4 v198, s[44:47], s53 offen offset:0 lds\n"
  "v_mfma_f32_16x16x32_bf16 v[128:131], a[108:111], a[164:167], v[128:131]\n"
  "s_waitcnt vmcnt(13)\n"
  "s_mov_b32 s17, s43\n"
  "ds_read_b64_tr_b16 a[64:65], v194 offset:16384\n"
  "v_mfma_f32_16x16x32_bf16 v[48:51], a[112:115], a[160:163], v[48:51]\n"
  "s_waitcnt lgkmcnt(4)\n"
  "s_lshl_b32 s57, s50, 8\n"
  "ds_read_b64_tr_b16 a[66:67], v194 offset:18432\n"
  "v_mfma_f32_16x16x32_bf16 v[48:51], a[116:119], a[164:167], v[48:51]\n"
  "s_sub_u32 s18, 0x80000, s57\n"
  "ds_read_b64_tr_b16 a[72:73], v195 offset:16384\n"
  "v_mfma_f32_16x16x32_bf16 v[144:147], a[120:123], a[160:163], v[144:147]\n"
  "s_waitcnt lgkmcnt(5)\n"
  "buffer_load_dwordx4 v199, s[44:47], s53 offen offset:1024 lds\n"
  "v_mfma_f32_16x16x32_bf16 v[144:147], a[124:127], a[164:167], v[144:147]\n"
  "ds_read_b128 a[8:11], v202 offset:50176\n"
  "v_permlane16_swap_b32 v65, v67\n"
  "v_mfma_f32_16x16x32_bf16 v[20:23], a[96:99], a[168:171], v[20:23]\n"
  "ds_read_b128 a[12:15], v202 offset:54272\n"
  "v_permlane16_swap_b32 v96, v98\n"
  "v_mfma_f32_16x16x32_bf16 v[20:23], a[100:103], a[172:175], v[20:23]\n"
  "ds_read_b64_tr_b16 a[74:75], v195 offset:18432\n"
  "v_permlane16_swap_b32 v97, v99\n"
  "v_mfma_f32_16x16x32_bf16 v[132:135], a[104:107], a[168:171], v[132:135]\n"
  "buffer_load_dwordx4 v200, s[44:47], s53 offen offset:2048 lds\n"
  "v_mfma_f32_16x16x32_bf16 v[132:135], a[108:111], a[172:175], v[132:135]\n"
  "ds_read_b64_tr_b16 a[80:81], v196 offset:16384\n"
  "v_permlane16_swap_b32 v4, v6\n"
  "v_mfma_f32_16x16x32_bf16 v[52:55], a[112:115], a[168:171], v[52:55]\n"
  "ds_read_b128 a[16:19], v203 offset:49152\n"
  "v_permlane16_swap_b32 v5, v7\n"
  "v_mfma_f32_16x16x32_bf16 v[52:55], a[116:119], a[172:175], v[52:55]\n"
  "ds_read_b128 a[20:23], v203 offset:53248\n"
  "v_permlane16_swap_b32 v36, v38\n"
  "v_mfma_f32_16x16x32_bf16 v[148:151], a[120:123], a[168:171], v[148:151]\n"
  "buffer_load_dwordx4 v201, s[44:47], s53 offen offset:3072 lds\n"
  "v_mfma_f32_16x16x32_bf16 v[148:151], a[124:127], a[172:175], v[148:151]\n"
  "ds_read_b64_tr_b16 a[82:83], v196 offset:18432\n"
  "v_permlane16_swap_b32 v37, v39\n"
  "v_mfma_f32_16x16x32_bf16 v[24:27], a[96:99], a[176:179], v[24:27]\n"
  "s_add_u32 m0, s51, 12288\n"
  "ds_read_b64_tr_b16 a[88:89], v197 offset:16384\n"
  "v_mfma_f32_16x16x32_bf16 v[24:27], a[100:103], a[180:183], v[24:27]\n"
  "s_add_u32 s54, s55, 819200\n"
  "ds_read_b64_tr_b16 a[90:91], v197 offset:18432\n"
  "v_mfma_f32_16x16x32_bf16 v[136:139], a[104:107], a[176:179], v[136:139]\n"
  "buffer_load_dwordx4 v198, s[44:47], s54 offen offset:0 lds\n"
  "v_mfma_f32_16x16x32_bf16 v[136:139], a[108:111], a[180:183], v[136:139]\n"
  "ds_read_b128 a[24:27], v203 offset:50176\n"
  "v_permlane16_swap_b32 v68, v70\n"
  "v_mfma_f32_16x16x32_bf16 v[56:59], a[112:115], a[176:179], v[56:59]\n"
  "ds_read_b128 a[28:31], v203 offset:54272\n"
  "v_permlane16_swap_b32 v69, v71\n"
  "v_mfma_f32_16x16x32_bf16 v[56:59], a[116:119], a[180:183], v[56:59]\n"
  "s_waitcnt vmcnt(13)\n"
  "ds_read_b64_tr_b16 a[68:69], v194 offset:20480\n"
  "v_permlane16_swap_b32 v100, v102\n"
  "v_mfma_f32_16x16x32_bf16 v[152:155], a[120:123], a[176:179], v[152:155]\n"
  "buffer_load_dwordx4 v199, s[44:47], s54 offen offset:1024 lds\n"
  "v_mfma_f32_16x16x32_bf16 v[152:155], a[124:127], a[180:183], v[152:155]\n"
  "ds_read_b64_tr_b16 a[70:71], v194 offset:22528\n"
  "v_permlane16_swap_b32 v101, v103\n"
  "v_mfma_f32_16x16x32_bf16 v[28:31], a[96:99], a[184:187], v[28:31]\n"
  "ds_read_b64_tr_b16 a[76:77], v195 offset:20480\n"
  "v_permlane16_swap_b32 v8, v10\n"
  "v_mfma_f32_16x16x32_bf16 v[28:31], a[100:103], a[188:191], v[28:31]\n"
  "ds_read_b64_tr_b16 a[78:79], v195 offset:22528\n"
  "v_permlane16_swap_b32 v9, v11\n"
  "v_mfma_f32_16x16x32_bf16 v[140:143], a[104:107], a[184:187], v[140:143]\n"
  "buffer_load_dwordx4 v200, s[44:47], s54 offen offset:2048 lds\n"
  "v_mfma_f32_16x16x32_bf16 v[140:143], a[108:111], a[188:191], v[140:143]\n"
  "ds_read_b64_tr_b16 a[84:85], v196 offset:20480\n"
  "v_permlane16_swap_b32 v40, v42\n"
  "v_mfma_f32_16x16x32_bf16 v[60:63], a[112:115], a[184:187], v[60:63]\n"
  "ds_read_b64_tr_b16 a[86:87], v196 offset:22528\n"
  "v_permlane16_swap_b32 v41, v43\n"
  "v_mfma_f32_16x16x32_bf16 v[60:63], a[116:119], a[188:191], v[60:63]\n"
  "ds_read_b64_tr_b16 a[92:93], v197 offset:20480\n"
  "v_permlane16_swap_b32 v72, v74\n"
  "v_mfma_f32_16x16x32_bf16 v[156:159], a[120:123], a[184:187], v[156:159]\n"
  "buffer_load_dwordx4 v201, s[44:47], s54 offen offset:3072 lds\n"
  "v_mfma_f32_16x16x32_bf16 v[156:159], a[124:127], a[188:191], v[156:159]\n"
  "s_waitcnt lgkmcnt(6)\n"
  "ds_read_b64_tr_b16 a[94:95], v197 offset:22528\n"
  "v_permlane16_swap_b32 v73, v75\n"
  "v_mfma_f32_16x16x32_bf16 v[16:19], a[64:67], a[0:3], v[16:19]\n"
  "s_waitcnt lgkmcnt(6)\n"
  "s_add_u32 m0, s51, 16384\n"
  "ds_read_b128 a[128:131], v202 offset:57344\n"
  "v_mfma_f32_16x16x32_bf16 v[16:19], a[68:71], a[4:7], v[16:19]\n"
  "s_add_u32 s53, s55, 524288\n"
  "ds_read_b128 a[132:135], v202 offset:61440\n"
  "v_mfma_f32_16x16x32_bf16 v[128:131], a[72:75], a[0:3], v[128:131]\n"
  "s_waitcnt lgkmcnt(6)\n"
  "buffer_load_dwordx4 v198, s[44:47], s53 offen offset:0 lds\n"
  "v_mfma_f32_16x16x32_bf16 v[128:131], a[76:79], a[4:7], v[128:131]\n"
  "s_waitcnt vmcnt(13)\n"
  "ds_read_b64_tr_b16 a[96:97], v194 offset:0\n"
  "v_permlane16_swap_b32 v104, v106\n"
  "v_mfma_f32_16x16x32_bf16 v[48:51], a[80:83], a[0:3], v[48:51]\n"
  "s_waitcnt lgkmcnt(5)\n"
  "ds_read_b64_tr_b16 a[98:99], v194 offset:2048\n"
  "v_permlane16_swap_b32 v105, v107\n"
  "v_mfma_f32_16x16x32_bf16 v[48:51], a[84:87], a[4:7], v[48:51]\n"
  "ds_read_b64_tr_b16 a[104:105], v195 offset:0\n"
  "v_permlane16_swap_b32 v12, v14\n"
  "v_mfma_f32_16x16x32_bf16 v[144:147], a[88:91], a[0:3], v[144:147]\n"
  "s_waitcnt lgkmcnt(5)\n"
  "buffer_load_dwordx4 v199, s[44:47], s53 offen offset:1024 lds\n"
  "v_mfma_f32_16x16x32_bf16 v[144:147], a[92:95], a[4:7], v[144:147]\n"
  "ds_read_b128 a[136:139], v202 offset:58368\n"
  "v_permlane16_swap_b32 v13, v15\n"
  "v_mfma_f32_16x16x32_bf16 v[20:23], a[64:67], a[8:11], v[20:23]\n"
  "ds_read_b128 a[140:143], v202 offset:62464\n"
  "v_permlane16_swap_b32 v44, v46\n"
  "v_mfma_f32_16x16x32_bf16 v[20:23], a[68:71], a[12:15], v[20:23]\n"
  "ds_read_b64_tr_b16 a[106:107], v195 offset:2048\n"
  "v_permlane16_swap_b32 v45, v47\n"
  "v_mfma_f32_16x16x32_bf16 v[132:135], a[72:75], a[8:11], v[132:135]\n"
  "buffer_load_dwordx4 v200, s[44:47], s53 offen offset:2048 lds\n"
  "v_mfma_f32_16x16x32_bf16 v[132:135], a[76:79], a[12:15], v[132:135]\n"
  "ds_read_b64_tr_b16 a[112:113], v196 offset:0\n"
  "v_permlane16_swap_b32 v76, v78\n"
  "v_mfma_f32_16x16x32_bf16 v[52:55], a[80:83], a[8:11], v[52:55]\n"
  "ds_read_b128 a[144:147], v203 offset:57344\n"
  "v_permlane16_swap_b32 v77, v79\n"
  "v_mfma_f32_16x16x32_bf16 v[52:55], a[84:87], a[12:15], v[52:55]\n"
  "ds_read_b128 a[148:151], v203 offset:61440\n"
  "v_permlane16_swap_b32 v108, v110\n"
  "v_mfma_f32_16x16x32_bf16 v[148:151], a[88:91], a[8:11], v[148:151]\n"
  "buffer_load_dwordx4 v201, s[44:47], s53 offen offset:3072 lds\n"
  "v_mfma_f32_16x16x32_bf16 v[148:151], a[92:95], a[12:15], v[148:151]\n"
  "ds_read_b64_tr_b16 a[114:115], v196 offset:2048\n"
  "v_permlane16_swap_b32 v109, v111\n"
  "v_mfma_f32_16x16x32_bf16 v[24:27], a[64:67], a[16:19], v[24:27]\n"
  "s_add_u32 m0, s51, 20480\n"
  "ds_read_b64_tr_b16 a[120:121], v197 offset:0\n"
  "v_mfma_f32_16x16x32_bf16 v[24:27], a[68:71], a[20:23], v[24:27]\n"
  "s_add_u32 s54, s55, 557056\n"
  "ds_read_b64_tr_b16 a[122:123], v197 offset:2048\n"
  "v_mfma_f32_16x16x32_bf16 v[136:139], a[72:75], a[16:19], v[136:139]\n"
  "buffer_load_dwordx4 v198, s[44:47], s54 offen offset:0 lds\n"
  "v_mfma_f32_16x16x32_bf16 v[136:139], a[76:79], a[20:23], v[136:139]\n"
  "ds_read_b128 a[152:155], v203 offset:58368\n"
  "v_mfma_f32_16x16x32_bf16 v[56:59], a[80:83], a[16:19], v[56:59]\n"
  "ds_read_b128 a[156:159], v203 offset:62464\n"
  "v_mfma_f32_16x16x32_bf16 v[56:59], a[84:87], a[20:23], v[56:59]\n"
  "s_waitcnt vmcnt(13)\n"
  "ds_read_b64_tr_b16 a[100:101], v194 offset:4096\n"
  "v_mfma_f32_16x16x32_bf16 v[152:155], a[88:91], a[16:19], v[152:155]\n"
  "buffer_load_dwordx4 v199, s[44:47], s54 offen offset:1024 lds\n"
  "v_mfma_f32_16x16x32_bf16 v[152:155], a[92:95], a[20:23], v[152:155]\n"
  "ds_read_b64_tr_b16 a[102:103], v194 offset:6144\n"
  "v_mfma_f32_16x16x32_bf16 v[28:31], a[64:67], a[24:27], v[28:31]\n"
  "ds_read_b64_tr_b16 a[108:109], v195 offset:4096\n"
  "v_mfma_f32_16x16x32_bf16 v[28:31], a[68:71], a[28:31], v[28:31]\n"
  "ds_read_b64_tr_b16 a[110:111], v195 offset:6144\n"
  "v_mfma_f32_16x16x32_bf16 v[140:143], a[72:75], a[24:27], v[140:143]\n"
  "buffer_load_dwordx4 v200, s[44:47], s54 offen offset:2048 lds\n"
  "v_mfma_f32_16x16x32_bf16 v[140:143], a[76:79], a[28:31], v[140:143]\n"
  "ds_read_b64_tr_b16 a[116:117], v196 offset:4096\n"
  "v_mfma_f32_16x16x32_bf16 v[60:63], a[80:83], a[24:27], v[60:63]\n"
  "ds_read_b64_tr_b16 a[118:119], v196 offset:6144\n"
  "v_mfma_f32_16x16x32_bf16 v[60:63], a[84:87], a[28:31], v[60:63]\n"
  "ds_read_b64_tr_b16 a[124:125], v197 offset:4096\n"
  "v_mfma_f32_16x16x32_bf16 v[156:159], a[88:91], a[24:27], v[156:159]\n"
  "buffer_load_dwordx4 v201, s[44:47], s54 offen offset:3072 lds\n"
  "v_mfma_f32_16x16x32_bf16 v[156:159], a[92:95], a[28:31], v[156:159]\n"
  "s_waitcnt lgkmcnt(6)\n"
  "ds_read_b64_tr_b16 a[126:127], v197 offset:6144\n"
  "v_mfma_f32_16x16x32_bf16 v[16:19], a[96:99], a[128:131], v[16:19]\n"
  "s_waitcnt lgkmcnt(6)\n"
  "s_waitcnt vmcnt(12)\n"
  "s_add_u32 m0, s51, 0\n"
  "ds_read_b64_tr_b16 a[64:65], v194 offset:8192\n"
  "v_mfma_f32_16x16x32_bf16 v[16:19], a[100:103], a[132:135], v[16:19]\n"
  "s_add_u32 s53, s55, 262144\n"
  "ds_read_b64_tr_b16 a[66:67], v194 offset:10240\n"
  "v_mfma_f32_16x16x32_bf16 v[128:131], a[104:107], a[128:131], v[128:131]\n"
  "s_waitcnt lgkmcnt(6)\n"
  "buffer_load_dwordx4 v198, s[44:47], s53 offen offset:0 lds\n"
  "v_mfma_f32_16x16x32_bf16 v[128:131], a[108:111], a[132:135], v[128:131]\n"
  "ds_read_b64_tr_b16 a[72:73], v195 offset:8192\n"
  "v_mfma_f32_16x16x32_bf16 v[48:51], a[112:115], a[128:131], v[48:51]\n"
  "s_waitcnt lgkmcnt(5)\n"
  "ds_read_b64_tr_b16 a[74:75], v195 offset:10240\n"
  "v_mfma_f32_16x16x32_bf16 v[48:51], a[116:119], a[132:135], v[48:51]\n"
  "ds_read_b64_tr_b16 a[80:81], v196 offset:8192\n"
  "v_mfma_f32_16x16x32_bf16 v[144:147], a[120:123], a[128:131], v[144:147]\n"
  "s_waitcnt lgkmcnt(5)\n"
  "buffer_load_dwordx4 v199, s[44:47], s53 offen offset:1024 lds\n"
  "v_mfma_f32_16x16x32_bf16 v[144:147], a[124:127], a[132:135], v[144:147]\n"
  "ds_read_b64_tr_b16 a[82:83], v196 offset:10240\n"
  "v_mfma_f32_16x16x32_bf16 v[20:23], a[96:99], a[136:139], v[20:23]\n"
  "ds_read_b64_tr_b16 a[88:89], v197 offset:8192\n"
  "v_cvt_pk_bf16_f32 v16, v16, v17\n"
  "v_mfma_f32_16x16x32_bf16 v[20:23], a[100:103], a[140:143], v[20:23]\n"
  "ds_read_b64_tr_b16 a[90:91], v197 offset:10240\n"
  "v_cvt_pk_bf16_f32 v17, v18, v19\n"
  "v_mfma_f32_16x16x32_bf16 v[132:135], a[104:107], a[136:139], v[132:135]\n"
  "buffer_load_dwordx4 v200, s[44:47], s53 offen offset:2048 lds\n"
  "v_mfma_f32_16x16x32_bf16 v[132:135], a[108:111], a[140:143], v[132:135]\n"
  "v_cvt_pk_bf16_f32 v18, v128, v129\n"
  "v_cvt_pk_bf16_f32 v19, v130, v131\n"
  "v_mfma_f32_16x16x32_bf16 v[52:55], a[112:115], a[136:139], v[52:55]\n"
  "v_cvt_pk_bf16_f32 v48, v48, v49\n"
  "v_cvt_pk_bf16_f32 v49, v50, v51\n"
  "v_mfma_f32_16x16x32_bf16 v[52:55], a[116:119], a[140:143], v[52:55]\n"
  "s_waitcnt vmcnt(11)\n"
  "ds_read_b64_tr_b16 a[68:69], v194 offset:12288\n"
  "v_cvt_pk_bf16_f32 v50, v144, v145\n"
  "v_mfma_f32_16x16x32_bf16 v[148:151], a[120:123], a[136:139], v[148:151]\n"
  "buffer_load_dwordx4 v201, s[44:47], s53 offen offset:3072 lds\n"
  "v_mfma_f32_16x16x32_bf16 v[148:151], a[124:127], a[140:143], v[148:151]\n"
  "ds_read_b64_tr_b16 a[70:71], v194 offset:14336\n"
  "v_cvt_pk_bf16_f32 v51, v146, v147\n"
  "v_mfma_f32_16x16x32_bf16 v[24:27], a[96:99], a[144:147], v[24:27]\n"
  "s_add_u32 m0, s51, 4096\n"
  "ds_read_b64_tr_b16 a[76:77], v195 offset:12288\n"
  "v_mfma_f32_16x16x32_bf16 v[24:27], a[100:103], a[148:151], v[24:27]\n"
  "s_add_u32 s54, s55, 294912\n"
  "ds_read_b64_tr_b16 a[78:79], v195 offset:14336\n"
  "v_mfma_f32_16x16x32_bf16 v[136:139], a[104:107], a[144:147], v[136:139]\n"
  "buffer_load_dwordx4 v198, s[44:47], s54 offen offset:0 lds\n"
  "v_mfma_f32_16x16x32_bf16 v[136:139], a[108:111], a[148:151], v[136:139]\n"
  "ds_read_b64_tr_b16 a[84:85], v196 offset:12288\n"
  "v_cvt_pk_bf16_f32 v20, v20, v21\n"
  "v_mfma_f32_16x16x32_bf16 v[56:59], a[112:115], a[144:147], v[56:59]\n"
  "ds_read_b64_tr_b16 a[86:87], v196 offset:14336\n"
  "v_cvt_pk_bf16_f32 v21, v22, v23\n"
  "v_mfma_f32_16x16x32_bf16 v[56:59], a[116:119], a[148:151], v[56:59]\n"
  "ds_read_b64_tr_b16 a[92:93], v197 offset:12288\n"
  "v_cvt_pk_bf16_f32 v22, v132, v133\n"
  "v_mfma_f32_16x16x32_bf16 v[152:155], a[120:123], a[144:147], v[152:155]\n"
  "buffer_load_dwordx4 v199, s[44:47], s54 offen offset:1024 lds\n"
  "v_mfma_f32_16x16x32_bf16 v[152:155], a[124:127], a[148:151], v[152:155]\n"
  "ds_read_b64_tr_b16 a[94:95], v197 offset:14336\n"
  "v_cvt_pk_bf16_f32 v23, v134, v135\n"
  "v_mfma_f32_16x16x32_bf16 v[28:31], a[96:99], a[152:155], v[28:31]\n"
  "v_cvt_pk_bf16_f32 v52, v52, v53\n"
  "v_cvt_pk_bf16_f32 v53, v54, v55\n"
  "v_mfma_f32_16x16x32_bf16 v[28:31], a[100:103], a[156:159], v[28:31]\n"
  "v_cvt_pk_bf16_f32 v54, v148, v149\n"
  "v_cvt_pk_bf16_f32 v55, v150, v151\n"
  "v_mfma_f32_16x16x32_bf16 v[140:143], a[104:107], a[152:155], v[140:143]\n"
  "buffer_load_dwordx4 v200, s[44:47], s54 offen offset:2048 lds\n"
  "v_mfma_f32_16x16x32_bf16 v[140:143], a[108:111], a[156:159], v[140:143]\n"
  "v_cvt_pk_bf16_f32 v27, v26, v27\n"
  "v_cvt_pk_bf16_f32 v26, v24, v25\n"
  "v_mfma_f32_16x16x32_bf16 v[60:63], a[112:115], a[152:155], v[60:63]\n"
  "v_cvt_pk_bf16_f32 v24, v136, v137\n"
  "v_cvt_pk_bf16_f32 v25, v138, v139\n"
  "v_mfma_f32_16x16x32_bf16 v[60:63], a[116:119], a[156:159], v[60:63]\n"
  "v_cvt_pk_bf16_f32 v59, v58, v59\n"
  "v_cvt_pk_bf16_f32 v58, v56, v57\n"
  "v_mfma_f32_16x16x32_bf16 v[156:159], a[120:123], a[152:155], v[156:159]\n"
  "buffer_load_dwordx4 v201, s[44:47], s54 offen offset:3072 lds\n"
  "s_mov_b32 m0, s63\n"
  "v_mfma_f32_16x16x32_bf16 v[156:159], a[124:127], a[156:159], v[156:159]\n"
  "s_waitcnt lgkmcnt(1)\n"
  "buffer_load_dwordx4 v228, s[24:27], s61 offen offset:0 lds\n"
  "buffer_load_dwordx4 v229, s[24:27], s61 offen offset:1024 lds\n"
  "buffer_load_dwordx4 v230, s[24:27], s61 offen offset:2048 lds\n"
  "buffer_load_dwordx4 v231, s[24:27], s61 offen offset:3072 lds\n"
  "v_mfma_f32_16x16x32_bf16 v[80:83], a[64:67], a[128:131], 0\n"
  "s_waitcnt vmcnt(16)\n"
  "s_add_u32 m0, s51, 8192\n"
  "ds_read_b64_tr_b16 a[96:97], v194 offset:16384\n"
  "v_mfma_f32_16x16x32_bf16 v[80:83], a[68:71], a[132:135], v[80:83]\n"
  "s_add_u32 s53, s55, 0\n"
  "ds_read_b64_tr_b16 a[98:99], v194 offset:18432\n"
  "v_mfma_f32_16x16x32_bf16 v[160:163], a[72:75], a[128:131], 0\n"
  "buffer_load_dwordx4 v198, s[44:47], s53 offen offset:0 lds\n"
  "v_mfma_f32_16x16x32_bf16 v[160:163], a[76:79], a[132:135], v[160:163]\n"
  "ds_read_b64_tr_b16 a[104:105], v195 offset:16384\n"
  "v_cvt_pk_bf16_f32 v56, v152, v153\n"
  "v_mfma_f32_16x16x32_bf16 v[112:115], a[80:83], a[128:131], 0\n"
  "ds_read_b64_tr_b16 a[106:107], v195 offset:18432\n"
  "v_cvt_pk_bf16_f32 v57, v154, v155\n"
  "v_mfma_f32_16x16x32_bf16 v[112:115], a[84:87], a[132:135], v[112:115]\n"
  "ds_read_b64_tr_b16 a[112:113], v196 offset:16384\n"
  "v_cvt_pk_bf16_f32 v31, v30, v31\n"
  "v_mfma_f32_16x16x32_bf16 v[176:179], a[88:91], a[128:131], 0\n"
  "s_waitcnt lgkmcnt(5)\n"
  "buffer_load_dwordx4 v199, s[44:47], s53 offen offset:1024 lds\n"
  "v_mfma_f32_16x16x32_bf16 v[176:179], a[92:95], a[132:135], v[176:179]\n"
  "ds_read_b64_tr_b16 a[114:115], v196 offset:18432\n"
  "v_cvt_pk_bf16_f32 v30, v28, v29\n"
  "v_mfma_f32_16x16x32_bf16 v[84:87], a[64:67], a[136:139], 0\n"
  "ds_read_b64_tr_b16 a[120:121], v197 offset:16384\n"
  "v_cvt_pk_bf16_f32 v28, v140, v141\n"
  "v_mfma_f32_16x16x32_bf16 v[84:87], a[68:71], a[140:143], v[84:87]\n"
  "ds_read_b64_tr_b16 a[122:123], v197 offset:18432\n"
  "v_cvt_pk_bf16_f32 v29, v142, v143\n"
  "v_mfma_f32_16x16x32_bf16 v[164:167], a[72:75], a[136:139], 0\n"
  "L_dry3_3:\n"
  "L_pad15:\n"
  "s_cbranch_execz L_dryr3\n"
  "buffer_load_dwordx4 v200, s[44:47], s53 offen offset:2048 lds\n"
  "v_mfma_f32_16x16x32_bf16 v[164:167], a[76:79], a[140:143], v[164:167]\n"
  "v_cvt_pk_bf16_f32 v63, v62, v63\n"
  "v_cvt_pk_bf16_f32 v62, v60, v61\n"
  "v_mfma_f32_16x16x32_bf16 v[116:119], a[80:83], a[136:139], 0\n"
  "v_cvt_pk_bf16_f32 v60, v156, v157\n"
  "v_cvt_pk_bf16_f32 v61, v158, v159\n"
  "v_mfma_f32_16x16x32_bf16 v[116:119], a[84:87], a[140:143], v[116:119]\n"
  "s_waitcnt vmcnt(15)\n"
  "ds_read_b64_tr_b16 a[100:101], v194 offset:20480\n"
  "v_accvgpr_write_b32 a192, v16\n"
  "v_mfma_f32_16x16x32_bf16 v[180:183], a[88:91], a[136:139], 0\n"
  "buffer_load_dwordx4 v201, s[44:47], s53 offen offset:3072 lds\n"
  "v_mfma_f32_16x16x32_bf16 v[180:183], a[92:95], a[140:143], v[180:183]\n"
  "ds_read_b64_tr_b16 a[102:103], v194 offset:22528\n"
  "v_accvgpr_write_b32 a193, v17\n"
  "v_mfma_f32_16x16x32_bf16 v[88:91], a[64:67], a[144:147], 0\n"
  "s_add_u32 m0, s51, 12288\n"
  "ds_read_b64_tr_b16 a[108:109], v195 offset:20480\n"
  "v_mfma_f32_16x16x32_bf16 v[88:91], a[68:71], a[148:151], v[88:91]\n"
  "s_add_u32 s54, s55, 32768\n"
  "ds_read_b64_tr_b16 a[110:111], v195 offset:22528\n"
  "v_mfma_f32_16x16x32_bf16 v[168:171], a[72:75], a[144:147], 0\n"
  "buffer_load_dwordx4 v198, s[44:47], s54 offen offset:0 lds\n"
  "v_mfma_f32_16x16x32_bf16 v[168:171], a[76:79], a[148:151], v[168:171]\n"
  "ds_read_b64_tr_b16 a[116:117], v196 offset:20480\n"
  "v_accvgpr_write_b32 a194, v18\n"
  "v_mfma_f32_16x16x32_bf16 v[120:123], a[80:83], a[144:147], 0\n"
  "ds_read_b64_tr_b16 a[118:119], v196 offset:22528\n"
  "v_accvgpr_write_b32 a195, v19\n"
  "v_mfma_f32_16x16x32_bf16 v[120:123], a[84:87], a[148:151], v[120:123]\n"
  "ds_read_b64_tr_b16 a[124:125], v197 offset:20480\n"
  "v_accvgpr_write_b32 a200, v24\n"
  "v_mfma_f32_16x16x32_bf16 v[184:187], a[88:91], a[144:147], 0\n"
  "buffer_load_dwordx4 v199, s[44:47], s54 offen offset:1024 lds\n"
  "v_mfma_f32_16x16x32_bf16 v[184:187], a[92:95], a[148:151], v[184:187]\n"
  "ds_read_b64_tr_b16 a[126:127], v197 offset:22528\n"
  "v_accvgpr_write_b32 a201, v25\n"
  "v_mfma_f32_16x16x32_bf16 v[92:95], a[64:67], a[152:155], 0\n"
  "v_accvgpr_write_b32 a202, v26\n"
  "v_accvgpr_write_b32 a203, v27\n"
  "v_mfma_f32_16x16x32_bf16 v[92:95], a[68:71], a[156:159], v[92:95]\n"
  "v_accvgpr_write_b32 a208, v48\n"
  "v_accvgpr_write_b32 a209, v49\n"
  "v_mfma_f32_16x16x32_bf16 v[172:175], a[72:75], a[152:155], 0\n"
  "buffer_load_dwordx4 v200, s[44:47], s54 offen offset:2048 lds\n"
  "v_mfma_f32_16x16x32_bf16 v[172:175], a[76:79], a[156:159], v[172:175]\n"
  "v_accvgpr_write_b32 a210, v50\n"
  "v_accvgpr_write_b32 a211, v51\n"
  "v_mfma_f32_16x16x32_bf16 v[124:127], a[80:83], a[152:155], 0\n"
  "v_accvgpr_write_b32 a216, v56\n"
  "v_accvgpr_write_b32 a217, v57\n"
  "v_mfma_f32_16x16x32_bf16 v[124:127], a[84:87], a[156:159], v[124:127]\n"
  "v_accvgpr_write_b32 a218, v58\n"
  "v_accvgpr_write_b32 a219, v59\n"
  "v_mfma_f32_16x16x32_bf16 v[188:191], a[88:91], a[152:155], 0\n"
  "buffer_load_dwordx4 v201, s[44:47], s54 offen offset:3072 lds\n"
  "v_mfma_f32_16x16x32_bf16 v[188:191], a[92:95], a[156:159], v[188:191]\n"
  "s_waitcnt lgkmcnt(1)\n"
  "v_mfma_f32_16x16x32_bf16 v[80:83], a[96:99], a[0:3], v[80:83]\n"
  "s_waitcnt vmcnt(16)\n"
  "s_add_u32 m0, s63, 16384\n"
  "ds_read_b64_tr_b16 a[64:65], v194 offset:0\n"
  "v_mfma_f32_16x16x32_bf16 v[80:83], a[100:103], a[4:7], v[80:83]\n"
  "buffer_load_dwordx4 v232, s[28:31], s61 offen offset:0 lds\n"
  "v_mfma_f32_16x16x32_bf16 v[160:163], a[104:107], a[0:3], v[160:163]\n"
  "s_mov_b64 s[20:21], s[24:25]\n"
  "ds_read_b64_tr_b16 a[66:67], v194 offset:2048\n"
  "v_mfma_f32_16x16x32_bf16 v[160:163], a[108:111], a[4:7], v[160:163]\n"
  "s_mov_b64 s[22:23], s[28:29]\n"
  "ds_read_b64_tr_b16 a[72:73], v195 offset:0\n"
  "v_mfma_f32_16x16x32_bf16 v[112:115], a[112:115], a[0:3], v[112:115]\n"
  "s_add_u32 s44, s42, s13\n"
  "s_addc_u32 s45, s43, 0\n"
  "buffer_load_dwordx4 v233, s[28:31], s61 offen offset:1024 lds\n"
  "v_mfma_f32_16x16x32_bf16 v[112:115], a[116:119], a[4:7], v[112:115]\n"
  "s_add_u32 s46, s44, 0x8000\n"
  "s_addc_u32 s47, s45, 0\n"
  "ds_read_b64_tr_b16 a[74:75], v195 offset:2048\n"
  "v_mfma_f32_16x16x32_bf16 v[176:179], a[120:123], a[0:3], v[176:179]\n"
  "s_waitcnt lgkmcnt(4)\n"
  "s_sub_u32 s51, s51, s63\n"
  "ds_read_b64_tr_b16 a[80:81], v196 offset:0\n"
  "v_mfma_f32_16x16x32_bf16 v[176:179], a[124:127], a[4:7], v[176:179]\n"
  "buffer_load_dwordx4 v234, s[28:31], s61 offen offset:2048 lds\n"
  "v_mfma_f32_16x16x32_bf16 v[84:87], a[96:99], a[8:11], v[84:87]\n"
  "ds_read_b64_tr_b16 a[82:83], v196 offset:2048\n"
  "v_mfma_f32_16x16x32_bf16 v[84:87], a[100:103], a[12:15], v[84:87]\n"
  "ds_read_b64_tr_b16 a[88:89], v197 offset:0\n"
  "v_mfma_f32_16x16x32_bf16 v[164:167], a[104:107], a[8:11], v[164:167]\n"
  "buffer_load_dwordx4 v235, s[28:31], s61 offen offset:3072 lds\n"
  "v_mfma_f32_16x16x32_bf16 v[164:167], a[108:111], a[12:15], v[164:167]\n"
  "ds_read_b64_tr_b16 a[90:91], v197 offset:2048\n"
  "v_mfma_f32_16x16x32_bf16 v[116:119], a[112:115], a[8:11], v[116:119]\n"
  "s_add_u32 s61, s61, 0x4000\n"
  "s_waitcnt vmcnt(12)\n"
  "s_barrier\n"
  "v_mfma_f32_16x16x32_bf16 v[116:119], a[116:119], a[12:15], v[116:119]\n"
  "ds_read_b128 a[128:131], v216 offset:0\n"
  "v_mfma_f32_16x16x32_bf16 v[180:183], a[120:123], a[8:11], v[180:183]\n"
  "ds_read_b128 a[132:135], v217 offset:0\n"
  "buffer_store_dword v250, v249, s[68:71], 0 offen sc1\n"
  "v_mfma_f32_16x16x32_bf16 v[180:183], a[124:127], a[12:15], v[180:183]\n"
  "ds_read_b128 a[136:139], v218 offset:0\n"
  "v_mfma_f32_16x16x32_bf16 v[88:91], a[96:99], a[16:19], v[88:91]\n"
  "ds_read_b128 a[140:143], v219 offset:0\n"
  "v_mfma_f32_16x16x32_bf16 v[88:91], a[100:103], a[20:23], v[88:91]\n"
  "ds_read_b128 a[144:147], v216 offset:4096\n"
  "v_mfma_f32_16x16x32_bf16 v[168:171], a[104:107], a[16:19], v[168:171]\n"
  "s_add_u32 m0, s63, 32768\n"
  "v_mfma_f32_16x16x32_bf16 v[168:171], a[108:111], a[20:23], v[168:171]\n"
  "s_waitcnt lgkmcnt(4)\n"
  "buffer_load_dwordx4 v228, s[24:27], s61 offen offset:0 lds\n"
  "v_mfma_f32_16x16x32_bf16 v[240:243], a[128:131], v[0:3], 0\n"
  "s_waitcnt lgkmcnt(3)\n"
  "v_mfma_f32_16x16x32_bf16 v[240:243], a[132:135], v[32:35], v[240:243]\n"
  "s_waitcnt lgkmcnt(2)\n"
  "ds_read_b64_tr_b16 a[68:69], v194 offset:4096\n"
  "v_mfma_f32_16x16x32_bf16 v[240:243], a[136:139], v[64:67], v[240:243]\n"
  "s_waitcnt lgkmcnt(2)\n"
  "ds_read_b64_tr_b16 a[70:71], v194 offset:6144\n"
  "v_mfma_f32_16x16x32_bf16 v[240:243], a[140:143], v[96:99], v[240:243]\n"
  "buffer_load_dwordx4 v229, s[24:27], s61 offen offset:1024 lds\n"
  "v_mfma_f32_16x16x32_bf16 v[244:247], a[128:131], v[4:7], 0\n"
  "ds_read_b64_tr_b16 a[76:77], v195 offset:4096\n"
  "v_mfma_f32_16x16x32_bf16 v[244:247], a[132:135], v[36:39], v[244:247]\n"
  "ds_read_b64_tr_b16 a[78:79], v195 offset:6144\n"
  "v_mfma_f32_16x16x32_bf16 v[244:247], a[136:139], v[68:71], v[244:247]\n"
  "ds_read_b64_tr_b16 a[84:85], v196 offset:4096\n"
  "v_mfma_f32_16x16x32_bf16 v[244:247], a[140:143], v[100:103], v[244:247]\n"
  "buffer_load_dwordx4 v230, s[24:27], s61 offen offset:2048 lds\n"
  "v_mfma_f32_16x16x32_bf16 v[248:251], a[128:131], v[8:11], 0\n"
  "ds_read_b64_tr_b16 a[86:87], v196 offset:6144\n"
  "v_mfma_f32_16x16x32_bf16 v[248:251], a[132:135], v[40:43], v[248:251]\n"
  "ds_read_b64_tr_b16 a[92:93], v197 offset:4096\n"
  "v_mfma_f32_16x16x32_bf16 v[248:251], a[136:139], v[72:75], v[248:251]\n"
  "ds_read_b64_tr_b16 a[94:95], v197 offset:6144\n"
  "v_mfma_f32_16x16x32_bf16 v[248:251], a[140:143], v[104:107], v[248:251]\n"
  "buffer_load_dwordx4 v231, s[24:27], s61 offen offset:3072 lds\n"
  "v_mfma_f32_16x16x32_bf16 v[252:255], a[128:131], v[12:15], 0\n"
  "v_mfma_f32_16x16x32_bf16 v[252:255], a[132:135], v[44:47], v[252:255]\n"
  "v_mfma_f32_16x16x32_bf16 v[252:255], a[136:139], v[76:79], v[252:255]\n"
  "s_add_u32 m0, s63, 49152\n"
  "v_mfma_f32_16x16x32_bf16 v[252:255], a[140:143], v[108:111], v[252:255]\n"
  "buffer_load_dwordx4 v232, s[28:31], s61 offen offset:0 lds\n"
  "v_mfma_f32_16x16x32_bf16 v[120:123], a[112:115], a[16:19], v[120:123]\n"
  "v_max3_f32 v240, v240, v241, v242\n"
  "v_max_f32 v240, v240, v243\n"
  "v_max3_f32 v244, v244, v245, v246\n"
  "v_mfma_f32_16x16x32_bf16 v[120:123], a[116:119], a[20:23], v[120:123]\n"
  "v_max_f32 v244, v244, v247\n"
  "v_max3_f32 v248, v248, v249, v250\n"
  "v_max_f32 v248, v248, v251\n"
  "v_mfma_f32_16x16x32_bf16 v[184:187], a[120:123], a[16:19], v[184:187]\n"
  "v_max3_f32 v252, v252, v253, v254\n"
  "v_max_f32 v252, v252, v255\n"
  "v_mov_b32 v241, v240\n"
  "v_mfma_f32_16x16x32_bf16 v[184:187], a[124:127], a[20:23], v[184:187]\n"
  "buffer_load_dwordx4 v233, s[28:31], s61 offen offset:1024 lds\n"
  "v_mov_b32 v245, v244\n"
  "v_mov_b32 v249, v248\n"
  "v_mov_b32 v253, v252\n"
  "v_mfma_f32_16x16x32_bf16 v[92:95], a[96:99], a[24:27], v[92:95]\n"
  "v_permlane16_swap_b32 v240, v241\n"
  "v_permlane16_swap_b32 v244, v245\n"
  "v_permlane16_swap_b32 v248, v249\n"
  "v_mfma_f32_16x16x32_bf16 v[92:95], a[100:103], a[28:31], v[92:95]\n"
  "v_permlane16_swap_b32 v252, v253\n"
  "v_max_f32 v240, v240, v241\n"
  "v_max_f32 v244, v244, v245\n"
  "v_mfma_f32_16x16x32_bf16 v[172:175], a[104:107], a[24:27], v[172:175]\n"
  "v_max_f32 v248, v248, v249\n"
  "v_max_f32 v252, v252, v253\n"
  "v_mov_b32 v241, v240\n"
  "v_mfma_f32_16x16x32_bf16 v[172:175], a[108:111], a[28:31], v[172:175]\n"
  "buffer_load_dwordx4 v234, s[28:31], s61 offen offset:2048 lds\n"
  "v_mov_b32 v245, v244\n"
  "v_mov_b32 v249, v248\n"
  "v_mov_b32 v253, v252\n"
  "v_mfma_f32_16x16x32_bf16 v[124:127], a[112:115], a[24:27], v[124:127]\n"
  "v_permlane32_swap_b32 v240, v241\n"
  "v_permlane32_swap_b32 v244, v245\n"
  "v_permlane32_swap_b32 v248, v249\n"
  "v_mfma_f32_16x16x32_bf16 v[124:127], a[116:119], a[28:31], v[124:127]\n"
  "v_permlane32_swap_b32 v252, v253\n"
  "v_max_f32 v240, v240, v241\n"
  "v_max_f32 v244, v244, v245\n"
  "v_mfma_f32_16x16x32_bf16 v[188:191], a[120:123], a[24:27], v[188:191]\n"
  "v_max_f32 v248, v248, v249\n"
  "v_max_f32 v252, v252, v253\n"
  "v_sub_f32 v128, 0xc2200000, v240\n"
  "v_mfma_f32_16x16x32_bf16 v[188:191], a[124:127], a[28:31], v[188:191]\n"
  "buffer_load_dwordx4 v235, s[28:31], s61 offen offset:3072 lds\n"
  "v_sub_f32 v129, 0xc2200000, v240\n"
  "v_sub_f32 v130, 0xc2200000, v240\n"
  "v_sub_f32 v131, 0xc2200000, v240\n"
  "v_mfma_f32_16x16x32_bf16 v[80:83], a[64:67], a[160:163], v[80:83]\n"
  "s_waitcnt vmcnt(17)\n"
  "ds_read_b64_tr_b16 a[96:97], v194 offset:8192\n"
  "v_sub_f32 v132, 0xc2200000, v244\n"
  "v_sub_f32 v133, 0xc2200000, v244\n"
  "v_sub_f32 v134, 0xc2200000, v244\n"
  "s_waitcnt lgkmcnt(1)\n"
  "v_mfma_f32_16x16x32_bf16 v[80:83], a[68:71], a[164:167], v[80:83]\n"
  "ds_read_b64_tr_b16 a[98:99], v194 offset:10240\n"
  "v_sub_f32 v135, 0xc2200000, v244\n"
  "v_sub_f32 v136, 0xc2200000, v248\n"
  "v_sub_f32 v137, 0xc2200000, v248\n"
  "v_mfma_f32_16x16x32_bf16 v[160:163], a[72:75], a[160:163], v[160:163]\n"
  "ds_read_b64_tr_b16 a[104:105], v195 offset:8192\n"
  "v_sub_f32 v138, 0xc2200000, v248\n"
  "v_sub_f32 v139, 0xc2200000, v248\n"
  "v_sub_f32 v140, 0xc2200000, v252\n"
  "v_mfma_f32_16x16x32_bf16 v[160:163], a[76:79], a[164:167], v[160:163]\n"
  "ds_read_b64_tr_b16 a[106:107], v195 offset:10240\n"
  "v_sub_f32 v141, 0xc2200000, v252\n"
  "v_sub_f32 v142, 0xc2200000, v252\n"
  "v_sub_f32 v143, 0xc2200000, v252\n"
  "v_mfma_f32_16x16x32_bf16 v[112:115], a[80:83], a[160:163], v[112:115]\n"
  "ds_read_b64_tr_b16 a[112:113], v196 offset:8192\n"
  "v_mfma_f32_16x16x32_bf16 v[112:115], a[84:87], a[164:167], v[112:115]\n"
  "ds_read_b64_tr_b16 a[114:115], v196 offset:10240\n"
  "v_mfma_f32_16x16x32_bf16 v[176:179], a[88:91], a[160:163], v[176:179]\n"
  "ds_read_b64_tr_b16 a[120:121], v197 offset:8192\n"
  "v_mfma_f32_16x16x32_bf16 v[176:179], a[92:95], a[164:167], v[176:179]\n"
  "ds_read_b64_tr_b16 a[122:123], v197 offset:10240\n"
  "v_mfma_f32_16x16x32_bf16 v[240:243], a[128:131], v[0:3], v[128:131]\n"
  "v_accvgpr_write_b32 a196, v20\n"
  "v_accvgpr_write_b32 a197, v21\n"
  "v_mfma_f32_16x16x32_bf16 v[240:243], a[132:135], v[32:35], v[240:243]\n"
  "ds_read_b128 a[148:151], v217 offset:4096\n"
  "v_accvgpr_write_b32 a198, v22\n"
  "v_mfma_f32_16x16x32_bf16 v[240:243], a[136:139], v[64:67], v[240:243]\n"
  "v_accvgpr_write_b32 a199, v23\n"
  "v_accvgpr_write_b32 a204, v28\n"
  "v_mfma_f32_16x16x32_bf16 v[240:243], a[140:143], v[96:99], v[240:243]\n"
  "v_accvgpr_write_b32 a205, v29\n"
  "v_accvgpr_write_b32 a206, v30\n"
  "v_mfma_f32_16x16x32_bf16 v[84:87], a[64:67], a[168:171], v[84:87]\n"
  "v_accvgpr_write_b32 a207, v31\n"
  "v_accvgpr_write_b32 a212, v52\n"
  "v_mfma_f32_16x16x32_bf16 v[84:87], a[68:71], a[172:175], v[84:87]\n"
  "v_accvgpr_write_b32 a213, v53\n"
  "v_accvgpr_write_b32 a214, v54\n"
  "v_mfma_f32_16x16x32_bf16 v[164:167], a[72:75], a[168:171], v[164:167]\n"
  "v_accvgpr_write_b32 a215, v55\n"
  "v_accvgpr_write_b32 a220, v60\n"
  "v_mfma_f32_16x16x32_bf16 v[164:167], a[76:79], a[172:175], v[164:167]\n"
  "v_accvgpr_write_b32 a221, v61\n"
  "v_accvgpr_write_b32 a222, v62\n"
  "v_mfma_f32_16x16x32_bf16 v[244:247], a[128:131], v[4:7], v[132:135]\n"
  "v_exp_f32 v240, v240\n"
  "v_mfma_f32_16x16x32_bf16 v[244:247], a[132:135], v[36:39], v[244:247]\n"
  "ds_read_b128 a[152:155], v218 offset:4096\n"
  "v_accvgpr_write_b32 a223, v63\n"
  "v_mfma_f32_16x16x32_bf16 v[244:247], a[136:139], v[68:71], v[244:247]\n"
  "v_exp_f32 v241, v241\n"
  "v_mfma_f32_16x16x32_bf16 v[244:247], a[140:143], v[100:103], v[244:247]\n"
  "v_cvt_pk_bf16_f32 v16, v240, v241\n"
  "v_and_b32 v156, 0xf0, v192\n"
  "v_mfma_f32_16x16x32_bf16 v[116:119], a[80:83], a[168:171], v[116:119]\n"
  "v_exp_f32 v242, v242\n"
  "v_mfma_f32_16x16x32_bf16 v[116:119], a[84:87], a[172:175], v[116:119]\n"
  "v_cmp_gt_u32 vcc, 32, v156\n"
  "v_mov_b32 v157, 0x3f803f80\n"
  "v_mfma_f32_16x16x32_bf16 v[180:183], a[88:91], a[168:171], v[180:183]\n"
  "v_exp_f32 v243, v243\n"
  "v_mfma_f32_16x16x32_bf16 v[180:183], a[92:95], a[172:175], v[180:183]\n"
  "v_cvt_pk_bf16_f32 v17, v242, v243\n"
  "v_mov_b32 v148, 0x44444444\n"
  "v_mfma_f32_16x16x32_bf16 v[248:251], a[128:131], v[8:11], v[136:139]\n"
  "v_exp_f32 v244, v244\n"
  "v_mfma_f32_16x16x32_bf16 v[248:251], a[132:135], v[40:43], v[248:251]\n"
  "ds_read_b128 a[156:159], v219 offset:4096\n"
  "v_cndmask_b32 v144, 0, v157, vcc\n"
  "v_mfma_f32_16x16x32_bf16 v[248:251], a[136:139], v[72:75], v[248:251]\n"
  "v_exp_f32 v245, v245\n"
  "v_mfma_f32_16x16x32_bf16 v[248:251], a[140:143], v[104:107], v[248:251]\n"
  "s_waitcnt vmcnt(13)\n"
  "ds_read_b64_tr_b16 a[100:101], v194 offset:12288\n"
  "v_cvt_pk_bf16_f32 v24, v244, v245\n"
  "v_mfma_f32_16x16x32_bf16 v[88:91], a[64:67], a[176:179], v[88:91]\n"
  "v_exp_f32 v246, v246\n"
  "v_mfma_f32_16x16x32_bf16 v[88:91], a[68:71], a[180:183], v[88:91]\n"
  "ds_read_b64_tr_b16 a[102:103], v194 offset:14336\n"
  "v_cndmask_b32 v145, 0, v157, vcc\n"
  "v_mfma_f32_16x16x32_bf16 v[168:171], a[72:75], a[176:179], v[168:171]\n"
  "v_exp_f32 v247, v247\n"
  "v_mfma_f32_16x16x32_bf16 v[168:171], a[76:79], a[180:183], v[168:171]\n"
  "ds_read_b64_tr_b16 a[108:109], v195 offset:12288\n"
  "v_cvt_pk_bf16_f32 v25, v246, v247\n"
  "v_mfma_f32_16x16x32_bf16 v[252:255], a[128:131], v[12:15], v[140:143]\n"
  "v_exp_f32 v248, v248\n"
  "v_mfma_f32_16x16x32_bf16 v[252:255], a[132:135], v[44:47], v[252:255]\n"
  "ds_read_b128 a[128:131], v216 offset:8192\n"
  "v_cndmask_b32 v146, 0, v157, vcc\n"
  "v_mfma_f32_16x16x32_bf16 v[252:255], a[136:139], v[76:79], v[252:255]\n"
  "v_exp_f32 v249, v249\n"
  "v_mfma_f32_16x16x32_bf16 v[252:255], a[140:143], v[108:111], v[252:255]\n"
  "ds_read_b64_tr_b16 a[110:111], v195 offset:14336\n"
  "v_cvt_pk_bf16_f32 v48, v248, v249\n"
  "v_mfma_f32_16x16x32_bf16 v[120:123], a[80:83], a[176:179], v[120:123]\n"
  "v_exp_f32 v250, v250\n"
  "v_mfma_f32_16x16x32_bf16 v[120:123], a[84:87], a[180:183], v[120:123]\n"
  "ds_read_b64_tr_b16 a[116:117], v196 offset:12288\n"
  "v_cndmask_b32 v147, 0, v157, vcc\n"
  "v_mfma_f32_16x16x32_bf16 v[184:187], a[88:91], a[176:179], v[184:187]\n"
  "v_exp_f32 v251, v251\n"
  "v_mfma_f32_16x16x32_bf16 v[184:187], a[92:95], a[180:183], v[184:187]\n"
  "ds_read_b64_tr_b16 a[118:119], v196 offset:14336\n"
  "v_cvt_pk_bf16_f32 v49, v250, v251\n"
  "v_mfma_f32_16x16x32_bf16 v[240:243], a[144:147], v[0:3], v[128:131]\n"
  "s_waitcnt lgkmcnt(4)\n"
  "v_exp_f32 v252, v252\n"
  "v_mfma_f32_16x16x32_bf16 v[240:243], a[148:151], v[32:35], v[240:243]\n"
  "ds_read_b128 a[132:135], v217 offset:8192\n"
  "v_cmp_eq_u32 vcc, 16, v156\n"
  "v_mfma_f32_16x16x32_bf16 v[240:243], a[152:155], v[64:67], v[240:243]\n"
  "v_exp_f32 v253, v253\n"
  "v_mfma_f32_16x16x32_bf16 v[240:243], a[156:159], v[96:99], v[240:243]\n"
  "ds_read_b64_tr_b16 a[124:125], v197 offset:12288\n"
  "v_cvt_pk_bf16_f32 v56, v252, v253\n"
  "v_mfma_f32_16x16x32_bf16 v[92:95], a[64:67], a[184:187], v[92:95]\n"
  "v_exp_f32 v254, v254\n"
  "v_mfma_f32_16x16x32_bf16 v[92:95], a[68:71], a[188:191], v[92:95]\n"
  "ds_read_b64_tr_b16 a[126:127], v197 offset:14336\n"
  "v_mov_b32 v157, 0xeeeeeeee\n"
  "v_mfma_f32_16x16x32_bf16 v[172:175], a[72:75], a[184:187], v[172:175]\n"
  "v_exp_f32 v255, v255\n"
  "v_mfma_f32_16x16x32_bf16 v[172:175], a[76:79], a[188:191], v[172:175]\n"
  "v_cvt_pk_bf16_f32 v57, v254, v255\n"
  "v_cndmask_b32 v148, v148, v157, vcc\n"
  "v_mfma_f32_16x16x32_bf16 v[244:247], a[144:147], v[4:7], v[132:135]\n"
  "v_exp_f32 v240, v240\n"
  "v_mfma_f32_16x16x32_bf16 v[244:247], a[148:151], v[36:39], v[244:247]\n"
  "ds_read_b128 a[136:139], v218 offset:8192\n"
  "v_mfma_f32_16x16x32_bf16 v[244:247], a[152:155], v[68:71], v[244:247]\n"
  "v_exp_f32 v241, v241\n"
  "v_mfma_f32_16x16x32_bf16 v[244:247], a[156:159], v[100:103], v[244:247]\n"
  "v_cvt_pk_bf16_f32 v18, v240, v241\n"
  "v_mfma_f32_16x16x32_bf16 v[124:127], a[80:83], a[184:187], v[124:127]\n"
  "v_exp_f32 v242, v242\n"
  "v_mfma_f32_16x16x32_bf16 v[124:127], a[84:87], a[188:191], v[124:127]\n"
  "v_mfma_f32_16x16x32_bf16 v[188:191], a[88:91], a[184:187], v[188:191]\n"
  "v_exp_f32 v243, v243\n"
  "v_mfma_f32_16x16x32_bf16 v[188:191], a[92:95], a[188:191], v[188:191]\n"
  "v_cvt_pk_bf16_f32 v19, v242, v243\n"
  "v_mfma_f32_16x16x32_bf16 v[248:251], a[144:147], v[8:11], v[136:139]\n"
  "v_exp_f32 v244, v244\n"
  "v_mfma_f32_16x16x32_bf16 v[248:251], a[148:151], v[40:43], v[248:251]\n"
  "ds_read_b128 a[140:143], v219 offset:8192\n"
  "v_mfma_f32_16x16x32_bf16 v[248:251], a[152:155], v[72:75], v[248:251]\n"
  "v_exp_f32 v245, v245\n"
  "v_mfma_f32_16x16x32_bf16 v[248:251], a[156:159], v[104:107], v[248:251]\n"
  "v_cvt_pk_bf16_f32 v26, v244, v245\n"
  "v_mfma_f32_16x16x32_bf16 v[80:83], a[96:99], a[32:35], v[80:83]\n"
  "v_exp_f32 v246, v246\n"
  "v_mfma_f32_16x16x32_bf16 v[80:83], a[100:103], a[36:39], v[80:83]\n"
  "v_mfma_f32_16x16x32_bf16 v[160:163], a[104:107], a[32:35], v[160:163]\n"
  "s_waitcnt lgkmcnt(1)\n"
  "v_exp_f32 v247, v247\n"
  "v_mfma_f32_16x16x32_bf16 v[160:163], a[108:111], a[36:39], v[160:163]\n"
  "v_cvt_pk_bf16_f32 v27, v246, v247\n"
  "v_mfma_f32_16x16x32_bf16 v[252:255], a[144:147], v[12:15], v[140:143]\n"
  "v_exp_f32 v248, v248\n"
  "v_mfma_f32_16x16x32_bf16 v[252:255], a[148:151], v[44:47], v[252:255]\n"
  "ds_read_b128 a[144:147], v216 offset:12288\n"
  "v_mfma_f32_16x16x32_bf16 v[252:255], a[152:155], v[76:79], v[252:255]\n"
  "v_exp_f32 v249, v249\n"
  "v_mfma_f32_16x16x32_bf16 v[252:255], a[156:159], v[108:111], v[252:255]\n"
  "v_cvt_pk_bf16_f32 v50, v248, v249\n"
  "v_mfma_f32_16x16x32_bf16 v[112:115], a[112:115], a[32:35], v[112:115]\n"
  "v_exp_f32 v250, v250\n"
  "v_mfma_f32_16x16x32_bf16 v[112:115], a[116:119], a[36:39], v[112:115]\n"
  "v_cvt_pk_bf16_f32 v80, v80, v81\n"
  "v_cvt_pk_bf16_f32 v81, v82, v83\n"
  "v_mfma_f32_16x16x32_bf16 v[176:179], a[120:123], a[32:35], v[176:179]\n"
  "v_exp_f32 v251, v251\n"
  "v_mfma_f32_16x16x32_bf16 v[176:179], a[124:127], a[36:39], v[176:179]\n"
  "v_cvt_pk_bf16_f32 v51, v250, v251\n"
  "v_cvt_pk_bf16_f32 v82, v160, v161\n"
  "v_mfma_f32_16x16x32_bf16 v[240:243], a[128:131], v[0:3], v[128:131]\n"
  "v_exp_f32 v252, v252\n"
  "v_mfma_f32_16x16x32_bf16 v[240:243], a[132:135], v[32:35], v[240:243]\n"
  "ds_read_b128 a[148:151], v217 offset:12288\n"
  "v_cvt_pk_bf16_f32 v83, v162, v163\n"
  "v_mfma_f32_16x16x32_bf16 v[240:243], a[136:139], v[64:67], v[240:243]\n"
  "s_waitcnt lgkmcnt(1)\n"
  "v_exp_f32 v253, v253\n"
  "v_mfma_f32_16x16x32_bf16 v[240:243], a[140:143], v[96:99], v[240:243]\n"
  "v_cvt_pk_bf16_f32 v58, v252, v253\n"
  "v_mfma_f32_16x16x32_bf16 v[84:87], a[96:99], a[40:43], v[84:87]\n"
  "v_exp_f32 v254, v254\n"
  "v_mfma_f32_16x16x32_bf16 v[84:87], a[100:103], a[44:47], v[84:87]\n"
  "v_cvt_pk_bf16_f32 v112, v112, v113\n"
  "v_cvt_pk_bf16_f32 v113, v114, v115\n"
  "v_mfma_f32_16x16x32_bf16 v[164:167], a[104:107], a[40:43], v[164:167]\n"
  "v_exp_f32 v255, v255\n"
  "v_mfma_f32_16x16x32_bf16 v[164:167], a[108:111], a[44:47], v[164:167]\n"
  "v_cvt_pk_bf16_f32 v59, v254, v255\n"
  "v_cvt_pk_bf16_f32 v114, v176, v177\n"
  "v_mfma_f32_16x16x32_bf16 v[244:247], a[128:131], v[4:7], v[132:135]\n"
  "s_add_u32 s61, s61, s9\n"
  "v_exp_f32 v240, v240\n"
  "v_mfma_f32_16x16x32_bf16 v[244:247], a[132:135], v[36:39], v[244:247]\n"
  "ds_read_b128 a[152:155], v218 offset:12288\n"
  "v_cvt_pk_bf16_f32 v115, v178, v179\n"
  "v_mfma_f32_16x16x32_bf16 v[244:247], a[136:139], v[68:71], v[244:247]\n"
  "s_and_b32 s61, s61, 0x7ffff\n"
  "v_exp_f32 v241, v241\n"
  "v_mfma_f32_16x16x32_bf16 v[244:247], a[140:143], v[100:103], v[244:247]\n"
  "v_cvt_pk_bf16_f32 v20, v240, v241\n"
  "v_mfma_f32_16x16x32_bf16 v[116:119], a[112:115], a[40:43], v[116:119]\n"
  "v_exp_f32 v242, v242\n"
  "v_mfma_f32_16x16x32_bf16 v[116:119], a[116:119], a[44:47], v[116:119]\n"
  "v_cvt_pk_bf16_f32 v84, v84, v85\n"
  "v_cvt_pk_bf16_f32 v85, v86, v87\n"
  "v_mfma_f32_16x16x32_bf16 v[180:183], a[120:123], a[40:43], v[180:183]\n"
  "v_exp_f32 v243, v243\n"
  "v_mfma_f32_16x16x32_bf16 v[180:183], a[124:127], a[44:47], v[180:183]\n"
  "v_cvt_pk_bf16_f32 v21, v242, v243\n"
  "v_cvt_pk_bf16_f32 v86, v164, v165\n"
  "v_mfma_f32_16x16x32_bf16 v[248:251], a[128:131], v[8:11], v[136:139]\n"
  "s_xor_b32 s52, s52, 0x10000\n"
  "v_exp_f32 v244, v244\n"
  "v_mfma_f32_16x16x32_bf16 v[248:251], a[132:135], v[40:43], v[248:251]\n"
  "ds_read_b128 a[156:159], v219 offset:12288\n"
  "v_cvt_pk_bf16_f32 v87, v166, v167\n"
  "v_mfma_f32_16x16x32_bf16 v[248:251], a[136:139], v[72:75], v[248:251]\n"
  "s_mov_b32 m0, s52\n"
  "v_exp_f32 v245, v245\n"
  "v_mfma_f32_16x16x32_bf16 v[248:251], a[140:143], v[104:107], v[248:251]\n"
  "v_cvt_pk_bf16_f32 v28, v244, v245\n"
  "v_mfma_f32_16x16x32_bf16 v[88:91], a[96:99], a[48:51], v[88:91]\n"
  "v_exp_f32 v246, v246\n"
  "v_mfma_f32_16x16x32_bf16 v[88:91], a[100:103], a[52:55], v[88:91]\n"
  "v_cvt_pk_bf16_f32 v116, v116, v117\n"
  "v_cvt_pk_bf16_f32 v117, v118, v119\n"
  "v_mfma_f32_16x16x32_bf16 v[168:171], a[104:107], a[48:51], v[168:171]\n"
  "v_exp_f32 v247, v247\n"
  "v_mfma_f32_16x16x32_bf16 v[168:171], a[108:111], a[52:55], v[168:171]\n"
  "v_cvt_pk_bf16_f32 v29, v246, v247\n"
  "v_cvt_pk_bf16_f32 v118, v180, v181\n"
  "s_waitcnt vmcnt(4)\n"
  "s_barrier\n"
  "v_mfma_f32_16x16x32_bf16 v[252:255], a[128:131], v[12:15], v[140:143]\n"
  "v_exp_f32 v248, v248\n"
  "v_mfma_f32_16x16x32_bf16 v[252:255], a[132:135], v[44:47], v[252:255]\n"
  "ds_read_b128 a[128:131], v216 offset:32768\n"
  "v_cvt_pk_bf16_f32 v119, v182, v183\n"
  "buffer_load_dwordx4 v229, s[24:27], s61 offen offset:1024 lds\n"
  "v_mfma_f32_16x16x32_bf16 v[252:255], a[136:139], v[76:79], v[252:255]\n"
  "v_exp_f32 v249, v249\n"
  "v_mfma_f32_16x16x32_bf16 v[252:255], a[140:143], v[108:111], v[252:255]\n"
  "ds_read_b64_tr_b16 a[160:161], v220 offset:16384\n"
  "v_cvt_pk_bf16_f32 v52, v248, v249\n"
  "v_mfma_f32_16x16x32_bf16 v[120:123], a[112:115], a[48:51], v[120:123]\n"
  "v_exp_f32 v250, v250\n"
  "v_mfma_f32_16x16x32_bf16 v[120:123], a[116:119], a[52:55], v[120:123]\n"
  "buffer_load_dwordx4 v228, s[24:27], s61 offen offset:0 lds\n"
  "v_cvt_pk_bf16_f32 v91, v90, v91\n"
  "v_cvt_pk_bf16_f32 v90, v88, v89\n"
  "v_mfma_f32_16x16x32_bf16 v[184:187], a[120:123], a[48:51], v[184:187]\n"
  "v_exp_f32 v251, v251\n"
  "v_mfma_f32_16x16x32_bf16 v[184:187], a[124:127], a[52:55], v[184:187]\n"
  "ds_read_b64_tr_b16 a[162:163], v220 offset:20480\n"
  "v_cvt_pk_bf16_f32 v53, v250, v251\n"
  "v_cvt_pk_bf16_f32 v88, v168, v169\n"
  "v_mfma_f32_16x16x32_bf16 v[240:243], a[144:147], v[0:3], v[128:131]\n"
  "s_waitcnt lgkmcnt(3)\n"
  "v_exp_f32 v252, v252\n"
  "v_mfma_f32_16x16x32_bf16 v[240:243], a[148:151], v[32:35], v[240:243]\n"
  "ds_read_b128 a[132:135], v217 offset:32768\n"
  "v_cvt_pk_bf16_f32 v89, v170, v171\n"
  "buffer_load_dwordx4 v231, s[24:27], s61 offen offset:3072 lds\n"
  "v_mfma_f32_16x16x32_bf16 v[240:243], a[152:155], v[64:67], v[240:243]\n"
  "v_exp_f32 v253, v253\n"
  "v_mfma_f32_16x16x32_bf16 v[240:243], a[156:159], v[96:99], v[240:243]\n"
  "ds_read_b64_tr_b16 a[164:165], v220 offset:24576\n"
  "v_cvt_pk_bf16_f32 v60, v252, v253\n"
  "v_mfma_f32_16x16x32_bf16 v[92:95], a[96:99], a[56:59], v[92:95]\n"
  "v_exp_f32 v254, v254\n"
  "v_mfma_f32_16x16x32_bf16 v[92:95], a[100:103], a[60:63], v[92:95]\n"
  "buffer_load_dwordx4 v230, s[24:27], s61 offen offset:2048 lds\n"
  "v_cvt_pk_bf16_f32 v120, v120, v121\n"
  "v_cvt_pk_bf16_f32 v121, v122, v123\n"
  "v_mfma_f32_16x16x32_bf16 v[172:175], a[104:107], a[56:59], v[172:175]\n"
  "v_exp_f32 v255, v255\n"
  "v_mfma_f32_16x16x32_bf16 v[172:175], a[108:111], a[60:63], v[172:175]\n"
  "ds_read_b64_tr_b16 a[166:167], v220 offset:28672\n"
  "v_cvt_pk_bf16_f32 v61, v254, v255\n"
  "v_cvt_pk_bf16_f32 v122, v184, v185\n"
  "v_mfma_f32_16x16x32_bf16 v[244:247], a[144:147], v[4:7], v[132:135]\n"
  "s_add_u32 m0, s52, 16384\n"
  "v_exp_f32 v240, v240\n"
  "v_mfma_f32_16x16x32_bf16 v[244:247], a[148:151], v[36:39], v[244:247]\n"
  "ds_read_b128 a[136:139], v218 offset:32768\n"
  "v_cvt_pk_bf16_f32 v123, v186, v187\n"
  "buffer_load_dwordx4 v233, s[28:31], s61 offen offset:1024 lds\n"
  "v_mfma_f32_16x16x32_bf16 v[244:247], a[152:155], v[68:71], v[244:247]\n"
  "v_exp_f32 v241, v241\n"
  "v_mfma_f32_16x16x32_bf16 v[244:247], a[156:159], v[100:103], v[244:247]\n"
  "ds_read_b64_tr_b16 a[168:169], v221 offset:16384\n"
  "v_cvt_pk_bf16_f32 v22, v240, v241\n"
  "v_mfma_f32_16x16x32_bf16 v[124:127], a[112:115], a[56:59], v[124:127]\n"
  "v_exp_f32 v242, v242\n"
  "v_mfma_f32_16x16x32_bf16 v[124:127], a[116:119], a[60:63], v[124:127]\n"
  "buffer_load_dwordx4 v232, s[28:31], s61 offen offset:0 lds\n"
  "v_cvt_pk_bf16_f32 v95, v94, v95\n"
  "v_cvt_pk_bf16_f32 v94, v92, v93\n"
  "v_mfma_f32_16x16x32_bf16 v[188:191], a[120:123], a[56:59], v[188:191]\n"
  "v_exp_f32 v243, v243\n"
  "v_mfma_f32_16x16x32_bf16 v[188:191], a[124:127], a[60:63], v[188:191]\n"
  "ds_read_b64_tr_b16 a[170:171], v221 offset:20480\n"
  "v_cvt_pk_bf16_f32 v23, v242, v243\n"
  "v_exp_f32 v244, v244\n"
  "v_mfma_f32_16x16x32_bf16 v[248:251], a[144:147], v[8:11], v[136:139]\n"
  "ds_read_b128 a[140:143], v219 offset:32768\n"
  "v_exp_f32 v245, v245\n"
  "v_exp_f32 v246, v246\n"
  "buffer_load_dwordx4 v235, s[28:31], s61 offen offset:3072 lds\n"
  "v_mfma_f32_16x16x32_bf16 v[248:251], a[148:151], v[40:43], v[248:251]\n"
  "ds_read_b64_tr_b16 a[172:173], v221 offset:24576\n"
  "v_cvt_pk_bf16_f32 v30, v244, v245\n"
  "v_exp_f32 v247, v247\n"
  "v_mfma_f32_16x16x32_bf16 v[248:251], a[152:155], v[72:75], v[248:251]\n"
  "buffer_load_dwordx4 v234, s[28:31], s61 offen offset:2048 lds\n"
  "v_cvt_pk_bf16_f32 v31, v246, v247\n"
  "v_cvt_pk_bf16_f32 v92, v172, v173\n"
  "v_mfma_f32_16x16x32_bf16 v[248:251], a[156:159], v[104:107], v[248:251]\n"
  "ds_read_b64_tr_b16 a[174:175], v221 offset:28672\n"
  "v_cvt_pk_bf16_f32 v93, v174, v175\n"
  "v_mfma_f32_16x16x32_bf16 v[252:255], a[144:147], v[12:15], v[140:143]\n"
  "ds_read_b128 a[144:147], v216 offset:36864\n"
  "v_cvt_pk_bf16_f32 v124, v124, v125\n"
  "v_cvt_pk_bf16_f32 v125, v126, v127\n"
  "v_mfma_f32_16x16x32_bf16 v[252:255], a[148:151], v[44:47], v[252:255]\n"
  "ds_read_b64_tr_b16 a[176:177], v222 offset:16384\n"
  "v_cvt_pk_bf16_f32 v126, v188, v189\n"
  "v_cvt_pk_bf16_f32 v127, v190, v191\n"
  "v_mfma_f32_16x16x32_bf16 v[252:255], a[152:155], v[76:79], v[252:255]\n"
  "v_exp_f32 v248, v248\n"
  "v_exp_f32 v249, v249\n"
  "v_mfma_f32_16x16x32_bf16 v[252:255], a[156:159], v[108:111], v[252:255]\n"
  "ds_read_b64_tr_b16 a[178:179], v222 offset:20480\n"
  "v_exp_f32 v250, v250\n"
  "v_cvt_pk_bf16_f32 v54, v248, v249\n"
  "v_exp_f32 v251, v251\n"
  "s_nop 0\n"
  "v_cvt_pk_bf16_f32 v55, v250, v251\n"
  "s_branch L_p1\n"
  "L_end:\n"
  "L_poll1:\n"
  "s_sleep 2\n"
  "buffer_load_dword v193, v238, s[68:71], 0 offen sc1\n"
  "s_waitcnt vmcnt(0)\n"
  "buffer_store_dword v193, v239, s[68:71], 0 offen offset:0 sc1\n"
  "s_add_u32 s66, s66, 1\n"
  "s_cmp_gt_u32 s66, 0x80000\n"
  "s_cbranch_scc1 L_poll1r\n"
  "v_cmp_eq_u32 vcc, s65, v193\n"
  "s_nop 3\n"
  "s_bfe_u32 s57, vcc_lo, 0x80008\n"
  "s_cmp_eq_u32 s57, 0xff\n"
  "s_cbranch_scc0 L_poll1\n"
  "s_branch L_poll1r\n"
  "L_slow:\n"
  "s_waitcnt vmcnt(0) lgkmcnt(0)\n"
  "s_barrier\n"
  "v_and_b32 v112, 0xf0, v192\n"
  "v_xor_b32 v112, v216, v112\n"
  "v_and_b32 v112, 0xffff, v112\n"
  "v_xor_b32 v115, 0x80, v112\n"
  "s_mov_b64 s[24:25], s[20:21]\n"
  "s_mov_b64 s[28:29], s[22:23]\n"
  "v_lshrrev_b32 v113, 8, v192\n"
  "v_lshlrev_b32 v113, 5, v113\n"
  "v_xor_b32 v113, v113, v192\n"
  "v_xor_b32 v114, 0x80, v113\n"
  "s_lshl_b32 s59, s50, 14\n"
  "s_add_u32 s59, s59, 65536\n"
  "v_and_b32 v184, 0xffff, v220\n"
  "v_add_u32 v184, s59, v184\n"
  "v_and_b32 v185, 0xffff, v221\n"
  "v_add_u32 v185, s59, v185\n"
  "v_and_b32 v186, 0xffff, v222\n"
  "v_add_u32 v186, s59, v186\n"
  "v_and_b32 v187, 0xffff, v223\n"
  "v_add_u32 v187, s59, v187\n"
  "v_and_b32 v188, 0xffff, v224\n"
  "v_add_u32 v188, s59, v188\n"
  "v_and_b32 v189, 0xffff, v225\n"
  "v_add_u32 v189, s59, v189\n"
  "v_and_b32 v190, 0xffff, v226\n"
  "v_add_u32 v190, s59, v190\n"
  "v_and_b32 v191, 0xffff, v227\n"
  "v_add_u32 v191, s59, v191\n"
  "v_mov_b32 v176, 0xff800000\n"
  "v_mov_b32 v178, 0xff800000\n"
  "v_mov_b32 v180, 0xff800000\n"
  "v_mov_b32 v182, 0xff800000\n"
  "s_mov_b32 s57, 0\n"
  "s_nop 7\n"
  "L_slow1:\n"
  "s_add_u32 s58, s57, 4096\n"
  "buffer_load_dwordx4 a[128:131], v112, s[24:27], s57 offen offset:0\n"
  "buffer_load_dwordx4 a[132:135], v112, s[24:27], s57 offen offset:64\n"
  "buffer_load_dwordx4 a[136:139], v115, s[24:27], s57 offen offset:0\n"
  "buffer_load_dwordx4 a[140:143], v115, s[24:27], s57 offen offset:64\n"
  "buffer_load_dwordx4 a[144:147], v112, s[24:27], s58 offen offset:0\n"
  "buffer_load_dwordx4 a[148:151], v112, s[24:27], s58 offen offset:64\n"
  "buffer_load_dwordx4 a[152:155], v115, s[24:27], s58 offen offset:0\n"
  "buffer_load_dwordx4 a[156:159], v115, s[24:27], s58 offen offset:64\n"
  "s_waitcnt vmcnt(0)\n"
  "v_mfma_f32_16x16x32_bf16 v[240:243], a[128:131], v[0:3], 0\n"
  "v_mfma_f32_16x16x32_bf16 v[240:243], a[132:135], v[32:35], v[240:243]\n"
  "v_mfma_f32_16x16x32_bf16 v[240:243], a[136:139], v[64:67], v[240:243]\n"
  "v_mfma_f32_16x16x32_bf16 v[240:243], a[140:143], v[96:99], v[240:243]\n"
  "v_mfma_f32_16x16x32_bf16 v[244:247], a[128:131], v[4:7], 0\n"
  "v_mfma_f32_16x16x32_bf16 v[244:247], a[132:135], v[36:39], v[244:247]\n"
  "v_mfma_f32_16x16x32_bf16 v[244:247], a[136:139], v[68:71], v[244:247]\n"
  "v_mfma_f32_16x16x32_bf16 v[244:247], a[140:143], v[100:103], v[244:247]\n"
  "v_mfma_f32_16x16x32_bf16 v[248:251], a[128:131], v[8:11], 0\n"
  "v_mfma_f32_16x16x32_bf16 v[248:251], a[132:135], v[40:43], v[248:251]\n"
  "v_mfma_f32_16x16x32_bf16 v[248:251], a[136:139], v[72:75], v[248:251]\n"
  "v_mfma_f32_16x16x32_bf16 v[248:251], a[140:143], v[104:107], v[248:251]\n"
  "v_mfma_f32_16x16x32_bf16 v[252:255], a[128:131], v[12:15], 0\n"
  "v_mfma_f32_16x16x32_bf16 v[252:255], a[132:135], v[44:47], v[252:255]\n"
  "v_mfma_f32_16x16x32_bf16 v[252:255], a[136:139], v[76:79], v[252:255]\n"
  "v_mfma_f32_16x16x32_bf16 v[252:255], a[140:143], v[108:111], v[252:255]\n"
  "v_mfma_f32_16x16x32_bf16 v[16:19], a[144:147], v[0:3], 0\n"
  "v_mfma_f32_16x16x32_bf16 v[16:19], a[148:151], v[32:35], v[16:19]\n"
  "v_mfma_f32_16x16x32_bf16 v[16:19], a[152:155], v[64:67], v[16:19]\n"
  "v_mfma_f32_16x16x32_bf16 v[16:19], a[156:159], v[96:99], v[16:19]\n"
  "v_mfma_f32_16x16x32_bf16 v[20:23], a[144:147], v[4:7], 0\n"
  "v_mfma_f32_16x16x32_bf16 v[20:23], a[148:151], v[36:39], v[20:23]\n"
  "v_mfma_f32_16x16x32_bf16 v[20:23], a[152:155], v[68:71], v[20:23]\n"
  "v_mfma_f32_16x16x32_bf16 v[20:23], a[156:159], v[100:103], v[20:23]\n"
  "v_mfma_f32_16x16x32_bf16 v[24:27], a[144:147], v[8:11], 0\n"
  "v_mfma_f32_16x16x32_bf16 v[24:27], a[148:151], v[40:43], v[24:27]\n"
  "v_mfma_f32_16x16x32_bf16 v[24:27], a[152:155], v[72:75], v[24:27]\n"
  "v_mfma_f32_16x16x32_bf16 v[24:27], a[156:159], v[104:107], v[24:27]\n"
  "v_mfma_f32_16x16x32_bf16 v[28:31], a[144:147], v[12:15], 0\n"
  "v_mfma_f32_16x16x32_bf16 v[28:31], a[148:151], v[44:47], v[28:31]\n"
  "v_mfma_f32_16x16x32_bf16 v[28:31], a[152:155], v[76:79], v[28:31]\n"
  "v_mfma_f32_16x16x32_bf16 v[28:31], a[156:159], v[108:111], v[28:31]\n"
  "s_nop 7\n"
  "v_max3_f32 v176, v240, v241, v176\n"
  "v_max3_f32 v176, v242, v243, v176\n"
  "v_max3_f32 v178, v244, v245, v178\n"
  "v_max3_f32 v178, v246, v247, v178\n"
  "v_max3_f32 v180, v248, v249, v180\n"
  "v_max3_f32 v180, v250, v251, v180\n"
  "v_max3_f32 v182, v252, v253, v182\n"
  "v_max3_f32 v182, v254, v255, v182\n"
  "v_max3_f32 v176, v16, v17, v176\n"
  "v_max3_f32 v176, v18, v19, v176\n"
  "v_max3_f32 v178, v20, v21, v178\n"
  "v_max3_f32 v178, v22, v23, v178\n"
  "v_max3_f32 v180, v24, v25, v180\n"
  "v_max3_f32 v180, v26, v27, v180\n"
  "v_max3_f32 v182, v28, v29, v182\n"
  "v_max3_f32 v182, v30, v31, v182\n"
  "s_add_u32 s57, s57, 8192\n"
  "s_cmp_lg_u32 s57, 0x80000\n"
  "s_cbranch_scc1 L_slow1\n"
  "v_mov_b32 v177, v176\n"
  "v_mov_b32 v179, v178\n"
  "v_mov_b32 v181, v180\n"
  "v_mov_b32 v183, v182\n"
  "v_permlane16_swap_b32 v176, v177\n"
  "v_permlane16_swap_b32 v178, v179\n"
  "v_permlane16_swap_b32 v180, v181\n"
  "v_permlane16_swap_b32 v182, v183\n"
  "v_max_f32 v176, v176, v177\n"
  "v_max_f32 v178, v178, v179\n"
  "v_max_f32 v180, v180, v181\n"
  "v_max_f32 v182, v182, v183\n"
  "v_mov_b32 v177, v176\n"
  "v_mov_b32 v179, v178\n"
  "v_mov_b32 v181, v180\n"
  "v_mov_b32 v183, v182\n"
  "v_permlane32_swap_b32 v176, v177\n"
  "v_permlane32_swap_b32 v178, v179\n"
  "v_permlane32_swap_b32 v180, v181\n"
  "v_permlane32_swap_b32 v182, v183\n"
  "v_max_f32 v176, v176, v177\n"
  "v_max_f32 v178, v178, v179\n"
  "v_max_f32 v180, v180, v181\n"
  "v_max_f32 v182, v182, v183\n"
  "v_sub_f32 v128, 0, v176\n"
  "v_sub_f32 v129, 0, v176\n"
  "v_sub_f32 v130, 0, v176\n"
  "v_sub_f32 v131, 0, v176\n"
  "v_sub_f32 v132, 0, v178\n"
  "v_sub_f32 v133, 0, v178\n"
  "v_sub_f32 v134, 0, v178\n"
  "v_sub_f32 v135, 0, v178\n"
  "v_sub_f32 v136, 0, v180\n"
  "v_sub_f32 v137, 0, v180\n"
  "v_sub_f32 v138, 0, v180\n"
  "v_sub_f32 v139, 0, v180\n"
  "v_sub_f32 v140, 0, v182\n"
  "v_sub_f32 v141, 0, v182\n"
  "v_sub_f32 v142, 0, v182\n"
  "v_sub_f32 v143, 0, v182\n"
  "v_accvgpr_write_b32 a240, 0\n"
  "v_accvgpr_write_b32 a241, 0\n"
  "v_accvgpr_write_b32 a242, 0\n"
  "v_accvgpr_write_b32 a243, 0\n"
  "v_accvgpr_write_b32 a244, 0\n"
  "v_accvgpr_write_b32 a245, 0\n"
  "v_accvgpr_write_b32 a246, 0\n"
  "v_accvgpr_write_b32 a247, 0\n"
  "v_accvgpr_write_b32 a248, 0\n"
  "v_accvgpr_write_b32 a249, 0\n"
  "v_accvgpr_write_b32 a250, 0\n"
  "v_accvgpr_write_b32 a251, 0\n"
  "v_accvgpr_write_b32 a252, 0\n"
  "v_accvgpr_write_b32 a253, 0\n"
  "v_accvgpr_write_b32 a254, 0\n"
  "v_accvgpr_write_b32 a255, 0\n"
  "v_mov_b32 v52, 0\n"
  "v_mov_b32 v53, 0\n"
  "v_mov_b32 v54, 0\n"
  "v_mov_b32 v55, 0\n"
  "v_mov_b32 v60, 0\n"
  "v_mov_b32 v61, 0\n"
  "v_mov_b32 v62, 0\n"
  "v_mov_b32 v63, 0\n"
  "v_mov_b32 v164, 0\n"
  "v_mov_b32 v165, 0\n"
  "v_mov_b32 v166, 0\n"
  "v_mov_b32 v167, 0\n"
  "v_mov_b32 v172, 0\n"
  "v_mov_b32 v173, 0\n"
  "v_mov_b32 v174, 0\n"
  "v_mov_b32 v175, 0\n"
  "v_mfma_f32_32x32x16_bf16 a[0:15], v[52:55], v[52:55], 0\n"
  "v_mfma_f32_32x32x16_bf16 a[16:31], v[52:55], v[52:55], 0\n"
  "v_mfma_f32_32x32x16_bf16 a[32:47], v[52:55], v[52:55], 0\n"
  "v_mfma_f32_32x32x16_bf16 a[48:63], v[52:55], v[52:55], 0\n"
  "v_mfma_f32_32x32x16_bf16 a[64:79], v[52:55], v[52:55], 0\n"
  "v_mfma_f32_32x32x16_bf16 a[80:95], v[52:55], v[52:55], 0\n"
  "v_mfma_f32_32x32x16_bf16 a[96:111], v[52:55], v[52:55], 0\n"
  "v_mfma_f32_32x32x16_bf16 a[112:127], v[52:55], v[52:55], 0\n"
  "s_mov_b32 s57, 0\n"
  "L_slow2:\n"
  "s_add_u32 s58, s57, 4096\n"
  "buffer_load_dwordx4 a[128:131], v112, s[24:27], s57 offen offset:0\n"
  "buffer_load_dwordx4 a[132:135], v112, s[24:27], s57 offen offset:64\n"
  "buffer_load_dwordx4 a[136:139], v115, s[24:27], s57 offen offset:0\n"
  "buffer_load_dwordx4 a[140:143], v115, s[24:27], s57 offen offset:64\n"
  "buffer_load_dwordx4 a[144:147], v112, s[24:27], s58 offen offset:0\n"
  "buffer_load_dwordx4 a[148:151], v112, s[24:27], s58 offen offset:64\n"
  "buffer_load_dwordx4 a[152:155], v115, s[24:27], s58 offen offset:0\n"
  "buffer_load_dwordx4 a[156:159], v115, s[24:27], s58 offen offset:64\n"
  "s_add_u32 m0, s59, 0\n"
  "s_nop 0\n"
  "buffer_load_dwordx4 v113, s[28:31], s57 offen offset:0 lds\n"
  "buffer_load_dwordx4 v114, s[28:31], s57 offen offset:1024 lds\n"
  "buffer_load_dwordx4 v113, s[28:31], s57 offen offset:2048 lds\n"
  "buffer_load_dwordx4 v114, s[28:31], s57 offen offset:3072 lds\n"
  "s_add_u32 m0, s59, 4096\n"
  "s_nop 0\n"
  "buffer_load_dwordx4 v113, s[28:31], s58 offen offset:0 lds\n"
  "buffer_load_dwordx4 v114, s[28:31], s58 offen offset:1024 lds\n"
  "buffer_load_dwordx4 v113, s[28:31], s58 offen offset:2048 lds\n"
  "buffer_load_dwordx4 v114, s[28:31], s58 offen offset:3072 lds\n"
  "s_waitcnt vmcnt(0)\n"
  "v_mfma_f32_16x16x32_bf16 v[240:243], a[128:131], v[0:3], 0\n"
  "v_mfma_f32_16x16x32_bf16 v[240:243], a[132:135], v[32:35], v[240:243]\n"
  "v_mfma_f32_16x16x32_bf16 v[240:243], a[136:139], v[64:67], v[240:243]\n"
  "v_mfma_f32_16x16x32_bf16 v[240:243], a[140:143], v[96:99], v[240:243]\n"
  "v_mfma_f32_16x16x32_bf16 v[244:247], a[128:131], v[4:7], 0\n"
  "v_mfma_f32_16x16x32_bf16 v[244:247], a[132:135], v[36:39], v[244:247]\n"
  "v_mfma_f32_16x16x32_bf16 v[244:247], a[136:139], v[68:71], v[244:247]\n"
  "v_mfma_f32_16x16x32_bf16 v[244:247], a[140:143], v[100:103], v[244:247]\n"
  "v_mfma_f32_16x16x32_bf16 v[248:251], a[128:131], v[8:11], 0\n"
  "v_mfma_f32_16x16x32_bf16 v[248:251], a[132:135], v[40:43], v[248:251]\n"
  "v_mfma_f32_16x16x32_bf16 v[248:251], a[136:139], v[72:75], v[248:251]\n"
  "v_mfma_f32_16x16x32_bf16 v[248:251], a[140:143], v[104:107], v[248:251]\n"
  "v_mfma_f32_16x16x32_bf16 v[252:255], a[128:131], v[12:15], 0\n"
  "v_mfma_f32_16x16x32_bf16 v[252:255], a[132:135], v[44:47], v[252:255]\n"
  "v_mfma_f32_16x16x32_bf16 v[252:255], a[136:139], v[76:79], v[252:255]\n"
  "v_mfma_f32_16x16x32_bf16 v[252:255], a[140:143], v[108:111], v[252:255]\n"
  "v_mfma_f32_16x16x32_bf16 v[16:19], a[144:147], v[0:3], 0\n"
  "v_mfma_f32_16x16x32_bf16 v[16:19], a[148:151], v[32:35], v[16:19]\n"
  "v_mfma_f32_16x16x32_bf16 v[16:19], a[152:155], v[64:67], v[16:19]\n"
  "v_mfma_f32_16x16x32_bf16 v[16:19], a[156:159], v[96:99], v[16:19]\n"
  "v_mfma_f32_16x16x32_bf16 v[20:23], a[144:147], v[4:7], 0\n"
  "v_mfma_f32_16x16x32_bf16 v[20:23], a[148:151], v[36:39], v[20:23]\n"
  "v_mfma_f32_16x16x32_bf16 v[20:23], a[152:155], v[68:71], v[20:23]\n"
  "v_mfma_f32_16x16x32_bf16 v[20:23], a[156:159], v[100:103], v[20:23]\n"
  "v_mfma_f32_16x16x32_bf16 v[24:27], a[144:147], v[8:11], 0\n"
  "v_mfma_f32_16x16x32_bf16 v[24:27], a[148:151], v[40:43], v[24:27]\n"
  "v_mfma_f32_16x16x32_bf16 v[24:27], a[152:155], v[72:75], v[24:27]\n"
  "v_mfma_f32_16x16x32_bf16 v[24:27], a[156:159], v[104:107], v[24:27]\n"
  "v_mfma_f32_16x16x32_bf16 v[28:31], a[144:147], v[12:15], 0\n"
  "v_mfma_f32_16x16x32_bf16 v[28:31], a[148:151], v[44:47], v[28:31]\n"
  "v_mfma_f32_16x16x32_bf16 v[28:31], a[152:155], v[76:79], v[28:31]\n"
  "v_mfma_f32_16x16x32_bf16 v[28:31], a[156:159], v[108:111], v[28:31]\n"
  "ds_read_b64_tr_b16 a[160:161], v184 offset:0\n"
  "ds_read_b64_tr_b16 a[162:163], v184 offset:4096\n"
  "ds_read_b64_tr_b16 a[164:165], v185 offset:0\n"
  "ds_read_b64_tr_b16 a[166:167], v185 offset:4096\n"
  "ds_read_b64_tr_b16 a[168:169], v186 offset:0\n"
  "ds_read_b64_tr_b16 a[170:171], v186 offset:4096\n"
  "ds_read_b64_tr_b16 a[172:173], v187 offset:0\n"
  "ds_read_b64_tr_b16 a[174:175], v187 offset:4096\n"
  "ds_read_b64_tr_b16 a[176:177], v188 offset:0\n"
  "ds_read_b64_tr_b16 a[178:179], v188 offset:4096\n"
  "ds_read_b64_tr_b16 a[180:181], v189 offset:0\n"
  "ds_read_b64_tr_b16 a[182:183], v189 offset:4096\n"
  "ds_read_b64_tr_b16 a[184:185], v190 offset:0\n"
  "ds_read_b64_tr_b16 a[186:187], v190 offset:4096\n"
  "ds_read_b64_tr_b16 a[188:189], v191 offset:0\n"
  "ds_read_b64_tr_b16 a[190:191], v191 offset:4096\n"
  "v_add_f32 v240, v240, v128\n"
  "v_add_f32 v241, v241, v129\n"
  "v_add_f32 v242, v242, v130\n"
  "v_add_f32 v243, v243, v131\n"
  "v_add_f32 v244, v244, v132\n"
  "v_add_f32 v245, v245, v133\n"
  "v_add_f32 v246, v246, v134\n"
  "v_add_f32 v247, v247, v135\n"
  "v_add_f32 v248, v248, v136\n"
  "v_add_f32 v249, v249, v137\n"
  "v_add_f32 v250, v250, v138\n"
  "v_add_f32 v251, v251, v139\n"
  "v_add_f32 v252, v252, v140\n"
  "v_add_f32 v253, v253, v141\n"
  "v_add_f32 v254, v254, v142\n"
  "v_add_f32 v255, v255, v143\n"
  "v_add_f32 v16, v16, v128\n"
  "v_add_f32 v17, v17, v129\n"
  "v_add_f32 v18, v18, v130\n"
  "v_add_f32 v19, v19, v131\n"
  "v_add_f32 v20, v20, v132\n"
  "v_add_f32 v21, v21, v133\n"
  "v_add_f32 v22, v22, v134\n"
  "v_add_f32 v23, v23, v135\n"
  "v_add_f32 v24, v24, v136\n"
  "v_add_f32 v25, v25, v137\n"
  "v_add_f32 v26, v26, v138\n"
  "v_add_f32 v27, v27, v139\n"
  "v_add_f32 v28, v28, v140\n"
  "v_add_f32 v29, v29, v141\n"
  "v_add_f32 v30, v30, v142\n"
  "v_add_f32 v31, v31, v143\n"
  "v_exp_f32 v240, v240\n"
  "v_exp_f32 v241, v241\n"
  "v_exp_f32 v242, v242\n"
  "v_exp_f32 v243, v243\n"
  "v_exp_f32 v244, v244\n"
  "v_exp_f32 v245, v245\n"
  "v_exp_f32 v246, v246\n"
  "v_exp_f32 v247, v247\n"
  "v_exp_f32 v248, v248\n"
  "v_exp_f32 v249, v249\n"
  "v_exp_f32 v250, v250\n"
  "v_exp_f32 v251, v251\n"
  "v_exp_f32 v252, v252\n"
  "v_exp_f32 v253, v253\n"
  "v_exp_f32 v254, v254\n"
  "v_exp_f32 v255, v255\n"
  "v_exp_f32 v16, v16\n"
  "v_exp_f32 v17, v17\n"
  "v_exp_f32 v18, v18\n"
  "v_exp_f32 v19, v19\n"
  "v_exp_f32 v20, v20\n"
  "v_exp_f32 v21, v21\n"
  "v_exp_f32 v22, v22\n"
  "v_exp_f32 v23, v23\n"
  "v_exp_f32 v24, v24\n"
  "v_exp_f32 v25, v25\n"
  "v_exp_f32 v26, v26\n"
  "v_exp_f32 v27, v27\n"
  "v_exp_f32 v28, v28\n"
  "v_exp_f32 v29, v29\n"
  "v_exp_f32 v30, v30\n"
  "v_exp_f32 v31, v31\n"
  "s_nop 0\n"
  "v_cvt_pk_bf16_f32 v48, v240, v241\n"
  "v_cvt_pk_bf16_f32 v49, v242, v243\n"
  "v_cvt_pk_bf16_f32 v56, v244, v245\n"
  "v_cvt_pk_bf16_f32 v57, v246, v247\n"
  "v_cvt_pk_bf16_f32 v160, v248, v249\n"
  "v_cvt_pk_bf16_f32 v161, v250, v251\n"
  "v_cvt_pk_bf16_f32 v168, v252, v253\n"
  "v_cvt_pk_bf16_f32 v169, v254, v255\n"
  "v_cvt_pk_bf16_f32 v50, v16, v17\n"
  "v_cvt_pk_bf16_f32 v51, v18, v19\n"
  "v_cvt_pk_bf16_f32 v58, v20, v21\n"
  "v_cvt_pk_bf16_f32 v59, v22, v23\n"
  "v_cvt_pk_bf16_f32 v162, v24, v25\n"
  "v_cvt_pk_bf16_f32 v163, v26, v27\n"
  "v_cvt_pk_bf16_f32 v170, v28, v29\n"
  "v_cvt_pk_bf16_f32 v171, v30, v31\n"
  "s_waitcnt lgkmcnt(0)\n"
  "s_nop 1\n"
  "v_mfma_f32_16x16x32_bf16 a[0:3], a[160:163], v[48:51], a[0:3]\n"
  "v_mfma_f32_16x16x32_bf16 a[4:7], a[160:163], v[56:59], a[4:7]\n"
  "v_mfma_f32_16x16x32_bf16 a[8:11], a[160:163], v[160:163], a[8:11]\n"
  "v_mfma_f32_16x16x32_bf16 a[12:15], a[160:163], v[168:171], a[12:15]\n"
  "v_mfma_f32_16x16x32_bf16 a[16:19], a[164:167], v[48:51], a[16:19]\n"
  "v_mfma_f32_16x16x32_bf16 a[20:23], a[164:167], v[56:59], a[20:23]\n"
  "v_mfma_f32_16x16x32_bf16 a[24:27], a[164:167], v[160:163], a[24:27]\n"
  "v_mfma_f32_16x16x32_bf16 a[28:31], a[164:167], v[168:171], a[28:31]\n"
  "v_mfma_f32_16x16x32_bf16 a[32:35], a[168:171], v[48:51], a[32:35]\n"
  "v_mfma_f32_16x16x32_bf16 a[36:39], a[168:171], v[56:59], a[36:39]\n"
  "v_mfma_f32_16x16x32_bf16 a[40:43], a[168:171], v[160:163], a[40:43]\n"
  "v_mfma_f32_16x16x32_bf16 a[44:47], a[168:171], v[168:171], a[44:47]\n"
  "v_mfma_f32_16x16x32_bf16 a[48:51], a[172:175], v[48:51], a[48:51]\n"
  "v_mfma_f32_16x16x32_bf16 a[52:55], a[172:175], v[56:59], a[52:55]\n"
  "v_mfma_f32_16x16x32_bf16 a[56:59], a[172:175], v[160:163], a[56:59]\n"
  "v_mfma_f32_16x16x32_bf16 a[60:63], a[172:175], v[168:171], a[60:63]\n"
  "v_mfma_f32_16x16x32_bf16 a[64:67], a[176:179], v[48:51], a[64:67]\n"
  "v_mfma_f32_16x16x32_bf16 a[68:71], a[176:179], v[56:59], a[68:71]\n"
  "v_mfma_f32_16x16x32_bf16 a[72:75], a[176:179], v[160:163], a[72:75]\n"
  "v_mfma_f32_16x16x32_bf16 a[76:79], a[176:179], v[168:171], a[76:79]\n"
  "v_mfma_f32_16x16x32_bf16 a[80:83], a[180:183], v[48:51], a[80:83]\n"
  "v_mfma_f32_16x16x32_bf16 a[84:87], a[180:183], v[56:59], a[84:87]\n"
  "v_mfma_f32_16x16x32_bf16 a[88:91], a[180:183], v[160:163], a[88:91]\n"
  "v_mfma_f32_16x16x32_bf16 a[92:95], a[180:183], v[168:171], a[92:95]\n"
  "v_mfma_f32_16x16x32_bf16 a[96:99], a[184:187], v[48:51], a[96:99]\n"
  "v_mfma_f32_16x16x32_bf16 a[100:103], a[184:187], v[56:59], a[100:103]\n"
  "v_mfma_f32_16x16x32_bf16 a[104:107], a[184:187], v[160:163], a[104:107]\n"
  "v_mfma_f32_16x16x32_bf16 a[108:111], a[184:187], v[168:171], a[108:111]\n"
  "v_mfma_f32_16x16x32_bf16 a[112:115], a[188:191], v[48:51], a[112:115]\n"
  "v_mfma_f32_16x16x32_bf16 a[116:119], a[188:191], v[56:59], a[116:119]\n"
  "v_mfma_f32_16x16x32_bf16 a[120:123], a[188:191], v[160:163], a[120:123]\n"
  "v_mfma_f32_16x16x32_bf16 a[124:127], a[188:191], v[168:171], a[124:127]\n"
  "v_smfmac_f32_16x16x64_bf16 a[240:243], v[144:147], v[48:55], v148\n"
  "v_smfmac_f32_16x16x64_bf16 a[244:247], v[144:147], v[56:63], v148\n"
  "v_smfmac_f32_16x16x64_bf16 a[248:251], v[144:147], v[160:167], v148\n"
  "v_smfmac_f32_16x16x64_bf16 a[252:255], v[144:147], v[168:175], v148\n"
  "s_add_u32 s57, s57, 8192\n"
  "s_cmp_lg_u32 s57, 0x80000\n"
  "s_cbranch_scc1 L_slow2\n"
  "s_nop 7\n"
  "s_add_u32 m0, s63, 0\n"
  "s_add_u32 s53, s13, 0\n"
  "buffer_load_dwordx4 v198, s[16:19], s53 offen offset:0 lds\n"
  "buffer_load_dwordx4 v199, s[16:19], s53 offen offset:1024 lds\n"
  "buffer_load_dwordx4 v200, s[16:19], s53 offen offset:2048 lds\n"
  "buffer_load_dwordx4 v201, s[16:19], s53 offen offset:3072 lds\n"
  "s_add_u32 m0, s63, 16384\n"
  "s_add_u32 s54, s13, 32768\n"
  "buffer_load_dwordx4 v198, s[16:19], s54 offen offset:0 lds\n"
  "buffer_load_dwordx4 v199, s[16:19], s54 offen offset:1024 lds\n"
  "buffer_load_dwordx4 v200, s[16:19], s54 offen offset:2048 lds\n"
  "buffer_load_dwordx4 v201, s[16:19], s54 offen offset:3072 lds\n"
  "s_add_u32 m0, s63, 32768\n"
  "s_add_u32 s53, s13, 65536\n"
  "buffer_load_dwordx4 v198, s[16:19], s53 offen offset:0 lds\n"
  "buffer_load_dwordx4 v199, s[16:19], s53 offen offset:1024 lds\n"
  "buffer_load_dwordx4 v200, s[16:19], s53 offen offset:2048 lds\n"
  "buffer_load_dwordx4 v201, s[16:19], s53 offen offset:3072 lds\n"
  "s_add_u32 m0, s63, 49152\n"
  "s_add_u32 s54, s13, 98304\n"
  "buffer_load_dwordx4 v198, s[16:19], s54 offen offset:0 lds\n"
  "buffer_load_dwordx4 v199, s[16:19], s54 offen offset:1024 lds\n"
  "buffer_load_dwordx4 v200, s[16:19], s54 offen offset:2048 lds\n"
  "buffer_load_dwordx4 v201, s[16:19], s54 offen offset:3072 lds\n"
  "s_add_u32 m0, s63, 131072\n"
  "s_add_u32 s53, s13, 131072\n"
  "buffer_load_dwordx4 v198, s[16:19], s53 offen offset:0 lds\n"
  "buffer_load_dwordx4 v199, s[16:19], s53 offen offset:1024 lds\n"
  "buffer_load_dwordx4 v200, s[16:19], s53 offen offset:2048 lds\n"
  "buffer_load_dwordx4 v201, s[16:19], s53 offen offset:3072 lds\n"
  "s_add_u32 m0, s63, 147456\n"
  "s_add_u32 s54, s13, 163840\n"
  "buffer_load_dwordx4 v198, s[16:19], s54 offen offset:0 lds\n"
  "buffer_load_dwordx4 v199, s[16:19], s54 offen offset:1024 lds\n"
  "buffer_load_dwordx4 v200, s[16:19], s54 offen offset:2048 lds\n"
  "buffer_load_dwordx4 v201, s[16:19], s54 offen offset:3072 lds\n"
  "v_accvgpr_read_b32 v88, a240\n"
  "v_accvgpr_read_b32 v89, a241\n"
  "v_accvgpr_read_b32 v90, a244\n"
  "v_accvgpr_read_b32 v91, a245\n"
  "v_accvgpr_read_b32 v92, a248\n"
  "v_accvgpr_read_b32 v93, a249\n"
  "v_accvgpr_read_b32 v94, a252\n"
  "v_accvgpr_read_b32 v95, a253\n"
  "v_add_f32 v88, v88, v89\n"
  "v_add_f32 v90, v90, v91\n"
  "v_add_f32 v92, v92, v93\n"
  "v_add_f32 v94, v94, v95\n"
  "ds_bpermute_b32 v112, v212, v88\n"
  "ds_bpermute_b32 v113, v212, v90\n"
  "ds_bpermute_b32 v114, v212, v92\n"
  "ds_bpermute_b32 v115, v212, v94\n"
  "s_waitcnt lgkmcnt(3)\n"
  "v_rcp_f32 v112, v112\n"
  "s_waitcnt lgkmcnt(2)\n"
  "v_rcp_f32 v113, v113\n"
  "s_waitcnt lgkmcnt(1)\n"
  "v_rcp_f32 v114, v114\n"
  "s_waitcnt lgkmcnt(0)\n"
  "v_rcp_f32 v115, v115\n"
  "v_mfma_f32_16x16x32_bf16 v[136:139], v[150:153], a[216:219], 0\n"
  "v_mfma_f32_16x16x32_bf16 v[140:143], v[154:157], a[218:221], 0\n"
  "v_mfma_f32_16x16x32_bf16 v[136:139], v[154:157], a[222:225], v[136:139]\n"
  "v_mfma_f32_16x16x32_bf16 v[140:143], v[154:157], a[226:229], v[140:143]\n"
  "v_mfma_f32_16x16x32_bf16 v[136:139], v[154:157], a[230:233], v[136:139]\n"
  "v_mfma_f32_16x16x32_bf16 v[140:143], v[154:157], a[234:237], v[140:143]\n"
  "v_mfma_f32_16x16x32_bf16 v[136:139], v[152:155], a[236:239], v[136:139]\n"
  "v_mfma_f32_16x16x32_bf16 v[128:131], v[150:153], a[192:195], 0\n"
  "v_mfma_f32_16x16x32_bf16 v[132:135], v[154:157], a[194:197], 0\n"
  "v_mfma_f32_16x16x32_bf16 v[128:131], v[154:157], a[198:201], v[128:131]\n"
  "v_mfma_f32_16x16x32_bf16 v[132:135], v[154:157], a[202:205], v[132:135]\n"
  "v_mfma_f32_16x16x32_bf16 v[128:131], v[154:157], a[206:209], v[128:131]\n"
  "v_mfma_f32_16x16x32_bf16 v[132:135], v[154:157], a[210:213], v[132:135]\n"
  "v_mfma_f32_16x16x32_bf16 v[128:131], v[152:155], a[212:215], v[128:131]\n"
  "v_mfma_f32_16x16x32_bf16 v[128:131], v[150:153], v[80:83], v[128:131]\n"
  "v_mfma_f32_16x16x32_bf16 v[132:135], v[152:155], v[80:83], v[132:135]\n"
  "v_mfma_f32_16x16x32_bf16 v[136:139], v[150:153], v[84:87], v[136:139]\n"
  "v_mfma_f32_16x16x32_bf16 v[140:143], v[152:155], v[84:87], v[140:143]\n"
  "v_mfma_f32_16x16x32_bf16 v[128:131], v[150:153], v[120:123], v[128:131]\n"
  "v_mfma_f32_16x16x32_bf16 v[132:135], v[152:155], v[120:123], v[132:135]\n"
  "v_mfma_f32_16x16x32_bf16 v[136:139], v[150:153], v[124:127], v[136:139]\n"
  "v_mfma_f32_16x16x32_bf16 v[140:143], v[152:155], v[124:127], v[140:143]\n"
  "v_accvgpr_read_b32 v0, a0\n"
  "v_accvgpr_read_b32 v1, a1\n"
  "v_accvgpr_read_b32 v2, a2\n"
  "v_accvgpr_read_b32 v3, a3\n"
  "v_accvgpr_read_b32 v4, a16\n"
  "v_accvgpr_read_b32 v5, a17\n"
  "v_accvgpr_read_b32 v6, a18\n"
  "v_accvgpr_read_b32 v7, a19\n"
  "v_mul_f32 v0, v112, v0\n"
  "v_mul_f32 v1, v112, v1\n"
  "v_mul_f32 v2, v112, v2\n"
  "v_mul_f32 v3, v112, v3\n"
  "v_mul_f32 v4, v112, v4\n"
  "v_mul_f32 v5, v112, v5\n"
  "v_mul_f32 v6, v112, v6\n"
  "v_mul_f32 v7, v112, v7\n"
  "v_cvt_pk_bf16_f32 v0, v0, v1\n"
  "v_cvt_pk_bf16_f32 v1, v2, v3\n"
  "v_cvt_pk_bf16_f32 v2, v4, v5\n"
  "v_cvt_pk_bf16_f32 v3, v6, v7\n"
  "ds_write_b128 v210, v[0:3] offset:0\n"
  "v_accvgpr_read_b32 v8, a4\n"
  "v_accvgpr_read_b32 v9, a5\n"
  "v_accvgpr_read_b32 v10, a6\n"
  "v_accvgpr_read_b32 v11, a7\n"
  "v_accvgpr_read_b32 v12, a20\n"
  "v_accvgpr_read_b32 v13, a21\n"
  "v_accvgpr_read_b32 v14, a22\n"
  "v_accvgpr_read_b32 v15, a23\n"
  "v_mul_f32 v8, v113, v8\n"
  "v_mul_f32 v9, v113, v9\n"
  "v_mul_f32 v10, v113, v10\n"
  "v_mul_f32 v11, v113, v11\n"
  "v_mul_f32 v12, v113, v12\n"
  "v_mul_f32 v13, v113, v13\n"
  "v_mul_f32 v14, v113, v14\n"
  "v_mul_f32 v15, v113, v15\n"
  "v_cvt_pk_bf16_f32 v8, v8, v9\n"
  "v_cvt_pk_bf16_f32 v9, v10, v11\n"
  "v_cvt_pk_bf16_f32 v10, v12, v13\n"
  "v_cvt_pk_bf16_f32 v11, v14, v15\n"
  "ds_write_b128 v210, v[8:11] offset:1024\n"
  "v_accvgpr_read_b32 v32, a8\n"
  "v_accvgpr_read_b32 v33, a9\n"
  "v_accvgpr_read_b32 v34, a10\n"
  "v_accvgpr_read_b32 v35, a11\n"
  "v_accvgpr_read_b32 v36, a24\n"
  "v_accvgpr_read_b32 v37, a25\n"
  "v_accvgpr_read_b32 v38, a26\n"
  "v_accvgpr_read_b32 v39, a27\n"
  "v_mul_f32 v32, v114, v32\n"
  "v_mul_f32 v33, v114, v33\n"
  "v_mul_f32 v34, v114, v34\n"
  "v_mul_f32 v35, v114, v35\n"
  "v_mul_f32 v36, v114, v36\n"
  "v_mul_f32 v37, v114, v37\n"
  "v_mul_f32 v38, v114, v38\n"
  "v_mul_f32 v39, v114, v39\n"
  "v_cvt_pk_bf16_f32 v32, v32, v33\n"
  "v_cvt_pk_bf16_f32 v33, v34, v35\n"
  "v_cvt_pk_bf16_f32 v34, v36, v37\n"
  "v_cvt_pk_bf16_f32 v35, v38, v39\n"
  "ds_write_b128 v211, v[32:35] offset:0\n"
  "v_accvgpr_read_b32 v40, a12\n"
  "v_accvgpr_read_b32 v41, a13\n"
  "v_accvgpr_read_b32 v42, a14\n"
  "v_accvgpr_read_b32 v43, a15\n"
  "v_accvgpr_read_b32 v44, a28\n"
  "v_accvgpr_read_b32 v45, a29\n"
  "v_accvgpr_read_b32 v46, a30\n"
  "v_accvgpr_read_b32 v47, a31\n"
  "v_mul_f32 v40, v115, v40\n"
  "v_mul_f32 v41, v115, v41\n"
  "v_mul_f32 v42, v115, v42\n"
  "v_mul_f32 v43, v115, v43\n"
  "v_mul_f32 v44, v115, v44\n"
  "v_mul_f32 v45, v115, v45\n"
  "v_mul_f32 v46, v115, v46\n"
  "v_mul_f32 v47, v115, v47\n"
  "v_cvt_pk_bf16_f32 v40, v40, v41\n"
  "v_cvt_pk_bf16_f32 v41, v42, v43\n"
  "v_cvt_pk_bf16_f32 v42, v44, v45\n"
  "v_cvt_pk_bf16_f32 v43, v46, v47\n"
  "ds_write_b128 v211, v[40:43] offset:1024\n"
  "v_accvgpr_read_b32 v0, a32\n"
  "v_accvgpr_read_b32 v1, a33\n"
  "v_accvgpr_read_b32 v2, a34\n"
  "v_accvgpr_read_b32 v3, a35\n"
  "v_accvgpr_read_b32 v4, a48\n"
  "v_accvgpr_read_b32 v5, a49\n"
  "v_accvgpr_read_b32 v6, a50\n"
  "v_accvgpr_read_b32 v7, a51\n"
  "v_mul_f32 v0, v112, v0\n"
  "v_mul_f32 v1, v112, v1\n"
  "v_mul_f32 v2, v112, v2\n"
  "v_mul_f32 v3, v112, v3\n"
  "v_mul_f32 v4, v112, v4\n"
  "v_mul_f32 v5, v112, v5\n"
  "v_mul_f32 v6, v112, v6\n"
  "v_mul_f32 v7, v112, v7\n"
  "v_cvt_pk_bf16_f32 v0, v0, v1\n"
  "v_cvt_pk_bf16_f32 v1, v2, v3\n"
  "v_cvt_pk_bf16_f32 v2, v4, v5\n"
  "v_cvt_pk_bf16_f32 v3, v6, v7\n"
  "ds_write_b128 v210, v[0:3] offset:4096\n"
  "v_accvgpr_read_b32 v8, a36\n"
  "v_accvgpr_read_b32 v9, a37\n"
  "v_accvgpr_read_b32 v10, a38\n"
  "v_accvgpr_read_b32 v11, a39\n"
  "v_accvgpr_read_b32 v12, a52\n"
  "v_accvgpr_read_b32 v13, a53\n"
  "v_accvgpr_read_b32 v14, a54\n"
  "v_accvgpr_read_b32 v15, a55\n"
  "v_mul_f32 v8, v113, v8\n"
  "v_mul_f32 v9, v113, v9\n"
  "v_mul_f32 v10, v113, v10\n"
  "v_mul_f32 v11, v113, v11\n"
  "v_mul_f32 v12, v113, v12\n"
  "v_mul_f32 v13, v113, v13\n"
  "v_mul_f32 v14, v113, v14\n"
  "v_mul_f32 v15, v113, v15\n"
  "v_cvt_pk_bf16_f32 v8, v8, v9\n"
  "v_cvt_pk_bf16_f32 v9, v10, v11\n"
  "v_cvt_pk_bf16_f32 v10, v12, v13\n"
  "v_cvt_pk_bf16_f32 v11, v14, v15\n"
  "ds_write_b128 v210, v[8:11] offset:5120\n"
  "v_accvgpr_read_b32 v32, a40\n"
  "v_accvgpr_read_b32 v33, a41\n"
  "v_accvgpr_read_b32 v34, a42\n"
  "v_accvgpr_read_b32 v35, a43\n"
  "v_accvgpr_read_b32 v36, a56\n"
  "v_accvgpr_read_b32 v37, a57\n"
  "v_accvgpr_read_b32 v38, a58\n"
  "v_accvgpr_read_b32 v39, a59\n"
  "v_mul_f32 v32, v114, v32\n"
  "v_mul_f32 v33, v114, v33\n"
  "v_mul_f32 v34, v114, v34\n"
  "v_mul_f32 v35, v114, v35\n"
  "v_mul_f32 v36, v114, v36\n"
  "v_mul_f32 v37, v114, v37\n"
  "v_mul_f32 v38, v114, v38\n"
  "v_mul_f32 v39, v114, v39\n"
  "v_cvt_pk_bf16_f32 v32, v32, v33\n"
  "v_cvt_pk_bf16_f32 v33, v34, v35\n"
  "v_cvt_pk_bf16_f32 v34, v36, v37\n"
  "v_cvt_pk_bf16_f32 v35, v38, v39\n"
  "ds_write_b128 v211, v[32:35] offset:4096\n"
  "v_accvgpr_read_b32 v40, a44\n"
  "v_accvgpr_read_b32 v41, a45\n"
  "v_accvgpr_read_b32 v42, a46\n"
  "v_accvgpr_read_b32 v43, a47\n"
  "v_accvgpr_read_b32 v44, a60\n"
  "v_accvgpr_read_b32 v45, a61\n"
  "v_accvgpr_read_b32 v46, a62\n"
  "v_accvgpr_read_b32 v47, a63\n"
  "v_mul_f32 v40, v115, v40\n"
  "v_mul_f32 v41, v115, v41\n"
  "v_mul_f32 v42, v115, v42\n"
  "v_mul_f32 v43, v115, v43\n"
  "v_mul_f32 v44, v115, v44\n"
  "v_mul_f32 v45, v115, v45\n"
  "v_mul_f32 v46, v115, v46\n"
  "v_mul_f32 v47, v115, v47\n"
  "v_cvt_pk_bf16_f32 v40, v40, v41\n"
  "v_cvt_pk_bf16_f32 v41, v42, v43\n"
  "v_cvt_pk_bf16_f32 v42, v44, v45\n"
  "v_cvt_pk_bf16_f32 v43, v46, v47\n"
  "ds_write_b128 v211, v[40:43] offset:5120\n"
  "v_accvgpr_read_b32 v0, a64\n"
  "v_accvgpr_read_b32 v1, a65\n"
  "v_accvgpr_read_b32 v2, a66\n"
  "v_accvgpr_read_b32 v3, a67\n"
  "v_accvgpr_read_b32 v4, a80\n"
  "v_accvgpr_read_b32 v5, a81\n"
  "v_accvgpr_read_b32 v6, a82\n"
  "v_accvgpr_read_b32 v7, a83\n"
  "v_mul_f32 v0, v112, v0\n"
  "v_mul_f32 v1, v112, v1\n"
  "v_mul_f32 v2, v112, v2\n"
  "v_mul_f32 v3, v112, v3\n"
  "v_mul_f32 v4, v112, v4\n"
  "v_mul_f32 v5, v112, v5\n"
  "v_mul_f32 v6, v112, v6\n"
  "v_mul_f32 v7, v112, v7\n"
  "v_cvt_pk_bf16_f32 v0, v0, v1\n"
  "v_cvt_pk_bf16_f32 v1, v2, v3\n"
  "v_cvt_pk_bf16_f32 v2, v4, v5\n"
  "v_cvt_pk_bf16_f32 v3, v6, v7\n"
  "ds_write_b128 v210, v[0:3] offset:8192\n"
  "v_accvgpr_read_b32 v8, a68\n"
  "v_accvgpr_read_b32 v9, a69\n"
  "v_accvgpr_read_b32 v10, a70\n"
  "v_accvgpr_read_b32 v11, a71\n"
  "v_accvgpr_read_b32 v12, a84\n"
  "v_accvgpr_read_b32 v13, a85\n"
  "v_accvgpr_read_b32 v14, a86\n"
  "v_accvgpr_read_b32 v15, a87\n"
  "v_mul_f32 v8, v113, v8\n"
  "v_mul_f32 v9, v113, v9\n"
  "v_mul_f32 v10, v113, v10\n"
  "v_mul_f32 v11, v113, v11\n"
  "v_mul_f32 v12, v113, v12\n"
  "v_mul_f32 v13, v113, v13\n"
  "v_mul_f32 v14, v113, v14\n"
  "v_mul_f32 v15, v113, v15\n"
  "v_cvt_pk_bf16_f32 v8, v8, v9\n"
  "v_cvt_pk_bf16_f32 v9, v10, v11\n"
  "v_cvt_pk_bf16_f32 v10, v12, v13\n"
  "v_cvt_pk_bf16_f32 v11, v14, v15\n"
  "ds_write_b128 v210, v[8:11] offset:9216\n"
  "v_accvgpr_read_b32 v32, a72\n"
  "v_accvgpr_read_b32 v33, a73\n"
  "v_accvgpr_read_b32 v34, a74\n"
  "v_accvgpr_read_b32 v35, a75\n"
  "v_accvgpr_read_b32 v36, a88\n"
  "v_accvgpr_read_b32 v37, a89\n"
  "v_accvgpr_read_b32 v38, a90\n"
  "v_accvgpr_read_b32 v39, a91\n"
  "v_mul_f32 v32, v114, v32\n"
  "v_mul_f32 v33, v114, v33\n"
  "v_mul_f32 v34, v114, v34\n"
  "v_mul_f32 v35, v114, v35\n"
  "v_mul_f32 v36, v114, v36\n"
  "v_mul_f32 v37, v114, v37\n"
  "v_mul_f32 v38, v114, v38\n"
  "v_mul_f32 v39, v114, v39\n"
  "v_cvt_pk_bf16_f32 v32, v32, v33\n"
  "v_cvt_pk_bf16_f32 v33, v34, v35\n"
  "v_cvt_pk_bf16_f32 v34, v36, v37\n"
  "v_cvt_pk_bf16_f32 v35, v38, v39\n"
  "ds_write_b128 v211, v[32:35] offset:8192\n"
  "v_accvgpr_read_b32 v40, a76\n"
  "v_accvgpr_read_b32 v41, a77\n"
  "v_accvgpr_read_b32 v42, a78\n"
  "v_accvgpr_read_b32 v43, a79\n"
  "v_accvgpr_read_b32 v44, a92\n"
  "v_accvgpr_read_b32 v45, a93\n"
  "v_accvgpr_read_b32 v46, a94\n"
  "v_accvgpr_read_b32 v47, a95\n"
  "v_mul_f32 v40, v115, v40\n"
  "v_mul_f32 v41, v115, v41\n"
  "v_mul_f32 v42, v115, v42\n"
  "v_mul_f32 v43, v115, v43\n"
  "v_mul_f32 v44, v115, v44\n"
  "v_mul_f32 v45, v115, v45\n"
  "v_mul_f32 v46, v115, v46\n"
  "v_mul_f32 v47, v115, v47\n"
  "v_cvt_pk_bf16_f32 v40, v40, v41\n"
  "v_cvt_pk_bf16_f32 v41, v42, v43\n"
  "v_cvt_pk_bf16_f32 v42, v44, v45\n"
  "v_cvt_pk_bf16_f32 v43, v46, v47\n"
  "ds_write_b128 v211, v[40:43] offset:9216\n"
  "v_accvgpr_read_b32 v0, a96\n"
  "v_accvgpr_read_b32 v1, a97\n"
  "v_accvgpr_read_b32 v2, a98\n"
  "v_accvgpr_read_b32 v3, a99\n"
  "v_accvgpr_read_b32 v4, a112\n"
  "v_accvgpr_read_b32 v5, a113\n"
  "v_accvgpr_read_b32 v6, a114\n"
  "v_accvgpr_read_b32 v7, a115\n"
  "v_mul_f32 v0, v112, v0\n"
  "v_mul_f32 v1, v112, v1\n"
  "v_mul_f32 v2, v112, v2\n"
  "v_mul_f32 v3, v112, v3\n"
  "v_mul_f32 v4, v112, v4\n"
  "v_mul_f32 v5, v112, v5\n"
  "v_mul_f32 v6, v112, v6\n"
  "v_mul_f32 v7, v112, v7\n"
  "v_cvt_pk_bf16_f32 v0, v0, v1\n"
  "v_cvt_pk_bf16_f32 v1, v2, v3\n"
  "v_cvt_pk_bf16_f32 v2, v4, v5\n"
  "v_cvt_pk_bf16_f32 v3, v6, v7\n"
  "ds_write_b128 v210, v[0:3] offset:12288\n"
  "v_accvgpr_read_b32 v8, a100\n"
  "v_accvgpr_read_b32 v9, a101\n"
  "v_accvgpr_read_b32 v10, a102\n"
  "v_accvgpr_read_b32 v11, a103\n"
  "v_accvgpr_read_b32 v12, a116\n"
  "v_accvgpr_read_b32 v13, a117\n"
  "v_accvgpr_read_b32 v14, a118\n"
  "v_accvgpr_read_b32 v15, a119\n"
  "v_mul_f32 v8, v113, v8\n"
  "v_mul_f32 v9, v113, v9\n"
  "v_mul_f32 v10, v113, v10\n"
  "v_mul_f32 v11, v113, v11\n"
  "v_mul_f32 v12, v113, v12\n"
  "v_mul_f32 v13, v113, v13\n"
  "v_mul_f32 v14, v113, v14\n"
  "v_mul_f32 v15, v113, v15\n"
  "v_cvt_pk_bf16_f32 v8, v8, v9\n"
  "v_cvt_pk_bf16_f32 v9, v10, v11\n"
  "v_cvt_pk_bf16_f32 v10, v12, v13\n"
  "v_cvt_pk_bf16_f32 v11, v14, v15\n"
  "ds_write_b128 v210, v[8:11] offset:13312\n"
  "v_accvgpr_read_b32 v32, a104\n"
  "v_accvgpr_read_b32 v33, a105\n"
  "v_accvgpr_read_b32 v34, a106\n"
  "v_accvgpr_read_b32 v35, a107\n"
  "v_accvgpr_read_b32 v36, a120\n"
  "v_accvgpr_read_b32 v37, a121\n"
  "v_accvgpr_read_b32 v38, a122\n"
  "v_accvgpr_read_b32 v39, a123\n"
  "v_mul_f32 v32, v114, v32\n"
  "v_mul_f32 v33, v114, v33\n"
  "v_mul_f32 v34, v114, v34\n"
  "v_mul_f32 v35, v114, v35\n"
  "v_mul_f32 v36, v114, v36\n"
  "v_mul_f32 v37, v114, v37\n"
  "v_mul_f32 v38, v114, v38\n"
  "v_mul_f32 v39, v114, v39\n"
  "v_cvt_pk_bf16_f32 v32, v32, v33\n"
  "v_cvt_pk_bf16_f32 v33, v34, v35\n"
  "v_cvt_pk_bf16_f32 v34, v36, v37\n"
  "v_cvt_pk_bf16_f32 v35, v38, v39\n"
  "ds_write_b128 v211, v[32:35] offset:12288\n"
  "v_accvgpr_read_b32 v40, a108\n"
  "v_accvgpr_read_b32 v41, a109\n"
  "v_accvgpr_read_b32 v42, a110\n"
  "v_accvgpr_read_b32 v43, a111\n"
  "v_accvgpr_read_b32 v44, a124\n"
  "v_accvgpr_read_b32 v45, a125\n"
  "v_accvgpr_read_b32 v46, a126\n"
  "v_accvgpr_read_b32 v47, a127\n"
  "v_mul_f32 v40, v115, v40\n"
  "v_mul_f32 v41, v115, v41\n"
  "v_mul_f32 v42, v115, v42\n"
  "v_mul_f32 v43, v115, v43\n"
  "v_mul_f32 v44, v115, v44\n"
  "v_mul_f32 v45, v115, v45\n"
  "v_mul_f32 v46, v115, v46\n"
  "v_mul_f32 v47, v115, v47\n"
  "v_cvt_pk_bf16_f32 v40, v40, v41\n"
  "v_cvt_pk_bf16_f32 v41, v42, v43\n"
  "v_cvt_pk_bf16_f32 v42, v44, v45\n"
  "v_cvt_pk_bf16_f32 v43, v46, v47\n"
  "ds_write_b128 v211, v[40:43] offset:13312\n"
  "s_waitcnt vmcnt(20)\n"
  "ds_read_b64_tr_b16 a[128:129], v194 offset:0\n"
  "ds_read_b64_tr_b16 a[130:131], v194 offset:2048\n"
  "ds_read_b64_tr_b16 a[136:137], v195 offset:0\n"
  "ds_read_b64_tr_b16 a[138:139], v195 offset:2048\n"
  "ds_read_b64_tr_b16 a[144:145], v196 offset:0\n"
  "ds_read_b64_tr_b16 a[146:147], v196 offset:2048\n"
  "ds_read_b64_tr_b16 a[152:153], v197 offset:0\n"
  "ds_read_b64_tr_b16 a[154:155], v197 offset:2048\n"
  "s_waitcnt vmcnt(16)\n"
  "ds_read_b64_tr_b16 a[132:133], v194 offset:16384\n"
  "ds_read_b64_tr_b16 a[134:135], v194 offset:18432\n"
  "ds_read_b64_tr_b16 a[140:141], v195 offset:16384\n"
  "ds_read_b64_tr_b16 a[142:143], v195 offset:18432\n"
  "ds_read_b64_tr_b16 a[148:149], v196 offset:16384\n"
  "ds_read_b64_tr_b16 a[150:151], v196 offset:18432\n"
  "ds_read_b64_tr_b16 a[156:157], v197 offset:16384\n"
  "ds_read_b64_tr_b16 a[158:159], v197 offset:18432\n"
  "s_add_u32 m0, s63, 0\n"
  "s_add_u32 s53, s13, 196608\n"
  "s_waitcnt lgkmcnt(8)\n"
  "buffer_load_dwordx4 v198, s[16:19], s53 offen offset:0 lds\n"
  "buffer_load_dwordx4 v199, s[16:19], s53 offen offset:1024 lds\n"
  "buffer_load_dwordx4 v200, s[16:19], s53 offen offset:2048 lds\n"
  "buffer_load_dwordx4 v201, s[16:19], s53 offen offset:3072 lds\n"
  "s_add_u32 m0, s63, 16384\n"
  "s_add_u32 s54, s13, 229376\n"
  "s_waitcnt lgkmcnt(0)\n"
  "buffer_load_dwordx4 v198, s[16:19], s54 offen offset:0 lds\n"
  "buffer_load_dwordx4 v199, s[16:19], s54 offen offset:1024 lds\n"
  "buffer_load_dwordx4 v200, s[16:19], s54 offen offset:2048 lds\n"
  "buffer_load_dwordx4 v201, s[16:19], s54 offen offset:3072 lds\n"
  "s_waitcnt vmcnt(20)\n"
  "ds_read_b64_tr_b16 v[64:65], v194 offset:32768\n"
  "ds_read_b64_tr_b16 v[66:67], v194 offset:34816\n"
  "ds_read_b64_tr_b16 v[72:73], v195 offset:32768\n"
  "ds_read_b64_tr_b16 v[74:75], v195 offset:34816\n"
  "ds_read_b64_tr_b16 v[96:97], v196 offset:32768\n"
  "ds_read_b64_tr_b16 v[98:99], v196 offset:34816\n"
  "ds_read_b64_tr_b16 v[104:105], v197 offset:32768\n"
  "ds_read_b64_tr_b16 v[106:107], v197 offset:34816\n"
  "s_waitcnt vmcnt(16)\n"
  "ds_read_b64_tr_b16 v[68:69], v194 offset:49152\n"
  "ds_read_b64_tr_b16 v[70:71], v194 offset:51200\n"
  "ds_read_b64_tr_b16 v[76:77], v195 offset:49152\n"
  "ds_read_b64_tr_b16 v[78:79], v195 offset:51200\n"
  "ds_read_b64_tr_b16 v[100:101], v196 offset:49152\n"
  "ds_read_b64_tr_b16 v[102:103], v196 offset:51200\n"
  "ds_read_b64_tr_b16 v[108:109], v197 offset:49152\n"
  "ds_read_b64_tr_b16 v[110:111], v197 offset:51200\n"
  "s_add_u32 m0, s63, 32768\n"
  "s_add_u32 s53, s13, 262144\n"
  "s_waitcnt lgkmcnt(8)\n"
  "buffer_load_dwordx4 v198, s[16:19], s53 offen offset:0 lds\n"
  "buffer_load_dwordx4 v199, s[16:19], s53 offen offset:1024 lds\n"
  "buffer_load_dwordx4 v200, s[16:19], s53 offen offset:2048 lds\n"
  "buffer_load_dwordx4 v201, s[16:19], s53 offen offset:3072 lds\n"
  "s_add_u32 m0, s63, 49152\n"
  "s_add_u32 s54, s13, 294912\n"
  "s_waitcnt lgkmcnt(0)\n"
  "buffer_load_dwordx4 v198, s[16:19], s54 offen offset:0 lds\n"
  "buffer_load_dwordx4 v199, s[16:19], s54 offen offset:1024 lds\n"
  "buffer_load_dwordx4 v200, s[16:19], s54 offen offset:2048 lds\n"
  "buffer_load_dwordx4 v201, s[16:19], s54 offen offset:3072 lds\n"
  "s_waitcnt vmcnt(20)\n"
  "ds_read_b64_tr_b16 a[224:225], v228 offset:0\n"
  "ds_read_b64_tr_b16 a[226:227], v228 offset:2048\n"
  "ds_read_b64_tr_b16 a[232:233], v229 offset:0\n"
  "ds_read_b64_tr_b16 a[234:235], v229 offset:2048\n"
  "ds_read_b64_tr_b16 a[240:241], v230 offset:0\n"
  "ds_read_b64_tr_b16 a[242:243], v230 offset:2048\n"
  "ds_read_b64_tr_b16 a[248:249], v231 offset:0\n"
  "ds_read_b64_tr_b16 a[250:251], v231 offset:2048\n"
  "s_waitcnt vmcnt(16)\n"
  "ds_read_b64_tr_b16 a[228:229], v228 offset:16384\n"
  "ds_read_b64_tr_b16 a[230:231], v228 offset:18432\n"
  "ds_read_b64_tr_b16 a[236:237], v229 offset:16384\n"
  "ds_read_b64_tr_b16 a[238:239], v229 offset:18432\n"
  "ds_read_b64_tr_b16 a[244:245], v230 offset:16384\n"
  "ds_read_b64_tr_b16 a[246:247], v230 offset:18432\n"
  "ds_read_b64_tr_b16 a[252:253], v231 offset:16384\n"
  "ds_read_b64_tr_b16 a[254:255], v231 offset:18432\n"
  "s_add_u32 m0, s63, 131072\n"
  "s_add_u32 s53, s13, 327680\n"
  "s_waitcnt lgkmcnt(8)\n"
  "buffer_load_dwordx4 v198, s[16:19], s53 offen offset:0 lds\n"
  "buffer_load_dwordx4 v199, s[16:19], s53 offen offset:1024 lds\n"
  "buffer_load_dwordx4 v200, s[16:19], s53 offen offset:2048 lds\n"
  "buffer_load_dwordx4 v201, s[16:19], s53 offen offset:3072 lds\n"
  "s_add_u32 m0, s63, 147456\n"
  "s_add_u32 s54, s13, 360448\n"
  "s_waitcnt lgkmcnt(0)\n"
  "buffer_load_dwordx4 v198, s[16:19], s54 offen offset:0 lds\n"
  "buffer_load_dwordx4 v199, s[16:19], s54 offen offset:1024 lds\n"
  "buffer_load_dwordx4 v200, s[16:19], s54 offen offset:2048 lds\n"
  "buffer_load_dwordx4 v201, s[16:19], s54 offen offset:3072 lds\n"
  "s_waitcnt vmcnt(20)\n"
  "ds_read_b64_tr_b16 a[192:193], v194 offset:0\n"
  "ds_read_b64_tr_b16 a[194:195], v194 offset:2048\n"
  "ds_read_b64_tr_b16 a[200:201], v195 offset:0\n"
  "ds_read_b64_tr_b16 a[202:203], v195 offset:2048\n"
  "ds_read_b64_tr_b16 a[208:209], v196 offset:0\n"
  "ds_read_b64_tr_b16 a[210:211], v196 offset:2048\n"
  "ds_read_b64_tr_b16 a[216:217], v197 offset:0\n"
  "ds_read_b64_tr_b16 a[218:219], v197 offset:2048\n"
  "s_waitcnt vmcnt(16)\n"
  "ds_read_b64_tr_b16 a[196:197], v194 offset:16384\n"
  "ds_read_b64_tr_b16 a[198:199], v194 offset:18432\n"
  "ds_read_b64_tr_b16 a[204:205], v195 offset:16384\n"
  "ds_read_b64_tr_b16 a[206:207], v195 offset:18432\n"
  "ds_read_b64_tr_b16 a[212:213], v196 offset:16384\n"
  "ds_read_b64_tr_b16 a[214:215], v196 offset:18432\n"
  "ds_read_b64_tr_b16 a[220:221], v197 offset:16384\n"
  "ds_read_b64_tr_b16 a[222:223], v197 offset:18432\n"
  "s_add_u32 m0, s63, 0\n"
  "s_add_u32 s53, s13, 393216\n"
  "s_waitcnt lgkmcnt(8)\n"
  "buffer_load_dwordx4 v198, s[16:19], s53 offen offset:0 lds\n"
  "buffer_load_dwordx4 v199, s[16:19], s53 offen offset:1024 lds\n"
  "buffer_load_dwordx4 v200, s[16:19], s53 offen offset:2048 lds\n"
  "buffer_load_dwordx4 v201, s[16:19], s53 offen offset:3072 lds\n"
  "s_add_u32 m0, s63, 16384\n"
  "s_add_u32 s54, s13, 425984\n"
  "s_waitcnt lgkmcnt(0)\n"
  "buffer_load_dwordx4 v198, s[16:19], s54 offen offset:0 lds\n"
  "buffer_load_dwordx4 v199, s[16:19], s54 offen offset:1024 lds\n"
  "buffer_load_dwordx4 v200, s[16:19], s54 offen offset:2048 lds\n"
  "buffer_load_dwordx4 v201, s[16:19], s54 offen offset:3072 lds\n"
  "s_waitcnt vmcnt(20)\n"
  "ds_read_b64_tr_b16 a[0:1], v194 offset:32768\n"
  "ds_read_b64_tr_b16 a[2:3], v194 offset:34816\n"
  "ds_read_b64_tr_b16 a[8:9], v195 offset:32768\n"
  "ds_read_b64_tr_b16 a[10:11], v195 offset:34816\n"
  "ds_read_b64_tr_b16 a[16:17], v196 offset:32768\n"
  "ds_read_b64_tr_b16 a[18:19], v196 offset:34816\n"
  "ds_read_b64_tr_b16 a[24:25], v197 offset:32768\n"
  "ds_read_b64_tr_b16 a[26:27], v197 offset:34816\n"
  "s_waitcnt vmcnt(16)\n"
  "ds_read_b64_tr_b16 a[4:5], v194 offset:49152\n"
  "ds_read_b64_tr_b16 a[6:7], v194 offset:51200\n"
  "ds_read_b64_tr_b16 a[12:13], v195 offset:49152\n"
  "ds_read_b64_tr_b16 a[14:15], v195 offset:51200\n"
  "ds_read_b64_tr_b16 a[20:21], v196 offset:49152\n"
  "ds_read_b64_tr_b16 a[22:23], v196 offset:51200\n"
  "ds_read_b64_tr_b16 a[28:29], v197 offset:49152\n"
  "ds_read_b64_tr_b16 a[30:31], v197 offset:51200\n"
  "s_waitcnt vmcnt(0) lgkmcnt(0)\n"
  "s_nop 7\n"
  "s_branch L_join\n"
  ".p2align 12\n"
  :: "s"(__builtin_amdgcn_kernarg_segment_ptr()), "s"(blockIdx.x), "s"(wv), "s"((unsigned)(size_t)lds)
  : "v0","v1","v2","v3","v4","v5","v6","v7","v8","v9","v10","v11","v12","v13","v14","v15","v16","v17","v18","v19","v20","v21","v22","v23","v24","v25","v26","v27","v28","v29","v30","v31","v32","v33","v34","v35","v36","v37","v38","v39","v40","v41","v42","v43","v44","v45","v46","v47","v48","v49","v50","v51","v52","v53","v54","v55","v56","v57","v58","v59","v60","v61","v62","v63","v64","v65","v66","v67","v68","v69","v70","v71","v72","v73","v74","v75","v76","v77","v78","v79","v80","v81","v82","v83","v84","v85","v86","v87","v88","v89","v90","v91","v92","v93","v94","v95","v96","v97","v98","v99","v100","v101","v102","v103","v104","v105","v106","v107","v108","v109","v110","v111","v112","v113","v114","v115","v116","v117","v118","v119","v120","v121","v122","v123","v124","v125","v126","v127","v128","v129","v130","v131","v132","v133","v134","v135","v136","v137","v138","v139","v140","v141","v142","v143","v144","v145","v146","v147","v148","v149","v150","v151","v152","v153","v154","v155","v156","v157","v158","v159","v160","v161","v162","v163","v164","v165","v166","v167","v168","v169","v170","v171","v172","v173","v174","v175","v176","v177","v178","v179","v180","v181","v182","v183","v184","v185","v186","v187","v188","v189","v190","v191","v192","v193","v194","v195","v196","v197","v198","v199","v200","v201","v202","v203","v204","v205","v206","v207","v208","v209","v210","v211","v212","v213","v214","v215","v216","v217","v218","v219","v220","v221","v222","v223","v224","v225","v226","v227","v228","v229","v230","v231","v232","v233","v234","v235","v236","v237","v238","v239","v240","v241","v242","v243","v244","v245","v246","v247","v248","v249","v250","v251","v252","v253","v254","v255","a0","a1","a2","a3","a4","a5","a6","a7","a8","a9","a10","a11","a12","a13","a14","a15","a16","a17","a18","a19","a20","a21","a22","a23","a24","a25","a26","a27","a28","a29","a30","a31","a32","a33","a34","a35","a36","a37","a38","a39","a40","a41","a42","a43","a44","a45","a46","a47","a48","a49","a50","a51","a52","a53","a54","a55","a56","a57","a58","a59","a60","a61","a62","a63","a64","a65","a66","a67","a68","a69","a70","a71","a72","a73","a74","a75","a76","a77","a78","a79","a80","a81","a82","a83","a84","a85","a86","a87","a88","a89","a90","a91","a92","a93","a94","a95","a96","a97","a98","a99","a100","a101","a102","a103","a104","a105","a106","a107","a108","a109","a110","a111","a112","a113","a114","a115","a116","a117","a118","a119","a120","a121","a122","a123","a124","a125","a126","a127","a128","a129","a130","a131","a132","a133","a134","a135","a136","a137","a138","a139","a140","a141","a142","a143","a144","a145","a146","a147","a148","a149","a150","a151","a152","a153","a154","a155","a156","a157","a158","a159","a160","a161","a162","a163","a164","a165","a166","a167","a168","a169","a170","a171","a172","a173","a174","a175","a176","a177","a178","a179","a180","a181","a182","a183","a184","a185","a186","a187","a188","a189","a190","a191","a192","a193","a194","a195","a196","a197","a198","a199","a200","a201","a202","a203","a204","a205","a206","a207","a208","a209","a210","a211","a212","a213","a214","a215","a216","a217","a218","a219","a220","a221","a222","a223","a224","a225","a226","a227","a228","a229","a230","a231","a232","a233","a234","a235","a236","a237","a238","a239","a240","a241","a242","a243","a244","a245","a246","a247","a248","a249","a250","a251","a252","a253","a254","a255","s8","s9","s10","s11","s12","s13","s14","s15","s16","s17","s18","s19","s20","s21","s22","s23","s24","s25","s26","s27","s28","s29","s30","s31","s32","s33","s34","s35","s36","s37","s38","s39","s40","s41","s42","s43","s44","s45","s46","s47","s48","s49","s50","s51","s52","s53","s54","s55","s56","s57","s58","s59","s60","s61","s62","s63","s64","s65","s66","s67","s68","s69","s70","s71","s72","s73","s74","s75","s76","s77","s78","s79","s80","s81","s82","s83","s84","s85","s86","s87","s88","s89","s90","s91","s92","s93","s94","s95","s96","s97","s98","s99","s100","s101","vcc","scc","memory");
}
extern "C" void kernel_launch(void* const* d_in, const int* in_sizes, int n_in, void* d_out, int out_size, void* d_ws, size_t ws_size, hipStream_t stream) {
  Params p; p.x=d_in[0]; p.k=d_in[1]; p.v=d_in[2]; p.wq=d_in[3]; p.wo=d_in[4]; p.w1=d_in[6]; p.w2=d_in[7]; p.out=d_out; p.ws=d_ws;
  fused_main<<<256,256,0,stream>>>(p);
}
```
